```python
import torch

def _softmax(x, axis=-1):
    return torch.softmax(x, dim=axis)
def _gelu(x):
    return torch.nn.functional.gelu(x, approximate="tanh")
def _silu(x):
    return torch.nn.functional.silu(x)
def _sigmoid(x):
    return torch.sigmoid(x)
def _layernorm(x, axis=-1, eps=1e-5):
    m = x.mean(dim=axis, keepdim=True)
    v = x.var(dim=axis, keepdim=True, unbiased=False)
    return (x - m) / torch.sqrt(v + eps)
def _scatter_add(tgt, idx, src, axis=0):
    return tgt.clone().index_add_(axis, idx, src)
def _segment_sum(src, seg, n):
    out = torch.zeros((n,) + src.shape[1:], dtype=src.dtype)
    return out.index_add_(0, seg, src)
def _pad(x, pw):
    flat = tuple(v for pair in reversed(pw) for v in pair)
    return torch.nn.functional.pad(x, flat)

kernel_name = 'gated_mix__ssm_conv_s4d__glu_gated_geglu__prompt'

def setup_inputs():

    _w = (lambda a: (lambda t: t.to(torch.bfloat16) if t.is_floating_point() else t)(torch.as_tensor(a)))
    def _A_setup():
        Bsz, L, D, N = 1, 2048, 512, 128
        return {
            "u": _w((lambda *s: torch.randn(*s, dtype=torch.bfloat16) * 0.05)(Bsz, L, D)),
            "log_A_real": _w((lambda *s: torch.randn(*s, dtype=torch.bfloat16) * 6.4)(D, N) * 0.5),
            "A_imag": _w((lambda *s: torch.randn(*s, dtype=torch.bfloat16) * 0.05)(D, N) * 3.14),
            "C_real": _w((lambda *s: torch.randn(*s, dtype=torch.bfloat16) * 0.05)(D, N) / N ** 0.5),
            "C_imag": _w((lambda *s: torch.randn(*s, dtype=torch.bfloat16) * 0.05)(D, N) / N ** 0.5),
            "log_dt": _w((lambda *s: torch.randn(*s, dtype=torch.bfloat16) * 0.05)(D) * 0.5 - 4.0),
            "D_skip": _w((lambda *s: torch.randn(*s, dtype=torch.bfloat16) * 0.05)(D)),
        }
    _A_in = _A_setup()

    def _B_setup():
        B, S, Dm, Df = 1, 2048, 512, 1376
        return {
            "x": _w((lambda *s: torch.randn(*s, dtype=torch.bfloat16) * 0.05)(B, S, Dm)),
            "w_gate": _w((lambda *s: torch.randn(*s, dtype=torch.bfloat16) * 1.6)(Dm, Df) * (Dm ** -0.5)),
            "w_up": _w((lambda *s: torch.randn(*s, dtype=torch.bfloat16) * 1.6)(Dm, Df) * (Dm ** -0.5)),
            "w_down": _w((lambda *s: torch.randn(*s, dtype=torch.bfloat16) * 1.6)(Df, Dm) * (Df ** -0.5)),
        }
    _B_in = _B_setup()

    return {
        "A_u": _A_in["u"],
        "A_log_A_real": _A_in["log_A_real"],
        "A_A_imag": _A_in["A_imag"],
        "A_C_real": _A_in["C_real"],
        "A_C_imag": _A_in["C_imag"],
        "A_log_dt": _A_in["log_dt"],
        "A_D_skip": _A_in["D_skip"],
        "B_x": _B_in["x"],
        "B_w_gate": _B_in["w_gate"],
        "B_w_up": _B_in["w_up"],
        "B_w_down": _B_in["w_down"],
        "gate_w": _w((lambda *s: torch.randn(*s, dtype=torch.bfloat16) * 0.05)(512) * 16.0),
    }

def reference(A_u, A_log_A_real, A_A_imag, A_C_real, A_C_imag, A_log_dt, A_D_skip, B_x, B_w_gate, B_w_up, B_w_down, gate_w):
    def _A_ref(u, log_A_real, A_imag, C_real, C_imag, log_dt, D_skip):
        L = u.shape[1]
        dt = torch.exp(log_dt)
        A = -torch.exp(log_A_real) + 1j * A_imag
        C = (C_real + 1j * C_imag) * dt[:, None]
        pos = (lambda x, dt: x.to(dt))(torch.arange(L), log_dt.dtype)
        P = torch.exp((A * dt[:, None])[:, :, None] * pos[None, None, :])
        K = 2.0 * torch.einsum("dn,dnl->dl", C, P).real
        L2 = 2 * L
        u_t = (lambda x, *d: x.permute(*d))(u, 0, 2, 1)
        Kf = (lambda x, *a, **k: torch.fft.rfft(x.float(), *a, **k))(K, L2)
        Uf = (lambda x, *a, **k: torch.fft.rfft(x.float(), *a, **k))(u_t, L2)
        y = (lambda x, *a, **k: torch.fft.irfft(x, *a, **k).to(torch.bfloat16))(Uf * Kf[None, :, :], L2)[:, :, :L]
        y = (lambda x, *d: x.permute(*d))(y, 0, 2, 1)
        return y + u * D_skip[None, None, :]

    def _B_ref(x, w_gate, w_up, w_down):
        g = torch.einsum("bsd,df->bsf", x, w_gate)
        u = torch.einsum("bsd,df->bsf", x, w_up)
        h = _gelu(g) * u
        return torch.einsum("bsf,fd->bsd", h, w_down)

    x = A_u
    g = _sigmoid((x * gate_w).sum(-1, keepdims=True))
    a = _A_ref(x, A_log_A_real, A_A_imag, A_C_real, A_C_imag, A_log_dt, A_D_skip)
    b = _B_ref(x, B_w_gate, B_w_up, B_w_down)
    return g * a + (1 - g) * b
```

```cpp
#include <hip/hip_runtime.h>
#include <hip/hip_bf16.h>
#include <stdint.h>
#define ROUND_SEM 0
typedef unsigned short u16;
constexpr int L=2048, D=512, N=128, F=1376;
__device__ __forceinline__ float b2f(u16 x){ return __uint_as_float(((unsigned)x)<<16); }
__device__ __forceinline__ u16 f2b(float f){ unsigned u=__float_as_uint(f); u += 0x7fffu + ((u>>16)&1u); return (u16)(u>>16); }
__device__ __forceinline__ float rb(float f){
  return f;
}
__global__ void k_kgen(const u16* logA, const u16* Aim, const u16* Cr, const u16* Ci, const u16* logdt, float* K){
  int d = blockIdx.y; int l = blockIdx.x*256+threadIdx.x;
  __shared__ float ar[N], ai[N], cr[N], ci[N];
  float dt = rb(expf(b2f(logdt[d])));
  if(threadIdx.x<N){ int n=threadIdx.x;
    ar[n] = -rb(expf(b2f(logA[d*N+n])))*dt; ai[n]=b2f(Aim[d*N+n])*dt; cr[n]=b2f(Cr[d*N+n])*dt; ci[n]=b2f(Ci[d*N+n])*dt; }
  __syncthreads();
  float pos = rb((float)l);
  float s=0;
  for(int n=0;n<N;n++){ float e=expf(ar[n]*pos); float th=ai[n]*pos;
    float rev = th*0.15915494309189535f; rev -= floorf(rev);
    float sn=__builtin_amdgcn_sinf(rev), cs=__builtin_amdgcn_cosf(rev);
    s += e*(cr[n]*cs - ci[n]*sn); }
  K[d*L+l]=2.f*s;
}
__global__ void k_conv(const u16* u, const float* K, float* y){
  int d = blockIdx.x;
  __shared__ float su[L], sk[L];
  for(int i=threadIdx.x;i<L;i+=256){ su[i]=b2f(u[i*D+d]); sk[i]=K[d*L+i]; }
  __syncthreads();
  for(int l=threadIdx.x;l<L;l+=256){ float s=0; for(int j=0;j<=l;j++) s+=sk[j]*su[l-j]; y[l*D+d]=s; }
}
__global__ void k_g1(const u16* x, const u16* wg, const u16* wu, u16* h){
  __shared__ float sx[16][D];
  int r0=blockIdx.y*16; int f=blockIdx.x*64+(threadIdx.x&63); int rq=threadIdx.x>>6;
  for(int i=threadIdx.x;i<16*D;i+=256) sx[i/D][i%D]=b2f(x[(r0+i/D)*D+i%D]);
  __syncthreads();
  if(f>=F) return;
  float g[4]={0,0,0,0}, uu[4]={0,0,0,0};
  for(int k=0;k<D;k++){ float a=b2f(wg[k*F+f]), b=b2f(wu[k*F+f]);
    for(int j=0;j<4;j++){ g[j]+=sx[rq*4+j][k]*a; uu[j]+=sx[rq*4+j][k]*b; } }
  for(int j=0;j<4;j++){ float gg=rb(g[j]), up=rb(uu[j]);
    float z=0.7978845608028654f*(gg+0.044715f*gg*gg*gg); float ge=rb(0.5f*gg*(1.f+tanhf(z)));
    h[(r0+rq*4+j)*F+f]=f2b(ge*up); }
}
__global__ void k_g2(const u16* h, const u16* wd, const u16* x, const u16* gw, const u16* dskip, const float* y, u16* out){
  __shared__ float sh[8][F]; __shared__ float sg[8];
  int r0=blockIdx.x*8;
  for(int i=threadIdx.x;i<8*F;i+=512) sh[i/F][i%F]=b2f(h[(r0+i/F)*F+i%F]);
  { int w=threadIdx.x>>6, lane=threadIdx.x&63; float s=0; for(int k=lane;k<D;k+=64) s+=rb(b2f(x[(r0+w)*D+k])*b2f(gw[k]));
    for(int o=32;o;o>>=1) s+=__shfl_xor(s,o); if(lane==0) sg[w]=rb(1.f/(1.f+expf(-rb(s)))); }
  __syncthreads();
  int n=threadIdx.x; float acc[8]={0,0,0,0,0,0,0,0};
  for(int k=0;k<F;k++){ float w=b2f(wd[k*D+n]); for(int j=0;j<8;j++) acc[j]+=sh[j][k]*w; }
  for(int j=0;j<8;j++){ int r=r0+j; float g=sg[j];
    float a=rb(rb(y[r*D+n])+rb(b2f(x[r*D+n])*b2f(dskip[n])));
    out[r*D+n]=f2b(rb(g*a)+rb(rb(1.f-g)*rb(acc[j]))); }
}
extern "C" void kernel_launch(void* const* d_in, const int* in_sizes, int n_in, void* d_out, int out_size, void* d_ws, size_t ws_size, hipStream_t stream){
  const u16* u=(const u16*)d_in[0];
  float* K=(float*)d_ws; float* y=K+D*L; u16* h=(u16*)(y+D*L);
  k_kgen<<<dim3(L/256,D),256,0,stream>>>((const u16*)d_in[1],(const u16*)d_in[2],(const u16*)d_in[3],(const u16*)d_in[4],(const u16*)d_in[5],K);
  k_conv<<<D,256,0,stream>>>(u,K,y);
  k_g1<<<dim3((F+63)/64,L/16),256,0,stream>>>(u,(const u16*)d_in[8],(const u16*)d_in[9],h);
  k_g2<<<L/8,512,0,stream>>>(h,(const u16*)d_in[10],u,(const u16*)d_in[11],(const u16*)d_in[6],y,(u16*)d_out);
}
```

```cpp
#include <hip/hip_runtime.h>
#include <hip/hip_bf16.h>
#include <stdint.h>
#define ROUND_SEM 1
typedef unsigned short u16;
constexpr int L=2048, D=512, N=128, F=1376;
__device__ __forceinline__ float b2f(u16 x){ return __uint_as_float(((unsigned)x)<<16); }
__device__ __forceinline__ u16 f2b(float f){ unsigned u=__float_as_uint(f); u += 0x7fffu + ((u>>16)&1u); return (u16)(u>>16); }
__device__ __forceinline__ float rb(float f){
  return b2f(f2b(f));
}
__global__ void k_kgen(const u16* logA, const u16* Aim, const u16* Cr, const u16* Ci, const u16* logdt, float* K){
  int d = blockIdx.y; int l = blockIdx.x*256+threadIdx.x;
  __shared__ float ar[N], ai[N], cr[N], ci[N];
  float dt = rb(expf(b2f(logdt[d])));
  if(threadIdx.x<N){ int n=threadIdx.x;
    ar[n] = -rb(expf(b2f(logA[d*N+n])))*dt; ai[n]=b2f(Aim[d*N+n])*dt; cr[n]=b2f(Cr[d*N+n])*dt; ci[n]=b2f(Ci[d*N+n])*dt; }
  __syncthreads();
  float pos = rb((float)l);
  float s=0;
  for(int n=0;n<N;n++){ float e=expf(ar[n]*pos); float th=ai[n]*pos;
    float rev = th*0.15915494309189535f; rev -= floorf(rev);
    float sn=__builtin_amdgcn_sinf(rev), cs=__builtin_amdgcn_cosf(rev);
    s += e*(cr[n]*cs - ci[n]*sn); }
  K[d*L+l]=2.f*s;
}
__global__ void k_conv(const u16* u, const float* K, float* y){
  int d = blockIdx.x;
  __shared__ float su[L], sk[L];
  for(int i=threadIdx.x;i<L;i+=256){ su[i]=b2f(u[i*D+d]); sk[i]=K[d*L+i]; }
  __syncthreads();
  for(int l=threadIdx.x;l<L;l+=256){ float s=0; for(int j=0;j<=l;j++) s+=sk[j]*su[l-j]; y[l*D+d]=s; }
}
__global__ void k_g1(const u16* x, const u16* wg, const u16* wu, u16* h){
  __shared__ float sx[16][D];
  int r0=blockIdx.y*16; int f=blockIdx.x*64+(threadIdx.x&63); int rq=threadIdx.x>>6;
  for(int i=threadIdx.x;i<16*D;i+=256) sx[i/D][i%D]=b2f(x[(r0+i/D)*D+i%D]);
  __syncthreads();
  if(f>=F) return;
  float g[4]={0,0,0,0}, uu[4]={0,0,0,0};
  for(int k=0;k<D;k++){ float a=b2f(wg[k*F+f]), b=b2f(wu[k*F+f]);
    for(int j=0;j<4;j++){ g[j]+=sx[rq*4+j][k]*a; uu[j]+=sx[rq*4+j][k]*b; } }
  for(int j=0;j<4;j++){ float gg=rb(g[j]), up=rb(uu[j]);
    float z=0.7978845608028654f*(gg+0.044715f*gg*gg*gg); float ge=rb(0.5f*gg*(1.f+tanhf(z)));
    h[(r0+rq*4+j)*F+f]=f2b(ge*up); }
}
__global__ void k_g2(const u16* h, const u16* wd, const u16* x, const u16* gw, const u16* dskip, const float* y, u16* out){
  __shared__ float sh[8][F]; __shared__ float sg[8];
  int r0=blockIdx.x*8;
  for(int i=threadIdx.x;i<8*F;i+=512) sh[i/F][i%F]=b2f(h[(r0+i/F)*F+i%F]);
  { int w=threadIdx.x>>6, lane=threadIdx.x&63; float s=0; for(int k=lane;k<D;k+=64) s+=rb(b2f(x[(r0+w)*D+k])*b2f(gw[k]));
    for(int o=32;o;o>>=1) s+=__shfl_xor(s,o); if(lane==0) sg[w]=rb(1.f/(1.f+expf(-rb(s)))); }
  __syncthreads();
  int n=threadIdx.x; float acc[8]={0,0,0,0,0,0,0,0};
  for(int k=0;k<F;k++){ float w=b2f(wd[k*D+n]); for(int j=0;j<8;j++) acc[j]+=sh[j][k]*w; }
  for(int j=0;j<8;j++){ int r=r0+j; float g=sg[j];
    float a=rb(rb(y[r*D+n])+rb(b2f(x[r*D+n])*b2f(dskip[n])));
    out[r*D+n]=f2b(rb(g*a)+rb(rb(1.f-g)*rb(acc[j]))); }
}
#define PROBE 2
typedef unsigned long long u64;
#define AS1 __attribute__((address_space(1)))
#define AS3 __attribute__((address_space(3)))
__device__ __forceinline__ void glds16(const char* g, AS3 char* lds){
  __builtin_amdgcn_global_load_lds((const AS1 unsigned*)g, (AS3 unsigned*)lds, 16, 0, 0);
}
template<int N> __device__ __forceinline__ void waitv(){ asm volatile("s_waitcnt vmcnt(%0)"::"n"(N):"memory"); }
__global__ __launch_bounds__(256) void k_probe(const char* x, const char* wg, const char* wu, u64* st){
  __shared__ __attribute__((aligned(16))) char smem[122880];
  u64 t0=__builtin_amdgcn_s_memrealtime();
  int b=blockIdx.x, xcd=b&7, r=b>>3;
  int w=__builtin_amdgcn_readfirstlane(threadIdx.x>>6), lane=threadIdx.x&63;
  AS3 char* lb=(AS3 char*)smem;
  constexpr int NW=4, PX=16/NW, PW=24/NW, P=PX+PW;
  int rp=(xcd&1)*8+(r&7); int ct=(xcd>>1)*4+(r>>3);
  int k0=0;
  if(ct>14) ct=14;
  int c0f=ct*96; if(c0f+96>F) c0f=F-96;
  const char* px[PX]; const char* pw[PW];
#pragma unroll
  for(int i=0;i<PX;i++){ int p=w*PX+i; int row=rp*128+p*8+(lane>>3); px[i]=x+(size_t)row*1024+(lane&7)*16; }
#pragma unroll
  for(int i=0;i<PW;i++){ int p=w*PW+i; int q=p*64+lane; int row=q/24, ch=q%24; const char* base=ch<12?wg:wu; int c=ch<12?ch:ch-12;
      pw[i]=base+(size_t)row*(F*2)+c0f*2+c*16; }
#pragma unroll 1
  for(int it=0;it<8;it++){
    int kt=(it+k0)&7;
    AS3 char* dst=lb+(it%3)*40960+w*P*1024;
#pragma unroll
    for(int i=0;i<PX;i++){ glds16(px[i]+kt*128,dst+i*1024); }
#pragma unroll
    for(int i=0;i<PW;i++){ glds16(pw[i]+kt*(64*F*2),dst+(PX+i)*1024); }
    waitv<P*2>();
  }
  waitv<0>();
  __syncthreads();
  u64 t1=__builtin_amdgcn_s_memrealtime();
  if(threadIdx.x==0){ st[b*2]=t0; st[b*2+1]=t1; }
}
__global__ void k_spin(const u64* st){
  int lane=threadIdx.x; u64 mn=~0ull,mx=0,mxs=0; u64 sum=0;
  for(int b=0;b<256;b++){ u64 a=st[b*2], e=st[b*2+1]; mn=a<mn?a:mn; mx=e>mx?e:mx; mxs=a>mxs?a:mxs; sum+=e-a; }
  u64 m=(mxs-mn)*100;
  if(m>300000) m=300000;
  u64 t=__builtin_amdgcn_s_memrealtime();
  while(__builtin_amdgcn_s_memrealtime()-t<m) __builtin_amdgcn_s_sleep(4);
}
extern "C" void kernel_launch(void* const* d_in, const int* in_sizes, int n_in, void* d_out, int out_size, void* d_ws, size_t ws_size, hipStream_t stream){
  const u16* u=(const u16*)d_in[0];
  float* K=(float*)d_ws; float* y=K+D*L; u16* h=(u16*)(y+D*L);
  u64* st=(u64*)((char*)d_ws+(64<<20));
  k_probe<<<256,256,0,stream>>>((const char*)d_in[0],(const char*)d_in[8],(const char*)d_in[9],st);
  k_spin<<<1,64,0,stream>>>(st);
  k_kgen<<<dim3(L/256,D),256,0,stream>>>((const u16*)d_in[1],(const u16*)d_in[2],(const u16*)d_in[3],(const u16*)d_in[4],(const u16*)d_in[5],K);
  k_conv<<<D,256,0,stream>>>(u,K,y);
  k_g1<<<dim3((F+63)/64,L/16),256,0,stream>>>(u,(const u16*)d_in[8],(const u16*)d_in[9],h);
  k_g2<<<L/8,512,0,stream>>>(h,(const u16*)d_in[10],u,(const u16*)d_in[11],(const u16*)d_in[6],y,(u16*)d_out);
}
```

```cpp
#include <hip/hip_runtime.h>
#include <stdint.h>

typedef unsigned short u16;
typedef unsigned long long u64;
typedef unsigned v4u __attribute__((ext_vector_type(4)));
typedef unsigned v2u __attribute__((ext_vector_type(2)));
typedef short s16x4 __attribute__((ext_vector_type(4)));
typedef short s16x8 __attribute__((ext_vector_type(8)));
typedef __bf16 bf16x8 __attribute__((ext_vector_type(8)));
typedef __bf16 bf16x2 __attribute__((ext_vector_type(2)));
typedef float f32x2 __attribute__((ext_vector_type(2)));
typedef float f32x4 __attribute__((ext_vector_type(4)));
typedef float f32x16 __attribute__((ext_vector_type(16)));
typedef __amdgpu_buffer_rsrc_t rsrc_t;
#define AS1 __attribute__((address_space(1)))
#define AS3 __attribute__((address_space(3)))
#define DI __device__ __forceinline__

constexpr int L = 2048, D = 512, F = 1376, NU = 86;
constexpr int STG = 41024, GO = 16384, UO = 16384 + 12288 + 32;
constexpr int R3 = 3 * STG;
constexpr int SCR = 288, SCW = 57 * SCR, KS = 2 * STG, KD = KS + 4 * SCW + 7 * SCR;
constexpr int KXS = 4160, KXC = 4 * KXS;
constexpr int KX = R3, KT = R3 + 2 * KXC, TBL = 1312;
constexpr int UB = R3, UCH = 4608, W0 = R3 + 2 * UCH;
constexpr int CTL = 163840 - 64;
static_assert(KD >= R3 && KD + 4 * 3072 <= CTL && KT + 2 * TBL <= CTL && W0 + 7 * 4096 <= CTL, "lds map");
constexpr int FLG = 4096, GV = FLG + 256 * 128;
constexpr int UT = 65536, AT = UT + L * D * 2, HB = AT + 2 * L * D * 2, WS_END = HB + L * F * 2;
constexpr int SPIN = 1 << 16;
constexpr int CODE_LINES = 224;

template <int N> DI void waitv() { asm volatile("s_waitcnt vmcnt(%0)" ::"n"(N) : "memory"); }
DI void waitl() { asm volatile("s_waitcnt lgkmcnt(0)" ::: "memory"); }
DI void bar() { asm volatile("s_barrier" ::: "memory"); }
DI void fence() { __builtin_amdgcn_sched_barrier(0); }
DI rsrc_t mk(const void* p, int bytes) { return __builtin_amdgcn_make_buffer_rsrc((void*)p, 0, bytes, 0x00020000); }
DI unsigned pk(float a, float b) { f32x2 v = {a, b}; return __builtin_bit_cast(unsigned, __builtin_convertvector(v, bf16x2)); }
DI float lo16(unsigned v) { return __uint_as_float(v << 16); }
DI float hi16(unsigned v) { return __uint_as_float(v & 0xffff0000u); }
DI float rbf(float a) { return lo16(pk(a, a)); }
DI v2u trd(unsigned a) { return __builtin_bit_cast(v2u, __builtin_amdgcn_ds_read_tr16_b64_v4i16((AS3 s16x4*)a)); }
DI v4u trd2(unsigned a, unsigned b) { v2u x = trd(a), y = trd(b); return v4u{x.x, x.y, y.x, y.y}; }
DI v4u ld128(unsigned a) { return *(AS3 v4u*)a; }
DI v2u ld64(unsigned a) { return *(AS3 v2u*)a; }
DI void st128(unsigned a, v4u v) { *(AS3 v4u*)a = v; }
DI f32x16 mma32(v4u a, v4u b, f32x16 c) {
  return __builtin_amdgcn_mfma_f32_32x32x16_bf16(__builtin_bit_cast(bf16x8, a), __builtin_bit_cast(bf16x8, b), c, 0, 0, 0);
}
DI f32x4 mma16(v4u a, v4u b, f32x4 c) {
  return __builtin_amdgcn_mfma_f32_16x16x32_bf16(__builtin_bit_cast(bf16x8, a), __builtin_bit_cast(bf16x8, b), c, 0, 0, 0);
}
DI void dma1(unsigned lds, int v0, rsrc_t rs, int so) {
  asm volatile("s_mov_b32 m0, %0\n\ts_nop 0\n\tbuffer_load_dwordx4 %1, %2, %3 offen lds" ::"s"(lds), "v"(v0), "s"(rs), "s"(so) : "memory");
}
DI void dma3(unsigned lds, const int* v, rsrc_t rs, int so) {
  asm volatile("s_mov_b32 m0, %0\n\ts_nop 0\n\tbuffer_load_dwordx4 %1, %4, %5 offen lds\n\t"
               "buffer_load_dwordx4 %2, %4, %5 offen offset:1024 lds\n\tbuffer_load_dwordx4 %3, %4, %5 offen offset:2048 lds"
               ::"s"(lds), "v"(v[0]), "v"(v[1]), "v"(v[2]), "s"(rs), "s"(so) : "memory");
}
DI void dma4(unsigned lds, const int* v, rsrc_t rs, int so) {
  asm volatile("s_mov_b32 m0, %0\n\ts_nop 0\n\tbuffer_load_dwordx4 %1, %5, %6 offen lds\n\t"
               "buffer_load_dwordx4 %2, %5, %6 offen offset:1024 lds\n\tbuffer_load_dwordx4 %3, %5, %6 offen offset:2048 lds\n\t"
               "buffer_load_dwordx4 %4, %5, %6 offen offset:3072 lds"
               ::"s"(lds), "v"(v[0]), "v"(v[1]), "v"(v[2]), "v"(v[3]), "s"(rs), "s"(so) : "memory");
}
DI void stws(v4u v, rsrc_t rs, int vo, int so) {
  __builtin_amdgcn_raw_buffer_store_b128(v, rs, vo, so, 16);
  asm volatile("s_nop 1" ::"v"(v) : "memory");
}
DI void set_flag(char* ws, int off, u64 ep) { __hip_atomic_store((AS1 u64*)(ws + off), ep, __ATOMIC_RELAXED, __HIP_MEMORY_SCOPE_AGENT); }
DI void poll(char* ws, int off, u64 ep) {
  AS1 u64* p = (AS1 u64*)(ws + off);
#pragma unroll 1
  for (int i = 0; i < SPIN; ++i) {
    u64 f = __hip_atomic_load(p, __ATOMIC_RELAXED, __HIP_MEMORY_SCOPE_AGENT);
    if (__all(f == ep)) break;
    __builtin_amdgcn_s_sleep(1);
  }
  __builtin_amdgcn_fence(__ATOMIC_ACQUIRE, "wavefront");
}
DI void lds_wait(unsigned a, unsigned want) {
#pragma unroll 1
  for (int i = 0; i < SPIN * 8; ++i) {
    unsigned f = __hip_atomic_load((AS3 unsigned*)a, __ATOMIC_RELAXED, __HIP_MEMORY_SCOPE_WORKGROUP);
    if (__builtin_amdgcn_readfirstlane(f) >= want) break;
    __builtin_amdgcn_s_sleep(1);
  }
  asm volatile("" ::: "memory");
}
DI void lds_inc(unsigned a, int lane) {
  if (lane == 0) __hip_atomic_fetch_add((AS3 unsigned*)a, 1u, __ATOMIC_RELAXED, __HIP_MEMORY_SCOPE_WORKGROUP);
}
DI float exp_acc(float x) {
  const float H = 1.44269502162933349609375f, Lo = 1.925963033500011079e-8f;
  float t = x * H, e = __builtin_fmaf(x, Lo, __builtin_fmaf(x, H, -t));
  float r = __builtin_amdgcn_exp2f(t);
  return __builtin_fmaf(r, e * 0.693147182464599609375f, r);
}
struct CW { f32x2 w, n; };
DI CW mkw(f32x2 w) { return CW{w, f32x2{-w.y, w.x}}; }
DI f32x2 cmul(f32x2 x, const CW& y) { return __builtin_elementwise_fma(x.yy, y.n, x.xx * y.w); }
constexpr int wslot_lds(int s) { return s < 7 ? W0 + 4096 * s : s < 17 ? 2 * STG + 4096 * (s - 7) : s < 27 ? 4096 * (s - 17) : s < 37 ? STG + 4096 * (s - 27) : W0 + 4096 * (s - 37); }
constexpr int wslot_q(int s) { return s < 15 ? 3 * s : s < 29 ? 3 * (s - 15) + 1 : 3 * (s - 29) + 2; }
DI unsigned nm(unsigned v) { return min(v, 0xfffffffeu); }
DI bool unset(v4u x) { return __any(max(max(x.x, x.y), max(x.z, x.w)) == 0xffffffffu); }
DI v4u settle(v4u x, rsrc_t rs, int vo, int so) {
  if (__builtin_expect(unset(x), 0)) {
    int i = 0;
#pragma unroll 1
    do { asm volatile("" ::: "memory"); x = __builtin_amdgcn_raw_buffer_load_b128(rs, vo, so, 16); } while (unset(x) && ++i < SPIN);
  }
  return x;
}

#define ST(i)
#define STR(i)
#define STW()
#define BAR(i) bar()

__global__ __launch_bounds__(512, 2) void gated_mix_fused(
    const u16* __restrict__ x, const u16* __restrict__ wg, const u16* __restrict__ wu, const u16* __restrict__ gw,
    const u16* __restrict__ wd, char* ws, const u16* __restrict__ logA, const u16* __restrict__ Aim,
    const u16* __restrict__ Cr, const u16* __restrict__ Ci, const u16* __restrict__ logdt, const u16* __restrict__ dskip,
    u16* __restrict__ out) {
  __shared__ __attribute__((aligned(16))) char smem[163840];
  const unsigned lb = (unsigned)(size_t)(AS3 char*)smem;
  u64 pc0; asm volatile("s_getpc_b64 %0" : "=s"(pc0));
  const int b = blockIdx.x, xcd = b & 7, r = b >> 3;
  const int w = __builtin_amdgcn_readfirstlane(threadIdx.x >> 6), lane = threadIdx.x & 63;
  const int rp = (xcd & 1) * 8 + (r & 7), ct = (xcd >> 1) * 4 + (r >> 3);
  const int k0 = ((r & 7) + 2 * (r >> 3) + (xcd >> 2)) & 7;
  const int rt = xcd * 4 + (r & 3), c2 = r >> 2;
  int vx[4], vg[3], vu[3];
#pragma unroll
  for (int p = 0; p < 4; ++p) vx[p] = (rp * 128 + (lane >> 3)) * 1024 + (((lane & 7) ^ ((lane >> 4) + 4 * (p & 1))) * 16) + p * 7168;
#pragma unroll
  for (int i = 0; i < 3; ++i) {
    const int q = i * 64 + lane, row = (q * 171) >> 11, c = q - row * 12;
    const int o = row * (F * 2) + c * 16 + ct * 192 - i * 1024, og = 2048 - i * 1024 + (row & ~1) * 2;
    vg[i] = ct < 15 ? o : (c | (row & 1)) ? 0x40000000 : og;
    vu[i] = ct < 15 ? o : (c | (~row & 1)) ? 0x40000000 : og;
  }
  asm volatile("" : "+v"(vx[0]), "+v"(vx[1]), "+v"(vx[2]), "+v"(vx[3]), "+v"(vg[0]), "+v"(vg[1]), "+v"(vg[2]), "+v"(vu[0]), "+v"(vu[1]), "+v"(vu[2]));
  asm volatile("" : "+s"(x), "+s"(wg), "+s"(wu), "+s"(gw), "+s"(wd), "+s"(ws), "+s"(logA), "+s"(Aim), "+s"(Cr), "+s"(Ci), "+s"(logdt), "+s"(dskip), "+s"(out));
  const rsrc_t rws = mk(ws, WS_END), rx = mk(x, L * D * 2), rd = mk(wd, F * D * 2);
  const rsrc_t rg = ct < 15 ? mk(wg, D * F * 2) : mk((const char*)gw - 2048, 2048 + D * 2);
  const rsrc_t ru = ct < 15 ? mk(wu, D * F * 2) : mk((const char*)gw - 2048, 2048 + D * 2);
  const int wrow = ct < 15 ? F * 2 : 2;
  auto part1 = [&](int t) {
    const int kt = (t + k0) & 7; const unsigned s = lb + (t % 3) * STG;
#pragma unroll
    for (int g = 0; g < 4; ++g) dma4(s + g * 4096, vx, rx, kt * 128 + g * 32768);
#pragma unroll
    for (int g = 0; g < 2; ++g) { const int so = (kt * 64 + g * 16) * wrow; dma3(s + GO + g * 3072, vg, rg, so); dma3(s + UO + g * 3072, vu, ru, so); }
  };
  auto part2 = [&](int t) {
    const int kt = (t + k0) & 7; const unsigned s = lb + (t % 3) * STG;
#pragma unroll
    for (int g = 2; g < 4; ++g) { const int so = (kt * 64 + g * 16) * wrow; dma3(s + GO + g * 3072, vg, rg, so); dma3(s + UO + g * 3072, vu, ru, so); }
  };
  unsigned pa = 0, pi = 0, pr = 0, pc = 0, pt = 0;
  if (w < 4) {
    const int i = b * 256 + w * 64 + lane;
    pa = ((const AS1 u16*)logA)[i]; pi = ((const AS1 u16*)Aim)[i]; pr = ((const AS1 u16*)Cr)[i]; pc = ((const AS1 u16*)Ci)[i];
    int d = 2 * b + (w >> 1); asm volatile("" : "+v"(d));
    pt = ((const AS1 u16*)logdt)[d];
  } else if (w == 5) {
    pa = __builtin_amdgcn_raw_buffer_load_b32(mk((const void*)(pc0 & ~255ull), CODE_LINES * 128), lane < CODE_LINES / 32 ? (lane * 32 + r) * 128 : 0x40000000, 0, 0);
  } else if (w == 4) {
    part1(0); part2(0);
  }
  fence();
  ST(40);

  if (w >= 4) {
    const int aw = w - 4;
    const int vd = (lane >> 3) * 1024 + (((lane & 7) ^ (((lane >> 4) & 1) * 4)) * 16) + c2 * 128;
    const int ch = aw >> 1, rh = aw & 1;
    v4u af[4][8];
    const unsigned a0 = lb + KX + ch * KXC + (lane & 3) * KXS - ((lane >> 2) & 3) * 8 + (lane >> 4) * 16 - rh * 256 + 280;
    auto afrags = [&](int p0) {
      unsigned a = a0; asm volatile("" : "+v"(a));
#pragma unroll
      for (int p = p0; p < p0 + 2; ++p)
#pragma unroll
        for (int e = 0; e < 8; ++e) {
          const int o = 3776 - 512 * e - 64 * p;
          const v2u u0 = *(volatile AS3 v2u*)(a + o), u1 = *(volatile AS3 v2u*)(a + o + 8);
          af[p][e] = v4u{u0.x, u0.y, u1.x, u1.y};
        }
      waitl();
    };
    u64 ep = 0;
    if (aw == 0) {
#pragma unroll 1
      for (int t = 0; t < 5; ++t) { part1(t + 1); waitv<28>(); BAR(t); part2(t + 1); }
      part1(6); afrags(0); waitv<28>(); BAR(5); part2(6);
      part1(7); afrags(2); waitv<28>(); BAR(6); part2(7);
      int v4[4];
#pragma unroll
      for (int p = 0; p < 4; ++p) v4[p] = vd + p * 7168;
#pragma unroll
      for (int s = 0; s < 7; ++s) dma4(lb + wslot_lds(s), v4, rd, wslot_q(s) * 32768);
      waitv<28>(); BAR(7);
#pragma unroll
      for (int s = 7; s < 13; ++s) dma4(lb + wslot_lds(s), v4, rd, wslot_q(s) * 32768);
      BAR(8);
      waitv<24>(); lds_inc(lb + CTL, lane);
    } else if (aw == 1) {
      ep = *(const __attribute__((address_space(4))) u64*)ws + 1;
      if (lane < 16) *(AS3 unsigned*)(lb + CTL + lane * 4) = 0;
      const v4u ones = {~0u, ~0u, ~0u, ~0u};
      if (ct < 15) {
        const int nu = ct < 14 ? 6 : 2;
#pragma unroll 1
        for (int t = 0; t < 4; ++t)
#pragma unroll 1
          for (int j = 0; j < nu; ++j) stws(ones, rws, lane * 16, HB + ((rp * 4 + t) * NU + ct * 6 + j) * 1024);
      } else {
        if (lane < 32) stws(ones, rws, lane * 16, GV + rp * 512);
#pragma unroll
        for (int i = 0; i < 11; ++i) pa ^= __builtin_amdgcn_raw_buffer_load_b32(rd, lane * 128 + i * 8192, rp * 88064, 0);
      }
#pragma unroll 4
      for (int i = 0; i < 16; ++i) stws(ones, rws, lane * 16, AT + b * 16384 + i * 1024);
      waitl(); BAR(0);
      const int half = (xcd >> 1) & 1, G = lane >> 4, q = (lane >> 2) & 3, p = lane & 3;
      v4u tv[4][2];
#pragma unroll
      for (int j = 0; j < 4; ++j)
#pragma unroll
        for (int s = 0; s < 2; ++s) {
          v2u h2[2];
#pragma unroll
          for (int t = 0; t < 2; ++t) {
            const int row = 64 * (G & 1) + 16 * p + 8 * s + 4 * t + q;
            h2[t] = trd(lb + row * 128 + (((4 * half + j) ^ ((row >> 1) & 7)) << 4) + 8 * (G >> 1));
          }
          tv[j][s] = v4u{h2[0].x, h2[0].y, h2[1].x, h2[1].y};
        }
      waitl(); BAR(1);
      const int vo = (32 * k0 + 16 * half + 2 * (G >> 1)) * 8192 + rp * 512 + (lane & 31) * 16;
      const bool lo = (lane & 2) == 0;
#pragma unroll
      for (int j = 0; j < 4; ++j) {
        v4u n0, n1;
#pragma unroll
        for (int e = 0; e < 4; ++e) {
          const unsigned a = tv[j][0][e], c = tv[j][1][e];
          const unsigned as = __builtin_amdgcn_update_dpp(0u, a, 0x4E, 0xf, 0xf, true), cs = __builtin_amdgcn_update_dpp(0u, c, 0x4E, 0xf, 0xf, true);
          n0[e] = lo ? a : cs; n1[e] = lo ? as : c;
        }
        stws(n0, rws, vo, UT + j * 32768);
        stws(n1, rws, vo, UT + j * 32768 + 8192);
      }
      BAR(2); BAR(3);
      waitv<0>();
      asm volatile("" ::"v"(pa));
      if (lane == 0) set_flag(ws, FLG + b * 128, ep);
      BAR(4); afrags(0); BAR(5); afrags(2);
      { const int cg = b >> 4, i = lane & 15;
        const int prp = lane < 16 ? i : rt >> 1, dk = ((cg >> 1) - prp) & 7;
        const int pct = lane < 16 ? 4 * (2 * (dk & 1) + (cg & 1)) + (dk >> 1) : i;
        const int pb = lane < 32 ? (prp >> 3) + 2 * (pct >> 2) + 8 * ((prp & 7) + 8 * (pct & 3)) : 32 * c2 + lane - 32;
        poll(ws, FLG + pb * 128, ep);
      }
      BAR(6);
      v4u uv[8];
#pragma unroll
      for (int i = 0; i < 8; ++i) uv[i] = __builtin_amdgcn_raw_buffer_load_b128(rws, lane * 16, UT + b * 8192 + i * 1024, 16);
      fence(); BAR(7); fence();
      st128(lb + UB + (lane >> 5) * UCH + (lane & 31) * 16, v4u{0u, 0u, 0u, 0u});
#pragma unroll
      for (int i = 0; i < 8; ++i) st128(lb + UB + (lane & 1) * UCH + 512 + (lane >> 1) * 16 + i * 512, uv[i]);
      waitl(); BAR(8);
    } else {
      const int kw = aw - 2;
      int d = 2 * b + kw; asm volatile("" : "+v"(d));
      const unsigned pd = ((const AS1 u16*)dskip)[d];
      const unsigned tb0 = lb + KT + kw * TBL;
      unsigned ix[4][12];
#pragma unroll
      for (int it = 0; it < 4; ++it) {
        float top = (float)(2047 - 512 * it - 8 * lane); asm volatile("" : "+v"(top));
#pragma unroll
        for (int i = 0; i < 12; i += 2) {
          const float l0 = top - i, l1 = top - (i + 1);
          const unsigned p = it == 3 ? pk(__builtin_fmaxf(l0, 128.f), __builtin_fmaxf(l1, 128.f)) : pk(l0, l1);
          unsigned i0 = (p & 0xffffu) * 2 + (tb0 - 2 * 0x427c), i1 = (p >> 16) * 2 + (tb0 - 2 * 0x427c);
          if (it == 3) { i0 = min(i0, tb0 + 2 * (515 - i) - 16 * lane); i1 = min(i1, tb0 + 2 * (514 - i) - 16 * lane); }
          ix[it][i] = i0; ix[it][i + 1] = i1;
          asm volatile("" : "+v"(ix[it][i])); if (i < 10) asm volatile("" : "+v"(ix[it][i + 1]));
        }
      }
      const int col = lane & 15, g4 = (lane >> 4) * 4;
      unsigned t0 = tb0 + 8 + 2 * (128 * (col >> 3) + (col & 7) + 8 * g4), t1[4], t2[4];
      { const int s1 = col < 8 ? 4 : 2, b1 = (col < 8 ? 256 + 64 * (col >> 2) + (col & 3) : 384 + 32 * ((col - 8) >> 1) + (col & 1)) + s1 * g4;
#pragma unroll
        for (int e = 0; e < 4; ++e) { t1[e] = tb0 + 8 + 2 * (b1 + s1 * e); t2[e] = tb0 + 8 + 2 * min(512 + 16 * col + g4 + e, 641); asm volatile("" : "+v"(t1[e]), "+v"(t2[e])); }
      }
      float dsk = lane == 0 ? lo16(pd) : 0.f; asm volatile("" : "+v"(dsk), "+v"(t0));
      BAR(0);
      unsigned kp[3][2];
#pragma unroll
      for (int k = 0; k < 3; ++k) {
        const v4u u = ld128(lb + KD + kw * 6144 + k * 1024 + lane * 16), v = ld128(lb + KD + kw * 6144 + 3072 + k * 1024 + lane * 16);
        float f[4];
#pragma unroll
        for (int e = 0; e < 4; ++e) { f[e] = __uint_as_float(u[e]) + __uint_as_float(v[e]); asm volatile("" : "+v"(f[e])); }
        if (k == 0) f[0] += dsk;
        kp[k][0] = pk(f[0], f[1]); kp[k][1] = pk(f[2], f[3]);
      }
      waitl(); BAR(1);
      if (lane == 0) *(AS3 v2u*)tb0 = v2u{0u, 0u};
#pragma unroll
      for (int e = 0; e < 4; ++e) {
        *(AS3 u16*)(t0 + 16 * e) = (u16)(kp[0][e >> 1] >> (16 * (e & 1)));
        *(AS3 u16*)t1[e] = (u16)(kp[1][e >> 1] >> (16 * (e & 1)));
        *(AS3 u16*)t2[e] = (u16)(kp[2][e >> 1] >> (16 * (e & 1)));
      }
      const unsigned scr = lb + KX + kw * KXC;
      auto expand = [&](int it) {
        asm volatile("" ::: "memory");
        unsigned v[11];
#pragma unroll
        for (int i = 0; i < 11; ++i) v[i] = *(AS3 u16*)ix[it][i];
        fence();
        unsigned q[10];
#pragma unroll
        for (int i = 0; i < 10; ++i) q[i] = v[i] | (v[i + 1] << 16);
        const unsigned o = scr + it * 1024 + lane * 16;
        st128(o + 3 * KXS, v4u{q[0], q[2], q[4], q[6]}); st128(o + 2 * KXS, v4u{q[1], q[3], q[5], q[7]});
        st128(o + 1 * KXS, v4u{q[2], q[4], q[6], q[8]}); st128(o, v4u{q[3], q[5], q[7], q[9]});
        fence();
      };
      expand(0);
      BAR(2);
      expand(1); expand(2);
      BAR(3);
      expand(3);
      if (lane < 8) st128(scr + (lane >> 1) * KXS + 4096 + (lane & 1) * 16, v4u{0u, 0u, 0u, 0u});
      waitl(); BAR(4); afrags(0); BAR(5); afrags(2); BAR(6); BAR(7); BAR(8);
    }
    const int vdw = vd + aw * 8192;
    auto wpiece = [&](int s) { dma1(lb + wslot_lds(s) + aw * 1024, vdw, rd, wslot_q(s) * 32768); };
#pragma unroll
    for (int s = 13; s < 37; ++s) wpiece(s);
    ST(32);
    f32x4 ya[8];
#pragma unroll
    for (int c = 0; c < 8; ++c) ya[c] = f32x4{0.f, 0.f, 0.f, 0.f};
    const unsigned u0 = lb + UB + ch * UCH + (lane & 15) * 32 + (lane >> 4) * 16 - rh * 256;
    v4u uf[2][8];
#pragma unroll
    for (int c = 0; c < 8; ++c) uf[0][c] = ld128(u0 + 480 + 512 * c);
    lds_wait(lb + CTL + 4, 4);
#pragma unroll
    for (int s = 37; s < 43; ++s) wpiece(s);
    ST(33);
    fence();
#pragma unroll
    for (int p = 0; p < 4; ++p) {
#pragma unroll
      for (int cp = 0; cp < 8; ++cp) {
        if (p < 3) uf[(p + 1) & 1][cp] = ld128(u0 + 480 + 512 * cp - 64 * (p + 1));
#pragma unroll
        for (int c = cp; c < 8; ++c) ya[c] = mma16(af[p][c - cp], uf[p & 1][cp], ya[c]);
        fence();
      }
      if (p == 1) { waitv<0>(); lds_inc(lb + CTL + 8, lane); }
      if (p == 2) { waitl(); lds_inc(lb + CTL + 16, lane); }
    }
    ST(34);
    { const int G = lane >> 4, a = lane & 15;
      const int vo = (2 * b + ch) * 8192 + (4 * (G & 1) + (a >> 2)) * 256 + rh * 128 + (G >> 1) * 64 + (a & 3) * 16;
#pragma unroll
      for (int c = 0; c < 8; c += 2) {
        const auto s0 = __builtin_amdgcn_permlane16_swap(nm(pk(ya[c][0], ya[c][1])), nm(pk(ya[c + 1][0], ya[c + 1][1])), false, false);
        const auto s1 = __builtin_amdgcn_permlane16_swap(nm(pk(ya[c][2], ya[c][3])), nm(pk(ya[c + 1][2], ya[c + 1][3])), false, false);
        stws(v4u{s0[0], s1[0], s0[1], s1[1]}, rws, vo, AT + c * 1024);
      }
    }
    waitv<0>();
    ST(35);
    { v4u av[2][2];
      const int vo = (64 * c2 + 16 * aw + (lane >> 3)) * 8192 + rt * 256 + (lane & 7) * 16;
#pragma unroll
      for (int i = 0; i < 2; ++i)
#pragma unroll
        for (int h = 0; h < 2; ++h) av[i][h] = __builtin_amdgcn_raw_buffer_load_b128(rws, vo, AT + i * 65536 + h * 128, 16);
      lds_wait(lb + CTL + 16, 4);
#pragma unroll
      for (int i = 0; i < 2; ++i) {
        const v4u p0 = settle(av[i][0], rws, vo, AT + i * 65536), p1 = settle(av[i][1], rws, vo, AT + i * 65536 + 128);
        v4u s;
#pragma unroll
        for (int e = 0; e < 4; ++e) s[e] = pk(lo16(p0[e]) + lo16(p1[e]), hi16(p0[e]) + hi16(p1[e]));
        st128(lb + UB + aw * 2048 + i * 1024 + lane * 16, s);
      }
    }
    waitl();
    BAR(9); STW();
    if (b == 0 && aw == 1) {
#pragma unroll 1
      for (int i = 0; i < 4; ++i) poll(ws, FLG + (i * 64 + lane) * 128, ep);
      if (lane == 0) set_flag(ws, 0, ep);
    }
    return;
  }

  {
    const float dt = rbf(exp_acc(lo16(pt)));
    const float ma = rbf(exp_acc(lo16(pa))) * dt * -1.44269504088896340736f, ta = lo16(pi) * dt;
    const float IH = 0.15915493667125701904296875f, IL = 6.420638316725915e-9f;
    const float ha = ta * IH, la = __builtin_fmaf(ta, IL, __builtin_fmaf(ta, IH, -ha));
    auto seed = [&](float m, float sg) {
      const float xa = __builtin_amdgcn_exp2f(ma * m), fa = __builtin_fmaf(la, m, __builtin_amdgcn_fractf(ha * m));
      return f32x2{xa * __builtin_amdgcn_cosf(fa), sg * xa * __builtin_amdgcn_sinf(fa)};
    };
    const CW w1 = mkw(seed(1.f, 1.f)), w8 = mkw(seed(8.f, -1.f)), w128 = mkw(seed(128.f, 1.f));
    unsigned sw = lb + KS + w * SCW + lane * 4; asm volatile("" : "+v"(sw));
    auto put = [&](int i, f32x2 z) { *(volatile AS3 unsigned*)(sw + i * SCR) = pk(z.x, z.y); };
    f32x2 c[17], z = w8.w;
    c[0] = f32x2{lo16(pr), lo16(pc)} * (2.f * dt);
    put(0, f32x2{1.f, 0.f}); put(1, z); put(16, c[0]);
    const CW w2 = mkw(cmul(w1.w, w1)); c[1] = cmul(c[0], w128); put(24, c[1]);
    const CW w4 = mkw(cmul(w2.w, w2)); c[2] = cmul(c[1], w128); put(32, c[2]);
#pragma unroll
    for (int i = 2; i < 16; ++i) {
      z = cmul(z, w8); c[i + 1] = cmul(c[i], w128);
      put(i, z); put(i == 2 ? 36 : i < 7 ? 34 + 2 * i : 41 + i, c[i + 1]);
    }
    f32x2 y0 = c[0], y1 = c[1];
#pragma unroll
    for (int f = 1; f < 8; ++f) { y0 = cmul(y0, w1); y1 = cmul(y1, w1); put(16 + f, y0); put(24 + f, y1); }
    y0 = c[2]; y1 = c[3];
#pragma unroll
    for (int f = 1; f < 4; ++f) { y0 = cmul(y0, w2); y1 = cmul(y1, w2); put(32 + f, y0); put(36 + f, y1); }
#pragma unroll
    for (int i = 4; i < 8; i += 2) { y0 = cmul(c[i], w4); y1 = cmul(c[i + 1], w4); put(33 + 2 * i, y0); put(35 + 2 * i, y1); }
    asm volatile("" ::: "memory");
    ST(43);
    unsigned sr = lb + KS + w * SCW + (lane & 15) * SCR + (lane >> 4) * 16; asm volatile("" : "+v"(sr));
    f32x4 ka[3] = {{0.f, 0.f, 0.f, 0.f}, {0.f, 0.f, 0.f, 0.f}, {0.f, 0.f, 0.f, 0.f}};
#pragma unroll
    for (int s = 0; s < 4; ++s) {
      const v4u pf = ld128(sr + s * 64);
#pragma unroll
      for (int k = 0; k < 3; ++k) ka[k] = mma16(pf, ld128(sr + (k + 1) * 16 * SCR + s * 64), ka[k]);
    }
#pragma unroll
    for (int k = 0; k < 3; ++k) st128(lb + KD + w * 3072 + k * 1024 + lane * 16, __builtin_bit_cast(v4u, ka[k]));
  }
  ST(44);
  const int wn = w & 1, wm = w >> 1;
  f32x16 acc[3][2];
#pragma unroll
  for (int j = 0; j < 3; ++j)
#pragma unroll
    for (int t = 0; t < 2; ++t) {
#pragma unroll
      for (int e = 0; e < 16; ++e) acc[j][t][e] = 0.f;
      asm volatile("" : "+v"(acc[j][t]));
    }
  {
    const int tok = lane & 31, h = lane >> 5;
    unsigned wb = lb + (((lane >> 4) & 1) ? UO : GO) + 192 * (8 * h + ((lane >> 2) & 3)) + 32 * wn + 8 * (lane & 3);
    unsigned xb[4];
#pragma unroll
    for (int s = 0; s < 4; ++s) xb[s] = lb + (64 * wm + tok) * 128 + (((2 * s + h) ^ ((tok >> 1) & 7)) << 4);
    v4u A[2][3], B[2][2];
    auto rd = [&](int buf, int s) {
#pragma unroll
      for (int t = 0; t < 2; ++t) B[buf][t] = ld128(xb[s] + 4096 * t);
#pragma unroll
      for (int j = 0; j < 3; ++j) A[buf][j] = trd2(wb + 3072 * s + 64 * j, wb + 3072 * s + 768 + 64 * j);
    };
    auto mm = [&](int buf) {
#pragma unroll
      for (int j = 0; j < 3; ++j)
#pragma unroll
        for (int t = 0; t < 2; ++t) acc[j][t] = mma32(A[buf][j], B[buf][t], acc[j][t]);
#pragma unroll
      for (int i = 0; i < 4; ++i) { __builtin_amdgcn_sched_group_barrier(0x8, 1, 0); __builtin_amdgcn_sched_group_barrier(0x100, 2, 0); }
      __builtin_amdgcn_sched_group_barrier(0x8, 2, 0);
      fence();
    };
    fence(); waitl(); BAR(0);
    rd(0, 0); fence();
    int stg = 0;
#pragma unroll 1
    for (int t = 0; t < 8; ++t) {
      rd(1, 1); mm(0); rd(0, 2); mm(1); rd(1, 3); mm(0);
      const int dl = stg == 2 ? -2 * STG : STG; stg = stg == 2 ? 0 : stg + 1;
      wb += dl;
#pragma unroll
      for (int s = 0; s < 4; ++s) xb[s] += dl;
      fence(); waitl(); BAR(t + 1); fence();
      rd(0, 0); mm(1);
    }
  }
  ST(32);
  if (ct < 15) {
#pragma unroll
    for (int j = 0; j < 3; ++j) {
      const int unit = ct * 6 + 2 * j + wn;
      if (unit < NU) {
#pragma unroll
        for (int t = 0; t < 2; ++t) {
          v4u hv;
#pragma unroll
          for (int e = 0; e < 4; ++e) {
            const f32x2 g = {acc[j][t][2 * e], acc[j][t][2 * e + 1]}, u = {acc[j][t][2 * e + 8], acc[j][t][2 * e + 9]};
            const f32x2 z = g * __builtin_elementwise_fma(g * g, f32x2{-0.1029432397f, -0.1029432397f}, f32x2{-2.3022081986f, -2.3022081986f});
            const f32x2 dn = f32x2{__builtin_amdgcn_exp2f(z.x), __builtin_amdgcn_exp2f(z.y)} + 1.f;
            const f32x2 hh = g * u * f32x2{__builtin_amdgcn_rcpf(dn.x), __builtin_amdgcn_rcpf(dn.y)};
            hv[e] = nm(pk(hh.x, hh.y));
          }
          stws(hv, rws, lane * 16, HB + ((rp * 4 + wm * 2 + t) * NU + unit) * 1024);
          fence();
        }
      }
    }
  } else if (wn == 0) {
#pragma unroll
    for (int t = 0; t < 2; ++t) {
      const float s = rbf(acc[0][t][0] + acc[0][t][9]);
      const float g = rbf(__builtin_amdgcn_rcpf(1.f + __builtin_amdgcn_exp2f(s * -1.44269504088896340736f)));
      if (lane < 32) __builtin_amdgcn_raw_buffer_store_b32(__float_as_uint(g), rws, lane * 4, GV + (rp * 128 + wm * 64 + t * 32) * 4, 16);
    }
  }
  ST(33);
  const int kh = wn, tt = wm;
  const unsigned wl = kh * 2048 + (4 * (lane >> 5) + ((lane >> 2) & 3)) * 128 + ((((lane >> 3) & 1) ^ kh) * 64) + ((lane >> 4) & 1) * 32 + (lane & 1) * 16 + ((lane >> 1) & 1) * 8;
  unsigned wa[3][2];
#pragma unroll
  for (int i = 0; i < 3; ++i) { wa[i][0] = lb + wl + i * 61440; wa[i][1] = lb + (wl ^ 64) + i * 61440; asm volatile("" : "+v"(wa[i][0]), "+v"(wa[i][1])); }
  auto wfrag = [&](int s, int c) { const int o = wslot_lds(s), i = o / 61440; return trd2(wa[i][c] + (o - i * 61440), wa[i][c] + (o - i * 61440) + 1024); };
  v4u wr[7][2];
  lds_wait(lb + CTL, 1);
#pragma unroll
  for (int s = 0; s < 7; ++s) { wr[s][0] = wfrag(s, 0); wr[s][1] = wfrag(s, 1); }
  waitl(); lds_inc(lb + CTL + 4, lane);
  ST(34);
  constexpr int WIN = 10;
  const int hA = HB + ((rt * 2 + tt) * NU + kh) * 1024;
  v4u hf[WIN];
  auto hload = [&](int s) { hf[s % WIN] = __builtin_amdgcn_raw_buffer_load_b128(rws, lane * 16, hA + wslot_q(s) * 2048, 16); };
#pragma unroll
  for (int s = 0; s < WIN; ++s) hload(s);
  fence();
  f32x16 o0, o1;
#pragma unroll
  for (int e = 0; e < 16; ++e) { o0[e] = 0.f; o1[e] = 0.f; }
  v4u wf[3][2];
  unsigned gu = 0;
#pragma unroll
  for (int s = 0; s < 43; ++s) {
    if (s == 5) lds_wait(lb + CTL + 8, 4);
    if (s == 20) lds_inc(lb + CTL + 12, lane);
    if (s == 38) lds_wait(lb + CTL + 12, 4);
    const v4u hb = settle(hf[s % WIN], rws, lane * 16, hA + wslot_q(s) * 2048);
    o1 = mma32(s < 7 ? wr[s < 7 ? s : 0][1] : wf[s % 3][1], hb, o1);
    if (s >= 5 && s < 41) { wf[(s + 2) % 3][1] = wfrag(s + 2, 1); wf[(s + 2) % 3][0] = wfrag(s + 2, 0); }
    fence();
    o0 = mma32(s < 7 ? wr[s < 7 ? s : 0][0] : wf[s % 3][0], hb, o0);
    if (s + WIN < 43) hload(s + WIN);
    if (s + WIN == 43) gu = __builtin_amdgcn_raw_buffer_load_b32(rws, (lane & 31) * 4, GV + (rt * 64 + tt * 32) * 4, 16);
    fence();
  }
  char* op = (char*)out + (rt * 64 + tt * 32 + (lane & 31)) * 1024 + c2 * 128 + kh * 64 + (lane >> 5) * 16;
  const unsigned ex = lb + 2 * STG + lane * 16;
#pragma unroll
  for (int m = 0; m < 4; ++m)
    st128(ex + w * 4096 + m * 1024, v4u{__float_as_uint(o1[4 * m]), __float_as_uint(o1[4 * m + 1]), __float_as_uint(o1[4 * m + 2]), __float_as_uint(o1[4 * m + 3])});
  if (__builtin_expect(__any(gu == 0xffffffffu), 0)) {
    int i = 0;
#pragma unroll 1
    do { asm volatile("" ::: "memory"); gu = __builtin_amdgcn_raw_buffer_load_b32(rws, (lane & 31) * 4, GV + (rt * 64 + tt * 32) * 4, 16); } while (__any(gu == 0xffffffffu) && ++i < SPIN);
  }
  const float gt = __uint_as_float(gu), gb = rbf(1.f - gt);
  const unsigned a0 = lb + UB + (32 * kh + 8 * (lane >> 5) + ((lane >> 2) & 3)) * 128 + ((lane >> 1) & 1) * 64 + (2 * tt + ((lane >> 4) & 1)) * 16 + (lane & 1) * 8;
  fence(); waitl(); BAR(9);
  v2u at[4]; v4u pp[4];
#pragma unroll
  for (int m = 0; m < 4; ++m) { pp[m] = ld128(ex + (w ^ 1) * 4096 + m * 1024); at[m] = trd(a0 + (16 * (m >> 1) + 4 * (m & 1)) * 128); }
  fence();
  unsigned ov[8];
#pragma unroll
  for (int m = 0; m < 4; ++m) {
    const float r0 = gt * lo16(at[m].x) + gb * (o0[4 * m] + __uint_as_float(pp[m].x)), r1 = gt * hi16(at[m].x) + gb * (o0[4 * m + 1] + __uint_as_float(pp[m].y));
    const float r2 = gt * lo16(at[m].y) + gb * (o0[4 * m + 2] + __uint_as_float(pp[m].z)), r3 = gt * hi16(at[m].y) + gb * (o0[4 * m + 3] + __uint_as_float(pp[m].w));
    ov[2 * m] = pk(r0, r1); ov[2 * m + 1] = pk(r2, r3);
    if (m & 1) *(AS1 v4u*)(op + 16 * (m - 1)) = v4u{ov[2 * m - 2], ov[2 * m - 1], ov[2 * m], ov[2 * m + 1]};
  }
  ST(36); STW();
}

extern "C" void kernel_launch(void* const* d_in, const int* in_sizes, int n_in, void* d_out, int out_size, void* d_ws, size_t ws_size,
                              hipStream_t stream) {
  gated_mix_fused<<<256, 512, 0, stream>>>((const u16*)d_in[0], (const u16*)d_in[8], (const u16*)d_in[9], (const u16*)d_in[11],
                                           (const u16*)d_in[10], (char*)d_ws, (const u16*)d_in[1], (const u16*)d_in[2], (const u16*)d_in[3],
                                           (const u16*)d_in[4], (const u16*)d_in[5], (const u16*)d_in[6], (u16*)d_out);
}
```

```cpp
#define STAMP
#define PW 0
#define PI 0
#define PM 3
#include <hip/hip_runtime.h>
#include <stdint.h>

typedef unsigned short u16;
typedef unsigned long long u64;
typedef unsigned v4u __attribute__((ext_vector_type(4)));
typedef unsigned v2u __attribute__((ext_vector_type(2)));
typedef short s16x4 __attribute__((ext_vector_type(4)));
typedef short s16x8 __attribute__((ext_vector_type(8)));
typedef __bf16 bf16x8 __attribute__((ext_vector_type(8)));
typedef __bf16 bf16x2 __attribute__((ext_vector_type(2)));
typedef float f32x2 __attribute__((ext_vector_type(2)));
typedef float f32x4 __attribute__((ext_vector_type(4)));
typedef float f32x16 __attribute__((ext_vector_type(16)));
typedef __amdgpu_buffer_rsrc_t rsrc_t;
#define AS1 __attribute__((address_space(1)))
#define AS3 __attribute__((address_space(3)))
#define DI __device__ __forceinline__

constexpr int L = 2048, D = 512, F = 1376, NU = 86;
constexpr int STG = 41024, GO = 16384, UO = 16384 + 12288 + 32;
constexpr int R3 = 3 * STG;
constexpr int SCR = 288, SCW = 57 * SCR, KS = 2 * STG, KD = KS + 4 * SCW + 7 * SCR;
constexpr int KXS = 4160, KXC = 4 * KXS;
constexpr int KX = R3, KT = R3 + 2 * KXC, TBL = 1312;
constexpr int UB = R3, UCH = 4608, W0 = R3 + 2 * UCH;
constexpr int CTL = 163840 - 64;
static_assert(KD >= R3 && KD + 4 * 3072 <= CTL && KT + 2 * TBL <= CTL && W0 + 7 * 4096 <= CTL, "lds map");
constexpr int FLG = 4096, GV = FLG + 256 * 128;
constexpr int UT = 65536, AT = UT + L * D * 2, HB = AT + 2 * L * D * 2, WS_END = HB + L * F * 2;
constexpr int SPIN = 1 << 16;
constexpr int CODE_LINES = 224;

template <int N> DI void waitv() { asm volatile("s_waitcnt vmcnt(%0)" ::"n"(N) : "memory"); }
DI void waitl() { asm volatile("s_waitcnt lgkmcnt(0)" ::: "memory"); }
DI void bar() { asm volatile("s_barrier" ::: "memory"); }
DI void fence() { __builtin_amdgcn_sched_barrier(0); }
DI rsrc_t mk(const void* p, int bytes) { return __builtin_amdgcn_make_buffer_rsrc((void*)p, 0, bytes, 0x00020000); }
DI unsigned pk(float a, float b) { f32x2 v = {a, b}; return __builtin_bit_cast(unsigned, __builtin_convertvector(v, bf16x2)); }
DI float lo16(unsigned v) { return __uint_as_float(v << 16); }
DI float hi16(unsigned v) { return __uint_as_float(v & 0xffff0000u); }
DI float rbf(float a) { return lo16(pk(a, a)); }
DI v2u trd(unsigned a) { return __builtin_bit_cast(v2u, __builtin_amdgcn_ds_read_tr16_b64_v4i16((AS3 s16x4*)a)); }
DI v4u trd2(unsigned a, unsigned b) { v2u x = trd(a), y = trd(b); return v4u{x.x, x.y, y.x, y.y}; }
DI v4u ld128(unsigned a) { return *(AS3 v4u*)a; }
DI v2u ld64(unsigned a) { return *(AS3 v2u*)a; }
DI void st128(unsigned a, v4u v) { *(AS3 v4u*)a = v; }
DI f32x16 mma32(v4u a, v4u b, f32x16 c) {
  return __builtin_amdgcn_mfma_f32_32x32x16_bf16(__builtin_bit_cast(bf16x8, a), __builtin_bit_cast(bf16x8, b), c, 0, 0, 0);
}
DI f32x4 mma16(v4u a, v4u b, f32x4 c) {
  return __builtin_amdgcn_mfma_f32_16x16x32_bf16(__builtin_bit_cast(bf16x8, a), __builtin_bit_cast(bf16x8, b), c, 0, 0, 0);
}
DI void dma1(unsigned lds, int v0, rsrc_t rs, int so) {
  asm volatile("s_mov_b32 m0, %0\n\ts_nop 0\n\tbuffer_load_dwordx4 %1, %2, %3 offen lds" ::"s"(lds), "v"(v0), "s"(rs), "s"(so) : "memory");
}
DI void dma3(unsigned lds, const int* v, rsrc_t rs, int so) {
  asm volatile("s_mov_b32 m0, %0\n\ts_nop 0\n\tbuffer_load_dwordx4 %1, %4, %5 offen lds\n\t"
               "buffer_load_dwordx4 %2, %4, %5 offen offset:1024 lds\n\tbuffer_load_dwordx4 %3, %4, %5 offen offset:2048 lds"
               ::"s"(lds), "v"(v[0]), "v"(v[1]), "v"(v[2]), "s"(rs), "s"(so) : "memory");
}
DI void dma4(unsigned lds, const int* v, rsrc_t rs, int so) {
  asm volatile("s_mov_b32 m0, %0\n\ts_nop 0\n\tbuffer_load_dwordx4 %1, %5, %6 offen lds\n\t"
               "buffer_load_dwordx4 %2, %5, %6 offen offset:1024 lds\n\tbuffer_load_dwordx4 %3, %5, %6 offen offset:2048 lds\n\t"
               "buffer_load_dwordx4 %4, %5, %6 offen offset:3072 lds"
               ::"s"(lds), "v"(v[0]), "v"(v[1]), "v"(v[2]), "v"(v[3]), "s"(rs), "s"(so) : "memory");
}
DI void stws(v4u v, rsrc_t rs, int vo, int so) {
  __builtin_amdgcn_raw_buffer_store_b128(v, rs, vo, so, 16);
  asm volatile("s_nop 1" ::"v"(v) : "memory");
}
DI void set_flag(char* ws, int off, u64 ep) { __hip_atomic_store((AS1 u64*)(ws + off), ep, __ATOMIC_RELAXED, __HIP_MEMORY_SCOPE_AGENT); }
DI void poll(char* ws, int off, u64 ep) {
  AS1 u64* p = (AS1 u64*)(ws + off);
#pragma unroll 1
  for (int i = 0; i < SPIN; ++i) {
    u64 f = __hip_atomic_load(p, __ATOMIC_RELAXED, __HIP_MEMORY_SCOPE_AGENT);
    if (__all(f == ep)) break;
    __builtin_amdgcn_s_sleep(1);
  }
  __builtin_amdgcn_fence(__ATOMIC_ACQUIRE, "wavefront");
}
DI void lds_wait(unsigned a, unsigned want) {
#pragma unroll 1
  for (int i = 0; i < SPIN * 8; ++i) {
    unsigned f = __hip_atomic_load((AS3 unsigned*)a, __ATOMIC_RELAXED, __HIP_MEMORY_SCOPE_WORKGROUP);
    if (__builtin_amdgcn_readfirstlane(f) >= want) break;
    __builtin_amdgcn_s_sleep(1);
  }
  asm volatile("" ::: "memory");
}
DI void lds_inc(unsigned a, int lane) {
  if (lane == 0) __hip_atomic_fetch_add((AS3 unsigned*)a, 1u, __ATOMIC_RELAXED, __HIP_MEMORY_SCOPE_WORKGROUP);
}
DI float exp_acc(float x) {
  const float H = 1.44269502162933349609375f, Lo = 1.925963033500011079e-8f;
  float t = x * H, e = __builtin_fmaf(x, Lo, __builtin_fmaf(x, H, -t));
  float r = __builtin_amdgcn_exp2f(t);
  return __builtin_fmaf(r, e * 0.693147182464599609375f, r);
}
struct CW { f32x2 w, n; };
DI CW mkw(f32x2 w) { return CW{w, f32x2{-w.y, w.x}}; }
DI f32x2 cmul(f32x2 x, const CW& y) { return __builtin_elementwise_fma(x.yy, y.n, x.xx * y.w); }
constexpr int wslot_lds(int s) { return s < 7 ? W0 + 4096 * s : s < 17 ? 2 * STG + 4096 * (s - 7) : s < 27 ? 4096 * (s - 17) : s < 37 ? STG + 4096 * (s - 27) : W0 + 4096 * (s - 37); }
constexpr int wslot_q(int s) { return s < 15 ? 3 * s : s < 29 ? 3 * (s - 15) + 1 : 3 * (s - 29) + 2; }
DI unsigned nm(unsigned v) { return min(v, 0xfffffffeu); }
DI bool unset(v4u x) { return __any(max(max(x.x, x.y), max(x.z, x.w)) == 0xffffffffu); }
DI v4u settle(v4u x, rsrc_t rs, int vo, int so) {
  if (__builtin_expect(unset(x), 0)) {
    int i = 0;
#pragma unroll 1
    do { asm volatile("" ::: "memory"); x = __builtin_amdgcn_raw_buffer_load_b128(rs, vo, so, 16); } while (unset(x) && ++i < SPIN);
  }
  return x;
}

#define ST(i) do { u64 t_; asm volatile("s_memtime %0\n\ts_waitcnt lgkmcnt(0)" : "=s"(t_) :: "memory"); stv = lane == (i) ? (unsigned)t_ : stv; asm volatile("" : "+v"(stv)); } while (0)
#define STR(i) do { u64 t_; asm volatile("s_memrealtime %0\n\ts_waitcnt lgkmcnt(0)" : "=s"(t_) :: "memory"); stv = lane == (i) ? (unsigned)t_ : stv; asm volatile("" : "+v"(stv)); } while (0)
#define STW() do { STR(63); ((AS1 unsigned*)(ws + (32 << 20)))[(b * 8 + w) * 64 + lane] = stv; } while (0)
#define BAR(i) do { ST(2 * (i)); bar(); ST(2 * (i) + 1); } while (0)

__global__ __launch_bounds__(512, 2) void gated_mix_fused(
    const u16* __restrict__ x, const u16* __restrict__ wg, const u16* __restrict__ wu, const u16* __restrict__ gw,
    const u16* __restrict__ wd, char* ws, const u16* __restrict__ logA, const u16* __restrict__ Aim,
    const u16* __restrict__ Cr, const u16* __restrict__ Ci, const u16* __restrict__ logdt, const u16* __restrict__ dskip,
    u16* __restrict__ out) {
  __shared__ __attribute__((aligned(16))) char smem[163840];
  const unsigned lb = (unsigned)(size_t)(AS3 char*)smem;
  u64 pc0; asm volatile("s_getpc_b64 %0" : "=s"(pc0));
  const int b = blockIdx.x, xcd = b & 7, r = b >> 3;
  const int w = __builtin_amdgcn_readfirstlane(threadIdx.x >> 6), lane = threadIdx.x & 63;
  unsigned stv = 0; ST(61); STR(62);
  const int rp = (xcd & 1) * 8 + (r & 7), ct = (xcd >> 1) * 4 + (r >> 3);
  const int k0 = ((r & 7) + 2 * (r >> 3) + (xcd >> 2)) & 7;
  const int rt = xcd * 4 + (r & 3), c2 = r >> 2;
  int vx[4], vg[3], vu[3];
#pragma unroll
  for (int p = 0; p < 4; ++p) vx[p] = (rp * 128 + (lane >> 3)) * 1024 + (((lane & 7) ^ ((lane >> 4) + 4 * (p & 1))) * 16) + p * 7168;
#pragma unroll
  for (int i = 0; i < 3; ++i) {
    const int q = i * 64 + lane, row = (q * 171) >> 11, c = q - row * 12;
    const int o = row * (F * 2) + c * 16 + ct * 192 - i * 1024, og = 2048 - i * 1024 + (row & ~1) * 2;
    vg[i] = ct < 15 ? o : (c | (row & 1)) ? 0x40000000 : og;
    vu[i] = ct < 15 ? o : (c | (~row & 1)) ? 0x40000000 : og;
  }
  asm volatile("" : "+v"(vx[0]), "+v"(vx[1]), "+v"(vx[2]), "+v"(vx[3]), "+v"(vg[0]), "+v"(vg[1]), "+v"(vg[2]), "+v"(vu[0]), "+v"(vu[1]), "+v"(vu[2]));
  asm volatile("" : "+s"(x), "+s"(wg), "+s"(wu), "+s"(gw), "+s"(wd), "+s"(ws), "+s"(logA), "+s"(Aim), "+s"(Cr), "+s"(Ci), "+s"(logdt), "+s"(dskip), "+s"(out));
  const rsrc_t rws = mk(ws, WS_END), rx = mk(x, L * D * 2), rd = mk(wd, F * D * 2);
  const rsrc_t rg = ct < 15 ? mk(wg, D * F * 2) : mk((const char*)gw - 2048, 2048 + D * 2);
  const rsrc_t ru = ct < 15 ? mk(wu, D * F * 2) : mk((const char*)gw - 2048, 2048 + D * 2);
  const int wrow = ct < 15 ? F * 2 : 2;
  auto part1 = [&](int t) {
    const int kt = (t + k0) & 7; const unsigned s = lb + (t % 3) * STG;
#pragma unroll
    for (int g = 0; g < 4; ++g) dma4(s + g * 4096, vx, rx, kt * 128 + g * 32768);
#pragma unroll
    for (int g = 0; g < 2; ++g) { const int so = (kt * 64 + g * 16) * wrow; dma3(s + GO + g * 3072, vg, rg, so); dma3(s + UO + g * 3072, vu, ru, so); }
  };
  auto part2 = [&](int t) {
    const int kt = (t + k0) & 7; const unsigned s = lb + (t % 3) * STG;
#pragma unroll
    for (int g = 2; g < 4; ++g) { const int so = (kt * 64 + g * 16) * wrow; dma3(s + GO + g * 3072, vg, rg, so); dma3(s + UO + g * 3072, vu, ru, so); }
  };
  unsigned pa = 0, pi = 0, pr = 0, pc = 0, pt = 0;
  if (w < 4) {
    const int i = b * 256 + w * 64 + lane;
    pa = ((const AS1 u16*)logA)[i]; pi = ((const AS1 u16*)Aim)[i]; pr = ((const AS1 u16*)Cr)[i]; pc = ((const AS1 u16*)Ci)[i];
    int d = 2 * b + (w >> 1); asm volatile("" : "+v"(d));
    pt = ((const AS1 u16*)logdt)[d];
  } else if (w == 5) {
    pa = __builtin_amdgcn_raw_buffer_load_b32(mk((const void*)(pc0 & ~255ull), CODE_LINES * 128), lane < CODE_LINES / 32 ? (lane * 32 + r) * 128 : 0x40000000, 0, 0);
  } else if (w == 4) {
    part1(0); part2(0);
  }
  fence();
  ST(40);

  if (w >= 4) {
    const int aw = w - 4;
    const int vd = (lane >> 3) * 1024 + (((lane & 7) ^ (((lane >> 4) & 1) * 4)) * 16) + c2 * 128;
    const int ch = aw >> 1, rh = aw & 1;
    v4u af[4][8];
    const unsigned a0 = lb + KX + ch * KXC + (lane & 3) * KXS - ((lane >> 2) & 3) * 8 + (lane >> 4) * 16 - rh * 256 + 280;
    auto afrags = [&](int p0) {
      unsigned a = a0; asm volatile("" : "+v"(a));
#pragma unroll
      for (int p = p0; p < p0 + 2; ++p)
#pragma unroll
        for (int e = 0; e < 8; ++e) {
          const int o = 3776 - 512 * e - 64 * p;
          const v2u u0 = *(volatile AS3 v2u*)(a + o), u1 = *(volatile AS3 v2u*)(a + o + 8);
          af[p][e] = v4u{u0.x, u0.y, u1.x, u1.y};
        }
      waitl();
    };
    u64 ep = 0;
    if (aw == 0) {
#pragma unroll 1
      for (int t = 0; t < 5; ++t) { part1(t + 1); waitv<28>(); BAR(t); part2(t + 1); }
      part1(6); afrags(0); waitv<28>(); BAR(5); part2(6);
      part1(7); afrags(2); waitv<28>(); BAR(6); part2(7);
      int v4[4];
#pragma unroll
      for (int p = 0; p < 4; ++p) v4[p] = vd + p * 7168;
#pragma unroll
      for (int s = 0; s < 7; ++s) dma4(lb + wslot_lds(s), v4, rd, wslot_q(s) * 32768);
      waitv<28>(); BAR(7);
#pragma unroll
      for (int s = 7; s < 13; ++s) dma4(lb + wslot_lds(s), v4, rd, wslot_q(s) * 32768);
      BAR(8);
      waitv<24>(); lds_inc(lb + CTL, lane);
    } else if (aw == 1) {
      ep = *(const __attribute__((address_space(4))) u64*)ws + 1;
      if (lane < 16) *(AS3 unsigned*)(lb + CTL + lane * 4) = 0;
      const v4u ones = {~0u, ~0u, ~0u, ~0u};
      if (ct < 15) {
        const int nu = ct < 14 ? 6 : 2;
#pragma unroll 1
        for (int t = 0; t < 4; ++t)
#pragma unroll 1
          for (int j = 0; j < nu; ++j) stws(ones, rws, lane * 16, HB + ((rp * 4 + t) * NU + ct * 6 + j) * 1024);
      } else {
        if (lane < 32) stws(ones, rws, lane * 16, GV + rp * 512);
#pragma unroll
        for (int i = 0; i < 11; ++i) pa ^= __builtin_amdgcn_raw_buffer_load_b32(rd, lane * 128 + i * 8192, rp * 88064, 0);
      }
#pragma unroll 4
      for (int i = 0; i < 16; ++i) stws(ones, rws, lane * 16, AT + b * 16384 + i * 1024);
      waitl(); BAR(0);
      const int half = (xcd >> 1) & 1, G = lane >> 4, q = (lane >> 2) & 3, p = lane & 3;
      v4u tv[4][2];
#pragma unroll
      for (int j = 0; j < 4; ++j)
#pragma unroll
        for (int s = 0; s < 2; ++s) {
          v2u h2[2];
#pragma unroll
          for (int t = 0; t < 2; ++t) {
            const int row = 64 * (G & 1) + 16 * p + 8 * s + 4 * t + q;
            h2[t] = trd(lb + row * 128 + (((4 * half + j) ^ ((row >> 1) & 7)) << 4) + 8 * (G >> 1));
          }
          tv[j][s] = v4u{h2[0].x, h2[0].y, h2[1].x, h2[1].y};
        }
      waitl(); BAR(1);
      const int vo = (32 * k0 + 16 * half + 2 * (G >> 1)) * 8192 + rp * 512 + (lane & 31) * 16;
      const bool lo = (lane & 2) == 0;
#pragma unroll
      for (int j = 0; j < 4; ++j) {
        v4u n0, n1;
#pragma unroll
        for (int e = 0; e < 4; ++e) {
          const unsigned a = tv[j][0][e], c = tv[j][1][e];
          const unsigned as = __builtin_amdgcn_update_dpp(0u, a, 0x4E, 0xf, 0xf, true), cs = __builtin_amdgcn_update_dpp(0u, c, 0x4E, 0xf, 0xf, true);
          n0[e] = lo ? a : cs; n1[e] = lo ? as : c;
        }
        stws(n0, rws, vo, UT + j * 32768);
        stws(n1, rws, vo, UT + j * 32768 + 8192);
      }
      BAR(2); BAR(3);
      waitv<0>();
      asm volatile("" ::"v"(pa));
      if (lane == 0) set_flag(ws, FLG + b * 128, ep);
      BAR(4); afrags(0); BAR(5); afrags(2);
      { const int cg = b >> 4, i = lane & 15;
        const int prp = lane < 16 ? i : rt >> 1, dk = ((cg >> 1) - prp) & 7;
        const int pct = lane < 16 ? 4 * (2 * (dk & 1) + (cg & 1)) + (dk >> 1) : i;
        const int pb = lane < 32 ? (prp >> 3) + 2 * (pct >> 2) + 8 * ((prp & 7) + 8 * (pct & 3)) : 32 * c2 + lane - 32;
        poll(ws, FLG + pb * 128, ep);
      }
      BAR(6);
      v4u uv[8];
#pragma unroll
      for (int i = 0; i < 8; ++i) uv[i] = __builtin_amdgcn_raw_buffer_load_b128(rws, lane * 16, UT + b * 8192 + i * 1024, 16);
      fence(); BAR(7); fence();
      st128(lb + UB + (lane >> 5) * UCH + (lane & 31) * 16, v4u{0u, 0u, 0u, 0u});
#pragma unroll
      for (int i = 0; i < 8; ++i) st128(lb + UB + (lane & 1) * UCH + 512 + (lane >> 1) * 16 + i * 512, uv[i]);
      waitl(); BAR(8);
    } else {
      const int kw = aw - 2;
      int d = 2 * b + kw; asm volatile("" : "+v"(d));
      const unsigned pd = ((const AS1 u16*)dskip)[d];
      const unsigned tb0 = lb + KT + kw * TBL;
      unsigned ix[4][12];
#pragma unroll
      for (int it = 0; it < 4; ++it) {
        float top = (float)(2047 - 512 * it - 8 * lane); asm volatile("" : "+v"(top));
#pragma unroll
        for (int i = 0; i < 12; i += 2) {
          const float l0 = top - i, l1 = top - (i + 1);
          const unsigned p = it == 3 ? pk(__builtin_fmaxf(l0, 128.f), __builtin_fmaxf(l1, 128.f)) : pk(l0, l1);
          unsigned i0 = (p & 0xffffu) * 2 + (tb0 - 2 * 0x427c), i1 = (p >> 16) * 2 + (tb0 - 2 * 0x427c);
          if (it == 3) { i0 = min(i0, tb0 + 2 * (515 - i) - 16 * lane); i1 = min(i1, tb0 + 2 * (514 - i) - 16 * lane); }
          ix[it][i] = i0; ix[it][i + 1] = i1;
          asm volatile("" : "+v"(ix[it][i])); if (i < 10) asm volatile("" : "+v"(ix[it][i + 1]));
        }
      }
      const int col = lane & 15, g4 = (lane >> 4) * 4;
      unsigned t0 = tb0 + 8 + 2 * (128 * (col >> 3) + (col & 7) + 8 * g4), t1[4], t2[4];
      { const int s1 = col < 8 ? 4 : 2, b1 = (col < 8 ? 256 + 64 * (col >> 2) + (col & 3) : 384 + 32 * ((col - 8) >> 1) + (col & 1)) + s1 * g4;
#pragma unroll
        for (int e = 0; e < 4; ++e) { t1[e] = tb0 + 8 + 2 * (b1 + s1 * e); t2[e] = tb0 + 8 + 2 * min(512 + 16 * col + g4 + e, 641); asm volatile("" : "+v"(t1[e]), "+v"(t2[e])); }
      }
      float dsk = lane == 0 ? lo16(pd) : 0.f; asm volatile("" : "+v"(dsk), "+v"(t0));
      BAR(0);
      unsigned kp[3][2];
#pragma unroll
      for (int k = 0; k < 3; ++k) {
        const v4u u = ld128(lb + KD + kw * 6144 + k * 1024 + lane * 16), v = ld128(lb + KD + kw * 6144 + 3072 + k * 1024 + lane * 16);
        float f[4];
#pragma unroll
        for (int e = 0; e < 4; ++e) { f[e] = __uint_as_float(u[e]) + __uint_as_float(v[e]); asm volatile("" : "+v"(f[e])); }
        if (k == 0) f[0] += dsk;
        kp[k][0] = pk(f[0], f[1]); kp[k][1] = pk(f[2], f[3]);
      }
      waitl(); BAR(1);
      if (lane == 0) *(AS3 v2u*)tb0 = v2u{0u, 0u};
#pragma unroll
      for (int e = 0; e < 4; ++e) {
        *(AS3 u16*)(t0 + 16 * e) = (u16)(kp[0][e >> 1] >> (16 * (e & 1)));
        *(AS3 u16*)t1[e] = (u16)(kp[1][e >> 1] >> (16 * (e & 1)));
        *(AS3 u16*)t2[e] = (u16)(kp[2][e >> 1] >> (16 * (e & 1)));
      }
      const unsigned scr = lb + KX + kw * KXC;
      auto expand = [&](int it) {
        asm volatile("" ::: "memory");
        unsigned v[11];
#pragma unroll
        for (int i = 0; i < 11; ++i) v[i] = *(AS3 u16*)ix[it][i];
        fence();
        unsigned q[10];
#pragma unroll
        for (int i = 0; i < 10; ++i) q[i] = v[i] | (v[i + 1] << 16);
        const unsigned o = scr + it * 1024 + lane * 16;
        st128(o + 3 * KXS, v4u{q[0], q[2], q[4], q[6]}); st128(o + 2 * KXS, v4u{q[1], q[3], q[5], q[7]});
        st128(o + 1 * KXS, v4u{q[2], q[4], q[6], q[8]}); st128(o, v4u{q[3], q[5], q[7], q[9]});
        fence();
      };
      expand(0);
      BAR(2);
      expand(1); expand(2);
      BAR(3);
      expand(3);
      if (lane < 8) st128(scr + (lane >> 1) * KXS + 4096 + (lane & 1) * 16, v4u{0u, 0u, 0u, 0u});
      waitl(); BAR(4); afrags(0); BAR(5); afrags(2); BAR(6); BAR(7); BAR(8);
    }
    const int vdw = vd + aw * 8192;
    auto wpiece = [&](int s) { dma1(lb + wslot_lds(s) + aw * 1024, vdw, rd, wslot_q(s) * 32768); };
#pragma unroll
    for (int s = 13; s < 37; ++s) wpiece(s);
    ST(32);
    f32x4 ya[8];
#pragma unroll
    for (int c = 0; c < 8; ++c) ya[c] = f32x4{0.f, 0.f, 0.f, 0.f};
    const unsigned u0 = lb + UB + ch * UCH + (lane & 15) * 32 + (lane >> 4) * 16 - rh * 256;
    v4u uf[2][8];
#pragma unroll
    for (int c = 0; c < 8; ++c) uf[0][c] = ld128(u0 + 480 + 512 * c);
    lds_wait(lb + CTL + 4, 4);
#pragma unroll
    for (int s = 37; s < 43; ++s) wpiece(s);
    ST(33);
    fence();
#pragma unroll
    for (int p = 0; p < 4; ++p) {
#pragma unroll
      for (int cp = 0; cp < 8; ++cp) {
        if (p < 3) uf[(p + 1) & 1][cp] = ld128(u0 + 480 + 512 * cp - 64 * (p + 1));
#pragma unroll
        for (int c = cp; c < 8; ++c) ya[c] = mma16(af[p][c - cp], uf[p & 1][cp], ya[c]);
        fence();
      }
      if (p == 1) { waitv<0>(); lds_inc(lb + CTL + 8, lane); }
      if (p == 2) { waitl(); lds_inc(lb + CTL + 16, lane); }
    }
    ST(34);
    { const int G = lane >> 4, a = lane & 15;
      const int vo = (2 * b + ch) * 8192 + (4 * (G & 1) + (a >> 2)) * 256 + rh * 128 + (G >> 1) * 64 + (a & 3) * 16;
#pragma unroll
      for (int c = 0; c < 8; c += 2) {
        const auto s0 = __builtin_amdgcn_permlane16_swap(nm(pk(ya[c][0], ya[c][1])), nm(pk(ya[c + 1][0], ya[c + 1][1])), false, false);
        const auto s1 = __builtin_amdgcn_permlane16_swap(nm(pk(ya[c][2], ya[c][3])), nm(pk(ya[c + 1][2], ya[c + 1][3])), false, false);
        stws(v4u{s0[0], s1[0], s0[1], s1[1]}, rws, vo, AT + c * 1024);
      }
    }
    waitv<0>();
    ST(35);
    { v4u av[2][2];
      const int vo = (64 * c2 + 16 * aw + (lane >> 3)) * 8192 + rt * 256 + (lane & 7) * 16;
#pragma unroll
      for (int i = 0; i < 2; ++i)
#pragma unroll
        for (int h = 0; h < 2; ++h) av[i][h] = __builtin_amdgcn_raw_buffer_load_b128(rws, vo, AT + i * 65536 + h * 128, 16);
      lds_wait(lb + CTL + 16, 4);
#pragma unroll
      for (int i = 0; i < 2; ++i) {
        const v4u p0 = settle(av[i][0], rws, vo, AT + i * 65536), p1 = settle(av[i][1], rws, vo, AT + i * 65536 + 128);
        v4u s;
#pragma unroll
        for (int e = 0; e < 4; ++e) s[e] = pk(lo16(p0[e]) + lo16(p1[e]), hi16(p0[e]) + hi16(p1[e]));
        st128(lb + UB + aw * 2048 + i * 1024 + lane * 16, s);
      }
    }
    waitl();
    BAR(9); STW();
    if (b == 0 && aw == 1) {
#pragma unroll 1
      for (int i = 0; i < 4; ++i) poll(ws, FLG + (i * 64 + lane) * 128, ep);
      if (lane == 0) set_flag(ws, 0, ep);
    }
    return;
  }

  {
    ST(41); waitv<0>(); ST(42); asm volatile("" : "+v"(pt), "+v"(pa), "+v"(pi), "+v"(pr), "+v"(pc));
    const float dt = rbf(exp_acc(lo16(pt)));
    const float ma = rbf(exp_acc(lo16(pa))) * dt * -1.44269504088896340736f, ta = lo16(pi) * dt;
    const float IH = 0.15915493667125701904296875f, IL = 6.420638316725915e-9f;
    const float ha = ta * IH, la = __builtin_fmaf(ta, IL, __builtin_fmaf(ta, IH, -ha));
    auto seed = [&](float m, float sg) {
      const float xa = __builtin_amdgcn_exp2f(ma * m), fa = __builtin_fmaf(la, m, __builtin_amdgcn_fractf(ha * m));
      return f32x2{xa * __builtin_amdgcn_cosf(fa), sg * xa * __builtin_amdgcn_sinf(fa)};
    };
    const CW w1 = mkw(seed(1.f, 1.f)), w8 = mkw(seed(8.f, -1.f)), w128 = mkw(seed(128.f, 1.f));
    unsigned sw = lb + KS + w * SCW + lane * 4; asm volatile("" : "+v"(sw));
    auto put = [&](int i, f32x2 z) { *(volatile AS3 unsigned*)(sw + i * SCR) = pk(z.x, z.y); };
    f32x2 c[17], z = w8.w;
    c[0] = f32x2{lo16(pr), lo16(pc)} * (2.f * dt);
    put(0, f32x2{1.f, 0.f}); put(1, z); put(16, c[0]);
    const CW w2 = mkw(cmul(w1.w, w1)); c[1] = cmul(c[0], w128); put(24, c[1]);
    const CW w4 = mkw(cmul(w2.w, w2)); c[2] = cmul(c[1], w128); put(32, c[2]);
#pragma unroll
    for (int i = 2; i < 16; ++i) {
      z = cmul(z, w8); c[i + 1] = cmul(c[i], w128);
      put(i, z); put(i == 2 ? 36 : i < 7 ? 34 + 2 * i : 41 + i, c[i + 1]);
    }
    f32x2 y0 = c[0], y1 = c[1];
#pragma unroll
    for (int f = 1; f < 8; ++f) { y0 = cmul(y0, w1); y1 = cmul(y1, w1); put(16 + f, y0); put(24 + f, y1); }
    y0 = c[2]; y1 = c[3];
#pragma unroll
    for (int f = 1; f < 4; ++f) { y0 = cmul(y0, w2); y1 = cmul(y1, w2); put(32 + f, y0); put(36 + f, y1); }
#pragma unroll
    for (int i = 4; i < 8; i += 2) { y0 = cmul(c[i], w4); y1 = cmul(c[i + 1], w4); put(33 + 2 * i, y0); put(35 + 2 * i, y1); }
    asm volatile("" ::: "memory");
    ST(43);
    unsigned sr = lb + KS + w * SCW + (lane & 15) * SCR + (lane >> 4) * 16; asm volatile("" : "+v"(sr));
    f32x4 ka[3] = {{0.f, 0.f, 0.f, 0.f}, {0.f, 0.f, 0.f, 0.f}, {0.f, 0.f, 0.f, 0.f}};
#pragma unroll
    for (int s = 0; s < 4; ++s) {
      const v4u pf = ld128(sr + s * 64);
#pragma unroll
      for (int k = 0; k < 3; ++k) ka[k] = mma16(pf, ld128(sr + (k + 1) * 16 * SCR + s * 64), ka[k]);
    }
#pragma unroll
    for (int k = 0; k < 3; ++k) st128(lb + KD + w * 3072 + k * 1024 + lane * 16, __builtin_bit_cast(v4u, ka[k]));
  }
  ST(44);
  const int wn = w & 1, wm = w >> 1;
  f32x16 acc[3][2];
#pragma unroll
  for (int j = 0; j < 3; ++j)
#pragma unroll
    for (int t = 0; t < 2; ++t) {
#pragma unroll
      for (int e = 0; e < 16; ++e) acc[j][t][e] = 0.f;
      asm volatile("" : "+v"(acc[j][t]));
    }
  {
    const int tok = lane & 31, h = lane >> 5;
    unsigned wb = lb + (((lane >> 4) & 1) ? UO : GO) + 192 * (8 * h + ((lane >> 2) & 3)) + 32 * wn + 8 * (lane & 3);
    unsigned xb[4];
#pragma unroll
    for (int s = 0; s < 4; ++s) xb[s] = lb + (64 * wm + tok) * 128 + (((2 * s + h) ^ ((tok >> 1) & 7)) << 4);
    v4u A[2][3], B[2][2];
    auto rd = [&](int buf, int s) {
#pragma unroll
      for (int t = 0; t < 2; ++t) B[buf][t] = ld128(xb[s] + 4096 * t);
#pragma unroll
      for (int j = 0; j < 3; ++j) A[buf][j] = trd2(wb + 3072 * s + 64 * j, wb + 3072 * s + 768 + 64 * j);
    };
    auto mm = [&](int buf) {
#pragma unroll
      for (int j = 0; j < 3; ++j)
#pragma unroll
        for (int t = 0; t < 2; ++t) acc[j][t] = mma32(A[buf][j], B[buf][t], acc[j][t]);
#pragma unroll
      for (int i = 0; i < 4; ++i) { __builtin_amdgcn_sched_group_barrier(0x8, 1, 0); __builtin_amdgcn_sched_group_barrier(0x100, 2, 0); }
      __builtin_amdgcn_sched_group_barrier(0x8, 2, 0);
      fence();
    };
    fence(); waitl(); BAR(0);
    rd(0, 0); fence();
    int stg = 0;
#pragma unroll 1
    for (int t = 0; t < 8; ++t) {
      rd(1, 1); mm(0); rd(0, 2); mm(1); rd(1, 3); mm(0);
      const int dl = stg == 2 ? -2 * STG : STG; stg = stg == 2 ? 0 : stg + 1;
      wb += dl;
#pragma unroll
      for (int s = 0; s < 4; ++s) xb[s] += dl;
      fence(); waitl(); BAR(t + 1); fence();
      rd(0, 0); mm(1);
    }
  }
  ST(32);
  if (ct < 15) {
#pragma unroll
    for (int j = 0; j < 3; ++j) {
      const int unit = ct * 6 + 2 * j + wn;
      if (unit < NU) {
#pragma unroll
        for (int t = 0; t < 2; ++t) {
          v4u hv;
#pragma unroll
          for (int e = 0; e < 4; ++e) {
            const f32x2 g = {acc[j][t][2 * e], acc[j][t][2 * e + 1]}, u = {acc[j][t][2 * e + 8], acc[j][t][2 * e + 9]};
            const f32x2 z = g * __builtin_elementwise_fma(g * g, f32x2{-0.1029432397f, -0.1029432397f}, f32x2{-2.3022081986f, -2.3022081986f});
            const f32x2 dn = f32x2{__builtin_amdgcn_exp2f(z.x), __builtin_amdgcn_exp2f(z.y)} + 1.f;
            const f32x2 hh = g * u * f32x2{__builtin_amdgcn_rcpf(dn.x), __builtin_amdgcn_rcpf(dn.y)};
            hv[e] = nm(pk(hh.x, hh.y));
          }
          stws(hv, rws, lane * 16, HB + ((rp * 4 + wm * 2 + t) * NU + unit) * 1024);
          fence();
        }
      }
    }
  } else if (wn == 0) {
#pragma unroll
    for (int t = 0; t < 2; ++t) {
      const float s = rbf(acc[0][t][0] + acc[0][t][9]);
      const float g = rbf(__builtin_amdgcn_rcpf(1.f + __builtin_amdgcn_exp2f(s * -1.44269504088896340736f)));
      if (lane < 32) __builtin_amdgcn_raw_buffer_store_b32(__float_as_uint(g), rws, lane * 4, GV + (rp * 128 + wm * 64 + t * 32) * 4, 16);
    }
  }
  ST(33);
  const int kh = wn, tt = wm;
  const unsigned wl = kh * 2048 + (4 * (lane >> 5) + ((lane >> 2) & 3)) * 128 + ((((lane >> 3) & 1) ^ kh) * 64) + ((lane >> 4) & 1) * 32 + (lane & 1) * 16 + ((lane >> 1) & 1) * 8;
  unsigned wa[3][2];
#pragma unroll
  for (int i = 0; i < 3; ++i) { wa[i][0] = lb + wl + i * 61440; wa[i][1] = lb + (wl ^ 64) + i * 61440; asm volatile("" : "+v"(wa[i][0]), "+v"(wa[i][1])); }
  auto wfrag = [&](int s, int c) { const int o = wslot_lds(s), i = o / 61440; return trd2(wa[i][c] + (o - i * 61440), wa[i][c] + (o - i * 61440) + 1024); };
  v4u wr[7][2];
  lds_wait(lb + CTL, 1);
#pragma unroll
  for (int s = 0; s < 7; ++s) { wr[s][0] = wfrag(s, 0); wr[s][1] = wfrag(s, 1); }
  waitl(); lds_inc(lb + CTL + 4, lane);
  ST(34);
  constexpr int WIN = 10;
  const int hA = HB + ((rt * 2 + tt) * NU + kh) * 1024;
  v4u hf[WIN];
  auto hload = [&](int s) { hf[s % WIN] = __builtin_amdgcn_raw_buffer_load_b128(rws, lane * 16, hA + wslot_q(s) * 2048, 16); };
#pragma unroll
  for (int s = 0; s < WIN; ++s) hload(s);
  fence();
  f32x16 o0, o1;
#pragma unroll
  for (int e = 0; e < 16; ++e) { o0[e] = 0.f; o1[e] = 0.f; }
  v4u wf[3][2];
  unsigned gu = 0;
#pragma unroll
  for (int s = 0; s < 43; ++s) {
    if (s == 5) lds_wait(lb + CTL + 8, 4);
    if (s == 20) lds_inc(lb + CTL + 12, lane);
    if (s == 38) lds_wait(lb + CTL + 12, 4);
    const v4u hb = settle(hf[s % WIN], rws, lane * 16, hA + wslot_q(s) * 2048);
    if (s == 0) ST(35);
    o1 = mma32(s < 7 ? wr[s < 7 ? s : 0][1] : wf[s % 3][1], hb, o1);
    if (s >= 5 && s < 41) { wf[(s + 2) % 3][1] = wfrag(s + 2, 1); wf[(s + 2) % 3][0] = wfrag(s + 2, 0); }
    fence();
    o0 = mma32(s < 7 ? wr[s < 7 ? s : 0][0] : wf[s % 3][0], hb, o0);
    if (s + WIN < 43) hload(s + WIN);
    if (s + WIN == 43) gu = __builtin_amdgcn_raw_buffer_load_b32(rws, (lane & 31) * 4, GV + (rt * 64 + tt * 32) * 4, 16);
    fence();
  }
  char* op = (char*)out + (rt * 64 + tt * 32 + (lane & 31)) * 1024 + c2 * 128 + kh * 64 + (lane >> 5) * 16;
  const unsigned ex = lb + 2 * STG + lane * 16;
#pragma unroll
  for (int m = 0; m < 4; ++m)
    st128(ex + w * 4096 + m * 1024, v4u{__float_as_uint(o1[4 * m]), __float_as_uint(o1[4 * m + 1]), __float_as_uint(o1[4 * m + 2]), __float_as_uint(o1[4 * m + 3])});
  if (__builtin_expect(__any(gu == 0xffffffffu), 0)) {
    int i = 0;
#pragma unroll 1
    do { asm volatile("" ::: "memory"); gu = __builtin_amdgcn_raw_buffer_load_b32(rws, (lane & 31) * 4, GV + (rt * 64 + tt * 32) * 4, 16); } while (__any(gu == 0xffffffffu) && ++i < SPIN);
  }
  const float gt = __uint_as_float(gu), gb = rbf(1.f - gt);
  const unsigned a0 = lb + UB + (32 * kh + 8 * (lane >> 5) + ((lane >> 2) & 3)) * 128 + ((lane >> 1) & 1) * 64 + (2 * tt + ((lane >> 4) & 1)) * 16 + (lane & 1) * 8;
  fence(); waitl(); BAR(9);
  v2u at[4]; v4u pp[4];
#pragma unroll
  for (int m = 0; m < 4; ++m) { pp[m] = ld128(ex + (w ^ 1) * 4096 + m * 1024); at[m] = trd(a0 + (16 * (m >> 1) + 4 * (m & 1)) * 128); }
  fence();
  unsigned ov[8];
#pragma unroll
  for (int m = 0; m < 4; ++m) {
    const float r0 = gt * lo16(at[m].x) + gb * (o0[4 * m] + __uint_as_float(pp[m].x)), r1 = gt * hi16(at[m].x) + gb * (o0[4 * m + 1] + __uint_as_float(pp[m].y));
    const float r2 = gt * lo16(at[m].y) + gb * (o0[4 * m + 2] + __uint_as_float(pp[m].z)), r3 = gt * hi16(at[m].y) + gb * (o0[4 * m + 3] + __uint_as_float(pp[m].w));
    ov[2 * m] = pk(r0, r1); ov[2 * m + 1] = pk(r2, r3);
    if (m & 1) *(AS1 v4u*)(op + 16 * (m - 1)) = v4u{ov[2 * m - 2], ov[2 * m - 1], ov[2 * m], ov[2 * m + 1]};
  }
  ST(37); waitv<0>();
  ST(36); STW();
}

__global__ void k_spin(const unsigned* st){
  __shared__ unsigned sm[4][256];
  int b=threadIdx.x;
  unsigned s0=st[(b*8)*64+62], e=0; for(int w=0;w<8;w++) e=max(e,st[(b*8+w)*64+63]);
  sm[0][b]=s0; sm[1][b]=e; sm[2][b]=st[(b*8+PW)*64+PI]-st[(b*8)*64+61];
  __syncthreads();
  if(b==0){ unsigned mn=~0u,mxs=0,mxe=0; unsigned long long sum=0; int n=0;
    for(int i=0;i<256;i++){ mn=min(mn,sm[0][i]); mxs=max(mxs,sm[0][i]); mxe=max(mxe,sm[1][i]); int ct=((i&7)>>1)*4+(i>>6); if(ct<14){ sum+=sm[2][i]; n++; } }
    unsigned long long m = PM==0 ? sum/n : PM==1 ? (mxe-mn)*10ull : PM==2 ? (mxs-mn)*10ull : 0ull;
    if(m>200000) m=200000;
    unsigned long long t=__builtin_amdgcn_s_memrealtime();
    while(__builtin_amdgcn_s_memrealtime()-t<m) __builtin_amdgcn_s_sleep(2);
  }
}
extern "C" void kernel_launch(void* const* d_in, const int* in_sizes, int n_in, void* d_out, int out_size, void* d_ws, size_t ws_size,
                              hipStream_t stream) {
  gated_mix_fused<<<256, 512, 0, stream>>>((const u16*)d_in[0], (const u16*)d_in[8], (const u16*)d_in[9], (const u16*)d_in[11],
                                           (const u16*)d_in[10], (char*)d_ws, (const u16*)d_in[1], (const u16*)d_in[2], (const u16*)d_in[3],
                                           (const u16*)d_in[4], (const u16*)d_in[5], (const u16*)d_in[6], (u16*)d_out);
  k_spin<<<1,256,0,stream>>>((const unsigned*)((char*)d_ws+(32<<20)));
}
```

```cpp
#define PM 0
#include <hip/hip_runtime.h>
#include <stdint.h>

typedef unsigned short u16;
typedef unsigned long long u64;
typedef unsigned v4u __attribute__((ext_vector_type(4)));
typedef unsigned v2u __attribute__((ext_vector_type(2)));
typedef short s16x4 __attribute__((ext_vector_type(4)));
typedef short s16x8 __attribute__((ext_vector_type(8)));
typedef __bf16 bf16x8 __attribute__((ext_vector_type(8)));
typedef __bf16 bf16x2 __attribute__((ext_vector_type(2)));
typedef float f32x2 __attribute__((ext_vector_type(2)));
typedef float f32x4 __attribute__((ext_vector_type(4)));
typedef float f32x16 __attribute__((ext_vector_type(16)));
typedef __amdgpu_buffer_rsrc_t rsrc_t;
#define AS1 __attribute__((address_space(1)))
#define AS3 __attribute__((address_space(3)))
#define DI __device__ __forceinline__

constexpr int L = 2048, D = 512, F = 1376, NU = 86;
constexpr int STG = 41024, GO = 16384, UO = 16384 + 12288 + 32;
constexpr int R3 = 3 * STG;
constexpr int SCR = 288, SCW = 57 * SCR, KS = 2 * STG, KD = KS + 4 * SCW + 7 * SCR;
constexpr int KXS = 4160, KXC = 4 * KXS;
constexpr int KX = R3, KT = R3 + 2 * KXC, TBL = 1312;
constexpr int UB = R3, UCH = 4608, W0 = R3 + 2 * UCH;
constexpr int CTL = 163840 - 64;
static_assert(KD >= R3 && KD + 4 * 3072 <= CTL && KT + 2 * TBL <= CTL && W0 + 7 * 4096 <= CTL, "lds map");
constexpr int FLG = 4096, GV = FLG + 256 * 128;
constexpr int UT = 65536, AT = UT + L * D * 2, HB = AT + 2 * L * D * 2, WS_END = HB + L * F * 2;
constexpr int SPIN = 1 << 16;
constexpr int CODE_LINES = 224;

template <int N> DI void waitv() { asm volatile("s_waitcnt vmcnt(%0)" ::"n"(N) : "memory"); }
DI void waitl() { asm volatile("s_waitcnt lgkmcnt(0)" ::: "memory"); }
DI void bar() { asm volatile("s_barrier" ::: "memory"); }
DI void fence() { __builtin_amdgcn_sched_barrier(0); }
DI rsrc_t mk(const void* p, int bytes) { return __builtin_amdgcn_make_buffer_rsrc((void*)p, 0, bytes, 0x00020000); }
DI unsigned pk(float a, float b) { f32x2 v = {a, b}; return __builtin_bit_cast(unsigned, __builtin_convertvector(v, bf16x2)); }
DI float lo16(unsigned v) { return __uint_as_float(v << 16); }
DI float hi16(unsigned v) { return __uint_as_float(v & 0xffff0000u); }
DI float rbf(float a) { return lo16(pk(a, a)); }
DI v2u trd(unsigned a) { return __builtin_bit_cast(v2u, __builtin_amdgcn_ds_read_tr16_b64_v4i16((AS3 s16x4*)a)); }
DI v4u trd2(unsigned a, unsigned b) { v2u x = trd(a), y = trd(b); return v4u{x.x, x.y, y.x, y.y}; }
DI v4u ld128(unsigned a) { return *(AS3 v4u*)a; }
DI v2u ld64(unsigned a) { return *(AS3 v2u*)a; }
DI void st128(unsigned a, v4u v) { *(AS3 v4u*)a = v; }
DI f32x16 mma32(v4u a, v4u b, f32x16 c) {
  return __builtin_amdgcn_mfma_f32_32x32x16_bf16(__builtin_bit_cast(bf16x8, a), __builtin_bit_cast(bf16x8, b), c, 0, 0, 0);
}
DI f32x4 mma16(v4u a, v4u b, f32x4 c) {
  return __builtin_amdgcn_mfma_f32_16x16x32_bf16(__builtin_bit_cast(bf16x8, a), __builtin_bit_cast(bf16x8, b), c, 0, 0, 0);
}
DI void dma1(unsigned lds, int v0, rsrc_t rs, int so) {
  asm volatile("s_mov_b32 m0, %0\n\ts_nop 0\n\tbuffer_load_dwordx4 %1, %2, %3 offen lds" ::"s"(lds), "v"(v0), "s"(rs), "s"(so) : "memory");
}
DI void dma3(unsigned lds, const int* v, rsrc_t rs, int so) {
  asm volatile("s_mov_b32 m0, %0\n\ts_nop 0\n\tbuffer_load_dwordx4 %1, %4, %5 offen lds\n\t"
               "buffer_load_dwordx4 %2, %4, %5 offen offset:1024 lds\n\tbuffer_load_dwordx4 %3, %4, %5 offen offset:2048 lds"
               ::"s"(lds), "v"(v[0]), "v"(v[1]), "v"(v[2]), "s"(rs), "s"(so) : "memory");
}
DI void dma4(unsigned lds, const int* v, rsrc_t rs, int so) {
  asm volatile("s_mov_b32 m0, %0\n\ts_nop 0\n\tbuffer_load_dwordx4 %1, %5, %6 offen lds\n\t"
               "buffer_load_dwordx4 %2, %5, %6 offen offset:1024 lds\n\tbuffer_load_dwordx4 %3, %5, %6 offen offset:2048 lds\n\t"
               "buffer_load_dwordx4 %4, %5, %6 offen offset:3072 lds"
               ::"s"(lds), "v"(v[0]), "v"(v[1]), "v"(v[2]), "v"(v[3]), "s"(rs), "s"(so) : "memory");
}
DI void stws(v4u v, rsrc_t rs, int vo, int so) {
  __builtin_amdgcn_raw_buffer_store_b128(v, rs, vo, so, 16);
  asm volatile("s_nop 1" ::"v"(v) : "memory");
}
DI void set_flag(char* ws, int off, u64 ep) { __hip_atomic_store((AS1 u64*)(ws + off), ep, __ATOMIC_RELAXED, __HIP_MEMORY_SCOPE_AGENT); }
DI void poll(char* ws, int off, u64 ep) {
  AS1 u64* p = (AS1 u64*)(ws + off);
#pragma unroll 1
  for (int i = 0; i < SPIN; ++i) {
    u64 f = __hip_atomic_load(p, __ATOMIC_RELAXED, __HIP_MEMORY_SCOPE_AGENT);
    if (__all(f == ep)) break;
    __builtin_amdgcn_s_sleep(1);
  }
  __builtin_amdgcn_fence(__ATOMIC_ACQUIRE, "wavefront");
}
DI void lds_wait(unsigned a, unsigned want) {
#pragma unroll 1
  for (int i = 0; i < SPIN * 8; ++i) {
    unsigned f = __hip_atomic_load((AS3 unsigned*)a, __ATOMIC_RELAXED, __HIP_MEMORY_SCOPE_WORKGROUP);
    if (__builtin_amdgcn_readfirstlane(f) >= want) break;
    __builtin_amdgcn_s_sleep(1);
  }
  asm volatile("" ::: "memory");
}
DI void lds_inc(unsigned a, int lane) {
  if (lane == 0) __hip_atomic_fetch_add((AS3 unsigned*)a, 1u, __ATOMIC_RELAXED, __HIP_MEMORY_SCOPE_WORKGROUP);
}
DI float exp_acc(float x) {
  const float H = 1.44269502162933349609375f, Lo = 1.925963033500011079e-8f;
  float t = x * H, e = __builtin_fmaf(x, Lo, __builtin_fmaf(x, H, -t));
  float r = __builtin_amdgcn_exp2f(t);
  return __builtin_fmaf(r, e * 0.693147182464599609375f, r);
}
struct CW { f32x2 w, n; };
DI CW mkw(f32x2 w) { return CW{w, f32x2{-w.y, w.x}}; }
DI f32x2 cmul(f32x2 x, const CW& y) { return __builtin_elementwise_fma(x.yy, y.n, x.xx * y.w); }
constexpr int wslot_lds(int s) { return s < 7 ? W0 + 4096 * s : s < 17 ? 2 * STG + 4096 * (s - 7) : s < 27 ? 4096 * (s - 17) : s < 37 ? STG + 4096 * (s - 27) : W0 + 4096 * (s - 37); }
constexpr int wslot_q(int s) { return s < 15 ? 3 * s : s < 29 ? 3 * (s - 15) + 1 : 3 * (s - 29) + 2; }
DI unsigned nm(unsigned v) { return min(v, 0xfffffffeu); }
DI bool unset(v4u x) { return __any(max(max(x.x, x.y), max(x.z, x.w)) == 0xffffffffu); }
DI v4u settle(v4u x, rsrc_t rs, int vo, int so) {
  if (__builtin_expect(unset(x), 0)) {
    int i = 0;
#pragma unroll 1
    do { asm volatile("" ::: "memory"); x = __builtin_amdgcn_raw_buffer_load_b128(rs, vo, so, 16); } while (unset(x) && ++i < SPIN);
  }
  return x;
}

#define ST(i)
#define STR(i)
#define STW()
#define BAR(i) bar()

__global__ __launch_bounds__(512, 2) void gated_mix_fused(
    const u16* __restrict__ x, const u16* __restrict__ wg, const u16* __restrict__ wu, const u16* __restrict__ gw,
    const u16* __restrict__ wd, char* ws, const u16* __restrict__ logA, const u16* __restrict__ Aim,
    const u16* __restrict__ Cr, const u16* __restrict__ Ci, const u16* __restrict__ logdt, const u16* __restrict__ dskip,
    u16* __restrict__ out) {
  __shared__ __attribute__((aligned(16))) char smem[163840];
  const unsigned lb = (unsigned)(size_t)(AS3 char*)smem;
  u64 pc0; asm volatile("s_getpc_b64 %0" : "=s"(pc0));
  const int b = blockIdx.x, xcd = b & 7, r = b >> 3;
  const int w = __builtin_amdgcn_readfirstlane(threadIdx.x >> 6), lane = threadIdx.x & 63;
  const int rp = (xcd & 1) * 8 + (r & 7), ct = (xcd >> 1) * 4 + (r >> 3);
  const int k0 = ((r & 7) + 2 * (r >> 3) + (xcd >> 2)) & 7;
  const int rt = xcd * 4 + (r & 3), c2 = r >> 2;
  int vx[4], vg[3], vu[3];
#pragma unroll
  for (int p = 0; p < 4; ++p) vx[p] = (rp * 128 + (lane >> 3)) * 1024 + (((lane & 7) ^ ((lane >> 4) + 4 * (p & 1))) * 16) + p * 7168;
#pragma unroll
  for (int i = 0; i < 3; ++i) {
    const int q = i * 64 + lane, row = (q * 171) >> 11, c = q - row * 12;
    const int o = row * (F * 2) + c * 16 + ct * 192 - i * 1024, og = 2048 - i * 1024 + (row & ~1) * 2;
    vg[i] = ct < 15 ? o : (c | (row & 1)) ? 0x40000000 : og;
    vu[i] = ct < 15 ? o : (c | (~row & 1)) ? 0x40000000 : og;
  }
  asm volatile("" : "+v"(vx[0]), "+v"(vx[1]), "+v"(vx[2]), "+v"(vx[3]), "+v"(vg[0]), "+v"(vg[1]), "+v"(vg[2]), "+v"(vu[0]), "+v"(vu[1]), "+v"(vu[2]));
  asm volatile("" : "+s"(x), "+s"(wg), "+s"(wu), "+s"(gw), "+s"(wd), "+s"(ws), "+s"(logA), "+s"(Aim), "+s"(Cr), "+s"(Ci), "+s"(logdt), "+s"(dskip), "+s"(out));
  const rsrc_t rws = mk(ws, WS_END), rx = mk(x, L * D * 2), rd = mk(wd, F * D * 2);
  const rsrc_t rg = ct < 15 ? mk(wg, D * F * 2) : mk((const char*)gw - 2048, 2048 + D * 2);
  const rsrc_t ru = ct < 15 ? mk(wu, D * F * 2) : mk((const char*)gw - 2048, 2048 + D * 2);
  const int wrow = ct < 15 ? F * 2 : 2;
  auto part1 = [&](int t) {
    const int kt = (t + k0) & 7; const unsigned s = lb + (t % 3) * STG;
#pragma unroll
    for (int g = 0; g < 4; ++g) dma4(s + g * 4096, vx, rx, kt * 128 + g * 32768);
#pragma unroll
    for (int g = 0; g < 2; ++g) { const int so = (kt * 64 + g * 16) * wrow; dma3(s + GO + g * 3072, vg, rg, so); dma3(s + UO + g * 3072, vu, ru, so); }
  };
  auto part2 = [&](int t) {
    const int kt = (t + k0) & 7; const unsigned s = lb + (t % 3) * STG;
#pragma unroll
    for (int g = 2; g < 4; ++g) { const int so = (kt * 64 + g * 16) * wrow; dma3(s + GO + g * 3072, vg, rg, so); dma3(s + UO + g * 3072, vu, ru, so); }
  };
  unsigned pa = 0, pi = 0, pr = 0, pc = 0, pt = 0;
  if (w < 4) {
    const int i = b * 256 + w * 64 + lane;
    pa = ((const AS1 u16*)logA)[i]; pi = ((const AS1 u16*)Aim)[i]; pr = ((const AS1 u16*)Cr)[i]; pc = ((const AS1 u16*)Ci)[i];
    int d = 2 * b + (w >> 1); asm volatile("" : "+v"(d));
    pt = ((const AS1 u16*)logdt)[d];
  } else if (w == 5) {
    pa = __builtin_amdgcn_raw_buffer_load_b32(mk((const void*)(pc0 & ~255ull), CODE_LINES * 128), lane < CODE_LINES / 32 ? (lane * 32 + r) * 128 : 0x40000000, 0, 0);
  } else if (w == 4) {
    part1(0); part2(0);
  }
  fence();
  ST(40);

  if (w >= 4) {
    const int aw = w - 4;
    const int vd = (lane >> 3) * 1024 + (((lane & 7) ^ (((lane >> 4) & 1) * 4)) * 16) + c2 * 128;
    const int ch = aw >> 1, rh = aw & 1;
    v4u af[4][8];
    const unsigned a0 = lb + KX + ch * KXC + (lane & 3) * KXS - ((lane >> 2) & 3) * 8 + (lane >> 4) * 16 - rh * 256 + 280;
    auto afrags = [&](int p0) {
      unsigned a = a0; asm volatile("" : "+v"(a));
#pragma unroll
      for (int p = p0; p < p0 + 2; ++p)
#pragma unroll
        for (int e = 0; e < 8; ++e) {
          const int o = 3776 - 512 * e - 64 * p;
          const v2u u0 = *(volatile AS3 v2u*)(a + o), u1 = *(volatile AS3 v2u*)(a + o + 8);
          af[p][e] = v4u{u0.x, u0.y, u1.x, u1.y};
        }
      waitl();
    };
    u64 ep = 0;
    if (aw == 0) {
#pragma unroll 1
      for (int t = 0; t < 5; ++t) { part1(t + 1); waitv<28>(); BAR(t); part2(t + 1); }
      part1(6); afrags(0); waitv<28>(); BAR(5); part2(6);
      part1(7); afrags(2); waitv<28>(); BAR(6); part2(7);
      int v4[4];
#pragma unroll
      for (int p = 0; p < 4; ++p) v4[p] = vd + p * 7168;
#pragma unroll
      for (int s = 0; s < 7; ++s) dma4(lb + wslot_lds(s), v4, rd, wslot_q(s) * 32768);
      waitv<28>(); BAR(7);
#pragma unroll
      for (int s = 7; s < 13; ++s) dma4(lb + wslot_lds(s), v4, rd, wslot_q(s) * 32768);
      BAR(8);
      waitv<24>(); lds_inc(lb + CTL, lane);
    } else if (aw == 1) {
      ep = *(const __attribute__((address_space(4))) u64*)ws + 1;
      if (lane < 16) *(AS3 unsigned*)(lb + CTL + lane * 4) = 0;
      const v4u ones = {~0u, ~0u, ~0u, ~0u};
      if (ct < 15) {
        const int nu = ct < 14 ? 6 : 2;
#pragma unroll 1
        for (int t = 0; t < 4; ++t)
#pragma unroll 1
          for (int j = 0; j < nu; ++j) stws(ones, rws, lane * 16, HB + ((rp * 4 + t) * NU + ct * 6 + j) * 1024);
      } else {
        if (lane < 32) stws(ones, rws, lane * 16, GV + rp * 512);
#pragma unroll
        for (int i = 0; i < 11; ++i) pa ^= __builtin_amdgcn_raw_buffer_load_b32(rd, lane * 128 + i * 8192, rp * 88064, 0);
      }
#pragma unroll 4
      for (int i = 0; i < 16; ++i) stws(ones, rws, lane * 16, AT + b * 16384 + i * 1024);
      waitl(); BAR(0);
      const int half = (xcd >> 1) & 1, G = lane >> 4, q = (lane >> 2) & 3, p = lane & 3;
      v4u tv[4][2];
#pragma unroll
      for (int j = 0; j < 4; ++j)
#pragma unroll
        for (int s = 0; s < 2; ++s) {
          v2u h2[2];
#pragma unroll
          for (int t = 0; t < 2; ++t) {
            const int row = 64 * (G & 1) + 16 * p + 8 * s + 4 * t + q;
            h2[t] = trd(lb + row * 128 + (((4 * half + j) ^ ((row >> 1) & 7)) << 4) + 8 * (G >> 1));
          }
          tv[j][s] = v4u{h2[0].x, h2[0].y, h2[1].x, h2[1].y};
        }
      waitl(); BAR(1);
      const int vo = (32 * k0 + 16 * half + 2 * (G >> 1)) * 8192 + rp * 512 + (lane & 31) * 16;
      const bool lo = (lane & 2) == 0;
#pragma unroll
      for (int j = 0; j < 4; ++j) {
        v4u n0, n1;
#pragma unroll
        for (int e = 0; e < 4; ++e) {
          const unsigned a = tv[j][0][e], c = tv[j][1][e];
          const unsigned as = __builtin_amdgcn_update_dpp(0u, a, 0x4E, 0xf, 0xf, true), cs = __builtin_amdgcn_update_dpp(0u, c, 0x4E, 0xf, 0xf, true);
          n0[e] = lo ? a : cs; n1[e] = lo ? as : c;
        }
        stws(n0, rws, vo, UT + j * 32768);
        stws(n1, rws, vo, UT + j * 32768 + 8192);
      }
      BAR(2); BAR(3);
      waitv<0>();
      asm volatile("" ::"v"(pa));
      if (lane == 0) set_flag(ws, FLG + b * 128, ep);
      BAR(4); afrags(0); BAR(5); afrags(2);
      { const int cg = b >> 4, i = lane & 15;
        const int prp = lane < 16 ? i : rt >> 1, dk = ((cg >> 1) - prp) & 7;
        const int pct = lane < 16 ? 4 * (2 * (dk & 1) + (cg & 1)) + (dk >> 1) : i;
        const int pb = lane < 32 ? (prp >> 3) + 2 * (pct >> 2) + 8 * ((prp & 7) + 8 * (pct & 3)) : 32 * c2 + lane - 32;
        poll(ws, FLG + pb * 128, ep);
      }
      BAR(6);
      v4u uv[8];
#pragma unroll
      for (int i = 0; i < 8; ++i) uv[i] = __builtin_amdgcn_raw_buffer_load_b128(rws, lane * 16, UT + b * 8192 + i * 1024, 16);
      fence(); BAR(7); fence();
      st128(lb + UB + (lane >> 5) * UCH + (lane & 31) * 16, v4u{0u, 0u, 0u, 0u});
#pragma unroll
      for (int i = 0; i < 8; ++i) st128(lb + UB + (lane & 1) * UCH + 512 + (lane >> 1) * 16 + i * 512, uv[i]);
      waitl(); BAR(8);
    } else {
      const int kw = aw - 2;
      int d = 2 * b + kw; asm volatile("" : "+v"(d));
      const unsigned pd = ((const AS1 u16*)dskip)[d];
      const unsigned tb0 = lb + KT + kw * TBL;
      unsigned ix[4][12];
#pragma unroll
      for (int it = 0; it < 4; ++it) {
        float top = (float)(2047 - 512 * it - 8 * lane); asm volatile("" : "+v"(top));
#pragma unroll
        for (int i = 0; i < 12; i += 2) {
          const float l0 = top - i, l1 = top - (i + 1);
          const unsigned p = it == 3 ? pk(__builtin_fmaxf(l0, 128.f), __builtin_fmaxf(l1, 128.f)) : pk(l0, l1);
          unsigned i0 = (p & 0xffffu) * 2 + (tb0 - 2 * 0x427c), i1 = (p >> 16) * 2 + (tb0 - 2 * 0x427c);
          if (it == 3) { i0 = min(i0, tb0 + 2 * (515 - i) - 16 * lane); i1 = min(i1, tb0 + 2 * (514 - i) - 16 * lane); }
          ix[it][i] = i0; ix[it][i + 1] = i1;
          asm volatile("" : "+v"(ix[it][i])); if (i < 10) asm volatile("" : "+v"(ix[it][i + 1]));
        }
      }
      const int col = lane & 15, g4 = (lane >> 4) * 4;
      unsigned t0 = tb0 + 8 + 2 * (128 * (col >> 3) + (col & 7) + 8 * g4), t1[4], t2[4];
      { const int s1 = col < 8 ? 4 : 2, b1 = (col < 8 ? 256 + 64 * (col >> 2) + (col & 3) : 384 + 32 * ((col - 8) >> 1) + (col & 1)) + s1 * g4;
#pragma unroll
        for (int e = 0; e < 4; ++e) { t1[e] = tb0 + 8 + 2 * (b1 + s1 * e); t2[e] = tb0 + 8 + 2 * min(512 + 16 * col + g4 + e, 641); asm volatile("" : "+v"(t1[e]), "+v"(t2[e])); }
      }
      float dsk = lane == 0 ? lo16(pd) : 0.f; asm volatile("" : "+v"(dsk), "+v"(t0));
      BAR(0);
      unsigned kp[3][2];
#pragma unroll
      for (int k = 0; k < 3; ++k) {
        const v4u u = ld128(lb + KD + kw * 6144 + k * 1024 + lane * 16), v = ld128(lb + KD + kw * 6144 + 3072 + k * 1024 + lane * 16);
        float f[4];
#pragma unroll
        for (int e = 0; e < 4; ++e) { f[e] = __uint_as_float(u[e]) + __uint_as_float(v[e]); asm volatile("" : "+v"(f[e])); }
        if (k == 0) f[0] += dsk;
        kp[k][0] = pk(f[0], f[1]); kp[k][1] = pk(f[2], f[3]);
      }
      waitl(); BAR(1);
      if (lane == 0) *(AS3 v2u*)tb0 = v2u{0u, 0u};
#pragma unroll
      for (int e = 0; e < 4; ++e) {
        *(AS3 u16*)(t0 + 16 * e) = (u16)(kp[0][e >> 1] >> (16 * (e & 1)));
        *(AS3 u16*)t1[e] = (u16)(kp[1][e >> 1] >> (16 * (e & 1)));
        *(AS3 u16*)t2[e] = (u16)(kp[2][e >> 1] >> (16 * (e & 1)));
      }
      const unsigned scr = lb + KX + kw * KXC;
      auto expand = [&](int it) {
        asm volatile("" ::: "memory");
        unsigned v[11];
#pragma unroll
        for (int i = 0; i < 11; ++i) v[i] = *(AS3 u16*)ix[it][i];
        fence();
        unsigned q[10];
#pragma unroll
        for (int i = 0; i < 10; ++i) q[i] = v[i] | (v[i + 1] << 16);
        const unsigned o = scr + it * 1024 + lane * 16;
        st128(o + 3 * KXS, v4u{q[0], q[2], q[4], q[6]}); st128(o + 2 * KXS, v4u{q[1], q[3], q[5], q[7]});
        st128(o + 1 * KXS, v4u{q[2], q[4], q[6], q[8]}); st128(o, v4u{q[3], q[5], q[7], q[9]});
        fence();
      };
      expand(0);
      BAR(2);
      expand(1); expand(2);
      BAR(3);
      expand(3);
      if (lane < 8) st128(scr + (lane >> 1) * KXS + 4096 + (lane & 1) * 16, v4u{0u, 0u, 0u, 0u});
      waitl(); BAR(4); afrags(0); BAR(5); afrags(2); BAR(6); BAR(7); BAR(8);
    }
    const int vdw = vd + aw * 8192;
    auto wpiece = [&](int s) { dma1(lb + wslot_lds(s) + aw * 1024, vdw, rd, wslot_q(s) * 32768); };
#pragma unroll
    for (int s = 13; s < 37; ++s) wpiece(s);
    ST(32);
    f32x4 ya[8];
#pragma unroll
    for (int c = 0; c < 8; ++c) ya[c] = f32x4{0.f, 0.f, 0.f, 0.f};
    const unsigned u0 = lb + UB + ch * UCH + (lane & 15) * 32 + (lane >> 4) * 16 - rh * 256;
    v4u uf[2][8];
#pragma unroll
    for (int c = 0; c < 8; ++c) uf[0][c] = ld128(u0 + 480 + 512 * c);
    lds_wait(lb + CTL + 4, 4);
#pragma unroll
    for (int s = 37; s < 43; ++s) wpiece(s);
    ST(33);
    fence();
#pragma unroll
    for (int p = 0; p < 4; ++p) {
#pragma unroll
      for (int cp = 0; cp < 8; ++cp) {
        if (p < 3) uf[(p + 1) & 1][cp] = ld128(u0 + 480 + 512 * cp - 64 * (p + 1));
#pragma unroll
        for (int c = cp; c < 8; ++c) ya[c] = mma16(af[p][c - cp], uf[p & 1][cp], ya[c]);
        fence();
      }
      if (p == 1) { waitv<0>(); lds_inc(lb + CTL + 8, lane); }
      if (p == 2) { waitl(); lds_inc(lb + CTL + 16, lane); }
    }
    ST(34);
    { const int G = lane >> 4, a = lane & 15;
      const int vo = (2 * b + ch) * 8192 + (4 * (G & 1) + (a >> 2)) * 256 + rh * 128 + (G >> 1) * 64 + (a & 3) * 16;
#pragma unroll
      for (int c = 0; c < 8; c += 2) {
        const auto s0 = __builtin_amdgcn_permlane16_swap(nm(pk(ya[c][0], ya[c][1])), nm(pk(ya[c + 1][0], ya[c + 1][1])), false, false);
        const auto s1 = __builtin_amdgcn_permlane16_swap(nm(pk(ya[c][2], ya[c][3])), nm(pk(ya[c + 1][2], ya[c + 1][3])), false, false);
        stws(v4u{s0[0], s1[0], s0[1], s1[1]}, rws, vo, AT + c * 1024);
      }
    }
    waitv<0>();
    ST(35);
    { v4u av[2][2];
      const int vo = (64 * c2 + 16 * aw + (lane >> 3)) * 8192 + rt * 256 + (lane & 7) * 16;
#pragma unroll
      for (int i = 0; i < 2; ++i)
#pragma unroll
        for (int h = 0; h < 2; ++h) av[i][h] = __builtin_amdgcn_raw_buffer_load_b128(rws, vo, AT + i * 65536 + h * 128, 16);
      lds_wait(lb + CTL + 16, 4);
#pragma unroll
      for (int i = 0; i < 2; ++i) {
        const v4u p0 = settle(av[i][0], rws, vo, AT + i * 65536), p1 = settle(av[i][1], rws, vo, AT + i * 65536 + 128);
        v4u s;
#pragma unroll
        for (int e = 0; e < 4; ++e) s[e] = pk(lo16(p0[e]) + lo16(p1[e]), hi16(p0[e]) + hi16(p1[e]));
        st128(lb + UB + aw * 2048 + i * 1024 + lane * 16, s);
      }
    }
    waitl();
    BAR(9); STW();
    if (b == 0 && aw == 1) {
#pragma unroll 1
      for (int i = 0; i < 4; ++i) poll(ws, FLG + (i * 64 + lane) * 128, ep);
      if (lane == 0) set_flag(ws, 0, ep);
    }
    return;
  }

  {
    const float dt = rbf(exp_acc(lo16(pt)));
    const float ma = rbf(exp_acc(lo16(pa))) * dt * -1.44269504088896340736f, ta = lo16(pi) * dt;
    const float IH = 0.15915493667125701904296875f, IL = 6.420638316725915e-9f;
    const float ha = ta * IH, la = __builtin_fmaf(ta, IL, __builtin_fmaf(ta, IH, -ha));
    auto seed = [&](float m, float sg) {
      const float xa = __builtin_amdgcn_exp2f(ma * m), fa = __builtin_fmaf(la, m, __builtin_amdgcn_fractf(ha * m));
      return f32x2{xa * __builtin_amdgcn_cosf(fa), sg * xa * __builtin_amdgcn_sinf(fa)};
    };
    const CW w1 = mkw(seed(1.f, 1.f)), w8 = mkw(seed(8.f, -1.f)), w128 = mkw(seed(128.f, 1.f));
    unsigned sw = lb + KS + w * SCW + lane * 4; asm volatile("" : "+v"(sw));
    auto put = [&](int i, f32x2 z) { *(volatile AS3 unsigned*)(sw + i * SCR) = pk(z.x, z.y); };
    f32x2 c[17], z = w8.w;
    c[0] = f32x2{lo16(pr), lo16(pc)} * (2.f * dt);
    put(0, f32x2{1.f, 0.f}); put(1, z); put(16, c[0]);
    const CW w2 = mkw(cmul(w1.w, w1)); c[1] = cmul(c[0], w128); put(24, c[1]);
    const CW w4 = mkw(cmul(w2.w, w2)); c[2] = cmul(c[1], w128); put(32, c[2]);
#pragma unroll
    for (int i = 2; i < 16; ++i) {
      z = cmul(z, w8); c[i + 1] = cmul(c[i], w128);
      put(i, z); put(i == 2 ? 36 : i < 7 ? 34 + 2 * i : 41 + i, c[i + 1]);
    }
    f32x2 y0 = c[0], y1 = c[1];
#pragma unroll
    for (int f = 1; f < 8; ++f) { y0 = cmul(y0, w1); y1 = cmul(y1, w1); put(16 + f, y0); put(24 + f, y1); }
    y0 = c[2]; y1 = c[3];
#pragma unroll
    for (int f = 1; f < 4; ++f) { y0 = cmul(y0, w2); y1 = cmul(y1, w2); put(32 + f, y0); put(36 + f, y1); }
#pragma unroll
    for (int i = 4; i < 8; i += 2) { y0 = cmul(c[i], w4); y1 = cmul(c[i + 1], w4); put(33 + 2 * i, y0); put(35 + 2 * i, y1); }
    asm volatile("" ::: "memory");
    ST(43);
    unsigned sr = lb + KS + w * SCW + (lane & 15) * SCR + (lane >> 4) * 16; asm volatile("" : "+v"(sr));
    f32x4 ka[3] = {{0.f, 0.f, 0.f, 0.f}, {0.f, 0.f, 0.f, 0.f}, {0.f, 0.f, 0.f, 0.f}};
#pragma unroll
    for (int s = 0; s < 4; ++s) {
      const v4u pf = ld128(sr + s * 64);
#pragma unroll
      for (int k = 0; k < 3; ++k) ka[k] = mma16(pf, ld128(sr + (k + 1) * 16 * SCR + s * 64), ka[k]);
    }
#pragma unroll
    for (int k = 0; k < 3; ++k) st128(lb + KD + w * 3072 + k * 1024 + lane * 16, __builtin_bit_cast(v4u, ka[k]));
  }
  ST(44);
  const int wn = w & 1, wm = w >> 1;
  f32x16 acc[3][2];
#pragma unroll
  for (int j = 0; j < 3; ++j)
#pragma unroll
    for (int t = 0; t < 2; ++t) {
#pragma unroll
      for (int e = 0; e < 16; ++e) acc[j][t][e] = 0.f;
      asm volatile("" : "+v"(acc[j][t]));
    }
  {
    const int tok = lane & 31, h = lane >> 5;
    unsigned wb = lb + (((lane >> 4) & 1) ? UO : GO) + 192 * (8 * h + ((lane >> 2) & 3)) + 32 * wn + 8 * (lane & 3);
    unsigned xb[4];
#pragma unroll
    for (int s = 0; s < 4; ++s) xb[s] = lb + (64 * wm + tok) * 128 + (((2 * s + h) ^ ((tok >> 1) & 7)) << 4);
    v4u A[2][3], B[2][2];
    auto rd = [&](int buf, int s) {
#pragma unroll
      for (int t = 0; t < 2; ++t) B[buf][t] = ld128(xb[s] + 4096 * t);
#pragma unroll
      for (int j = 0; j < 3; ++j) A[buf][j] = trd2(wb + 3072 * s + 64 * j, wb + 3072 * s + 768 + 64 * j);
    };
    auto mm = [&](int buf) {
#pragma unroll
      for (int j = 0; j < 3; ++j)
#pragma unroll
        for (int t = 0; t < 2; ++t) acc[j][t] = mma32(A[buf][j], B[buf][t], acc[j][t]);
#pragma unroll
      for (int i = 0; i < 4; ++i) { __builtin_amdgcn_sched_group_barrier(0x8, 1, 0); __builtin_amdgcn_sched_group_barrier(0x100, 2, 0); }
      __builtin_amdgcn_sched_group_barrier(0x8, 2, 0);
      fence();
    };
    fence(); waitl(); BAR(0);
    rd(0, 0); fence();
    int stg = 0;
#pragma unroll 1
    for (int t = 0; t < 8; ++t) {
      rd(1, 1); mm(0); rd(0, 2); mm(1); rd(1, 3); mm(0);
      const int dl = stg == 2 ? -2 * STG : STG; stg = stg == 2 ? 0 : stg + 1;
      wb += dl;
#pragma unroll
      for (int s = 0; s < 4; ++s) xb[s] += dl;
      fence(); waitl(); BAR(t + 1); fence();
      rd(0, 0); mm(1);
    }
  }
  ST(32);
  if (ct < 15) {
#pragma unroll
    for (int j = 0; j < 3; ++j) {
      const int unit = ct * 6 + 2 * j + wn;
      if (unit < NU) {
#pragma unroll
        for (int t = 0; t < 2; ++t) {
          v4u hv;
#pragma unroll
          for (int e = 0; e < 4; ++e) {
            const f32x2 g = {acc[j][t][2 * e], acc[j][t][2 * e + 1]}, u = {acc[j][t][2 * e + 8], acc[j][t][2 * e + 9]};
            const f32x2 z = g * __builtin_elementwise_fma(g * g, f32x2{-0.1029432397f, -0.1029432397f}, f32x2{-2.3022081986f, -2.3022081986f});
            const f32x2 dn = f32x2{__builtin_amdgcn_exp2f(z.x), __builtin_amdgcn_exp2f(z.y)} + 1.f;
            const f32x2 hh = g * u * f32x2{__builtin_amdgcn_rcpf(dn.x), __builtin_amdgcn_rcpf(dn.y)};
            hv[e] = nm(pk(hh.x, hh.y));
          }
          stws(hv, rws, lane * 16, HB + ((rp * 4 + wm * 2 + t) * NU + unit) * 1024);
          fence();
        }
      }
    }
  } else if (wn == 0) {
#pragma unroll
    for (int t = 0; t < 2; ++t) {
      const float s = rbf(acc[0][t][0] + acc[0][t][9]);
      const float g = rbf(__builtin_amdgcn_rcpf(1.f + __builtin_amdgcn_exp2f(s * -1.44269504088896340736f)));
      if (lane < 32) __builtin_amdgcn_raw_buffer_store_b32(__float_as_uint(g), rws, lane * 4, GV + (rp * 128 + wm * 64 + t * 32) * 4, 16);
    }
  }
  ST(33);
  const int kh = wn, tt = wm;
  const unsigned wl = kh * 2048 + (4 * (lane >> 5) + ((lane >> 2) & 3)) * 128 + ((((lane >> 3) & 1) ^ kh) * 64) + ((lane >> 4) & 1) * 32 + (lane & 1) * 16 + ((lane >> 1) & 1) * 8;
  unsigned wa[3][2];
#pragma unroll
  for (int i = 0; i < 3; ++i) { wa[i][0] = lb + wl + i * 61440; wa[i][1] = lb + (wl ^ 64) + i * 61440; asm volatile("" : "+v"(wa[i][0]), "+v"(wa[i][1])); }
  auto wfrag = [&](int s, int c) { const int o = wslot_lds(s), i = o / 61440; return trd2(wa[i][c] + (o - i * 61440), wa[i][c] + (o - i * 61440) + 1024); };
  v4u wr[7][2];
  lds_wait(lb + CTL, 1);
#pragma unroll
  for (int s = 0; s < 7; ++s) { wr[s][0] = wfrag(s, 0); wr[s][1] = wfrag(s, 1); }
  waitl(); lds_inc(lb + CTL + 4, lane);
  ST(34);
  constexpr int WIN = 10;
  const int hA = HB + ((rt * 2 + tt) * NU + kh) * 1024;
  v4u hf[WIN];
  auto hload = [&](int s) { hf[s % WIN] = __builtin_amdgcn_raw_buffer_load_b128(rws, lane * 16, hA + wslot_q(s) * 2048, 16); };
#pragma unroll
  for (int s = 0; s < WIN; ++s) hload(s);
  fence();
  f32x16 o0, o1;
#pragma unroll
  for (int e = 0; e < 16; ++e) { o0[e] = 0.f; o1[e] = 0.f; }
  v4u wf[3][2];
  unsigned gu = 0;
#pragma unroll
  for (int s = 0; s < 43; ++s) {
    if (s == 5) lds_wait(lb + CTL + 8, 4);
    if (s == 20) lds_inc(lb + CTL + 12, lane);
    if (s == 38) lds_wait(lb + CTL + 12, 4);
    const v4u hb = settle(hf[s % WIN], rws, lane * 16, hA + wslot_q(s) * 2048);
    o1 = mma32(s < 7 ? wr[s < 7 ? s : 0][1] : wf[s % 3][1], hb, o1);
    if (s >= 5 && s < 41) { wf[(s + 2) % 3][1] = wfrag(s + 2, 1); wf[(s + 2) % 3][0] = wfrag(s + 2, 0); }
    fence();
    o0 = mma32(s < 7 ? wr[s < 7 ? s : 0][0] : wf[s % 3][0], hb, o0);
    if (s + WIN < 43) hload(s + WIN);
    if (s + WIN == 43) gu = __builtin_amdgcn_raw_buffer_load_b32(rws, (lane & 31) * 4, GV + (rt * 64 + tt * 32) * 4, 16);
    fence();
  }
  char* op = (char*)out + (rt * 64 + tt * 32 + (lane & 31)) * 1024 + c2 * 128 + kh * 64 + (lane >> 5) * 16;
  const unsigned ex = lb + 2 * STG + lane * 16;
#pragma unroll
  for (int m = 0; m < 4; ++m)
    st128(ex + w * 4096 + m * 1024, v4u{__float_as_uint(o1[4 * m]), __float_as_uint(o1[4 * m + 1]), __float_as_uint(o1[4 * m + 2]), __float_as_uint(o1[4 * m + 3])});
  if (__builtin_expect(__any(gu == 0xffffffffu), 0)) {
    int i = 0;
#pragma unroll 1
    do { asm volatile("" ::: "memory"); gu = __builtin_amdgcn_raw_buffer_load_b32(rws, (lane & 31) * 4, GV + (rt * 64 + tt * 32) * 4, 16); } while (__any(gu == 0xffffffffu) && ++i < SPIN);
  }
  const float gt = __uint_as_float(gu), gb = rbf(1.f - gt);
  const unsigned a0 = lb + UB + (32 * kh + 8 * (lane >> 5) + ((lane >> 2) & 3)) * 128 + ((lane >> 1) & 1) * 64 + (2 * tt + ((lane >> 4) & 1)) * 16 + (lane & 1) * 8;
  fence(); waitl(); BAR(9);
  v2u at[4]; v4u pp[4];
#pragma unroll
  for (int m = 0; m < 4; ++m) { pp[m] = ld128(ex + (w ^ 1) * 4096 + m * 1024); at[m] = trd(a0 + (16 * (m >> 1) + 4 * (m & 1)) * 128); }
  fence();
  unsigned ov[8];
#pragma unroll
  for (int m = 0; m < 4; ++m) {
    const float r0 = gt * lo16(at[m].x) + gb * (o0[4 * m] + __uint_as_float(pp[m].x)), r1 = gt * hi16(at[m].x) + gb * (o0[4 * m + 1] + __uint_as_float(pp[m].y));
    const float r2 = gt * lo16(at[m].y) + gb * (o0[4 * m + 2] + __uint_as_float(pp[m].z)), r3 = gt * hi16(at[m].y) + gb * (o0[4 * m + 3] + __uint_as_float(pp[m].w));
    ov[2 * m] = pk(r0, r1); ov[2 * m + 1] = pk(r2, r3);
    if (m & 1) stws(v4u{ov[2 * m - 2], ov[2 * m - 1], ov[2 * m], ov[2 * m + 1]}, mk(out, L * D * 2), (int)(op - (char*)out) + 16 * (m - 1), 0);
  }
  ST(36); STW();
}

__global__ void k_tiny(unsigned* p){ if(threadIdx.x==0) p[0]=(unsigned)__builtin_amdgcn_s_memrealtime(); }
__global__ void k_spin(const unsigned* st, const unsigned* pre){
  unsigned now=(unsigned)__builtin_amdgcn_s_memrealtime();
  __shared__ unsigned sm[2][256];
  int b=threadIdx.x;
  unsigned s0=st[(b*8)*64+62], e=0; for(int w=0;w<8;w++) e=max(e,st[(b*8+w)*64+63]);
  sm[0][b]=s0; sm[1][b]=e;
  __syncthreads();
  if(b==0){ unsigned mn=~0u,mxe=0;
    for(int i=0;i<256;i++){ mn=min(mn,sm[0][i]); mxe=max(mxe,sm[1][i]); }
    unsigned long long m = PM==0 ? (mn-pre[0])*20ull : PM==1 ? (now-mxe)*20ull : ((mn^mxe)==0x12345u);
    if(m>200000) m=200000;
    unsigned long long t=__builtin_amdgcn_s_memrealtime();
    while(__builtin_amdgcn_s_memrealtime()-t<m) __builtin_amdgcn_s_sleep(2);
  }
}
extern "C" void kernel_launch(void* const* d_in, const int* in_sizes, int n_in, void* d_out, int out_size, void* d_ws, size_t ws_size,
                              hipStream_t stream) {
  gated_mix_fused<<<256, 512, 0, stream>>>((const u16*)d_in[0], (const u16*)d_in[8], (const u16*)d_in[9], (const u16*)d_in[11],
                                           (const u16*)d_in[10], (char*)d_ws, (const u16*)d_in[1], (const u16*)d_in[2], (const u16*)d_in[3],
                                           (const u16*)d_in[4], (const u16*)d_in[5], (const u16*)d_in[6], (u16*)d_out);
}
```

```cpp
#include <hip/hip_runtime.h>
#include <stdint.h>
#include <type_traits>

typedef unsigned short u16;
typedef unsigned long long u64;
typedef unsigned v4u __attribute__((ext_vector_type(4)));
typedef unsigned v2u __attribute__((ext_vector_type(2)));
typedef unsigned v8u __attribute__((ext_vector_type(8)));
typedef unsigned v16u __attribute__((ext_vector_type(16)));
typedef short s16x4 __attribute__((ext_vector_type(4)));
typedef short s16x8 __attribute__((ext_vector_type(8)));
typedef __bf16 bf16x8 __attribute__((ext_vector_type(8)));
typedef __bf16 bf16x2 __attribute__((ext_vector_type(2)));
typedef float f32x2 __attribute__((ext_vector_type(2)));
typedef float f32x4 __attribute__((ext_vector_type(4)));
typedef float f32x16 __attribute__((ext_vector_type(16)));
typedef __amdgpu_buffer_rsrc_t rsrc_t;
#define AS1 __attribute__((address_space(1)))
#define AS3 __attribute__((address_space(3)))
#define DI __device__ __forceinline__

constexpr int L = 2048, D = 512, F = 1376, NU = 86;
constexpr int STG = 41024, GO = 16384, UO = 16384 + 12288 + 32;
constexpr int R3 = 3 * STG;
constexpr int SCR = 288, SCW = 42 * SCR, KS = 2 * STG, KSI = 53248, KD = 131072;
constexpr int UB = R3, UCH = 4608;
constexpr int KXS = 2240, KXC = 4 * KXS, KX = UB + 2 * UCH;
constexpr int KT = KX + 2 * KXC, TBL = 1312, W2 = KT + 2 * TBL;
constexpr int CTL = 163840 - 64, DUM = CTL - 256;
static_assert(KS + 4 * SCW <= KD && KD + 4 * 3072 <= KT && W2 + 8192 <= DUM && KS + 3 * SCW - KSI < 65536 && KSI + 41 * SCR < 65536, "lds map");
constexpr int NREG = 11;
constexpr int FL_U = 4096, FL_H = FL_U + 32768, FL_A = FL_H + 32768, GV = FL_A + 32768;
constexpr int UT = 131072, AT = UT + L * D * 2, HB = AT + 2 * L * D * 2, WS_END = HB + L * F * 2;
constexpr int SPIN = 1 << 16;
constexpr int CODE_LINES = 160;
#define THR 10
#define CPJ 3
#define WINN 7

template <int N> DI void waitv() { asm volatile("s_waitcnt vmcnt(%0)" ::"n"(N) : "memory"); }
DI void waitl() { asm volatile("s_waitcnt lgkmcnt(0)" ::: "memory"); }
DI void bar() { asm volatile("s_barrier" ::: "memory"); }
DI void fence() { __builtin_amdgcn_sched_barrier(0); }
DI rsrc_t mk(const void* p, int bytes) { return __builtin_amdgcn_make_buffer_rsrc((void*)p, 0, bytes, 0x00020000); }
DI unsigned pk(float a, float b) { f32x2 v = {a, b}; return __builtin_bit_cast(unsigned, __builtin_convertvector(v, bf16x2)); }
DI float lo16(unsigned v) { return __uint_as_float(v << 16); }
DI float hi16(unsigned v) { return __uint_as_float(v & 0xffff0000u); }
DI float rbf(float a) { return lo16(pk(a, a)); }
DI v2u trd(unsigned a) { return __builtin_bit_cast(v2u, __builtin_amdgcn_ds_read_tr16_b64_v4i16((AS3 s16x4*)a)); }
DI v4u trd2(unsigned a, unsigned b) { v2u x = trd(a), y = trd(b); return v4u{x.x, x.y, y.x, y.y}; }
DI v4u ld128(unsigned a) { return *(AS3 v4u*)a; }
DI v2u ld64(unsigned a) { return *(AS3 v2u*)a; }
DI void st128(unsigned a, v4u v) { *(AS3 v4u*)a = v; }
DI f32x16 mma32(v4u a, v4u b, f32x16 c) {
  return __builtin_amdgcn_mfma_f32_32x32x16_bf16(__builtin_bit_cast(bf16x8, a), __builtin_bit_cast(bf16x8, b), c, 0, 0, 0);
}
DI f32x4 mma16(v4u a, v4u b, f32x4 c) {
  return __builtin_amdgcn_mfma_f32_16x16x32_bf16(__builtin_bit_cast(bf16x8, a), __builtin_bit_cast(bf16x8, b), c, 0, 0, 0);
}
DI void dma1(unsigned lds, int v0, rsrc_t rs, int so) {
  asm volatile("s_mov_b32 m0, %0\n\ts_nop 0\n\tbuffer_load_dwordx4 %1, %2, %3 offen lds" ::"s"(lds), "v"(v0), "s"(rs), "s"(so) : "memory");
}
DI void dma2(unsigned lds, int v0, int v1, rsrc_t rs, int so) {
  asm volatile("s_mov_b32 m0, %0\n\ts_nop 0\n\tbuffer_load_dwordx4 %1, %3, %4 offen lds\n\tbuffer_load_dwordx4 %2, %3, %4 offen offset:1024 lds"
               ::"s"(lds), "v"(v0), "v"(v1), "s"(rs), "s"(so) : "memory");
}
DI void dma4(unsigned lds, const int* v, rsrc_t rs, int so) {
  asm volatile("s_mov_b32 m0, %0\n\ts_nop 0\n\tbuffer_load_dwordx4 %1, %5, %6 offen lds\n\t"
               "buffer_load_dwordx4 %2, %5, %6 offen offset:1024 lds\n\tbuffer_load_dwordx4 %3, %5, %6 offen offset:2048 lds\n\t"
               "buffer_load_dwordx4 %4, %5, %6 offen offset:3072 lds"
               ::"s"(lds), "v"(v[0]), "v"(v[1]), "v"(v[2]), "v"(v[3]), "s"(rs), "s"(so) : "memory");
}
#define DMA_I(v, rs, so, off) "buffer_load_dwordx4 " v ", " rs ", " so " offen offset:" off " lds\n\t"
#define DMA_X4 DMA_I("%3", "%7", "%0", "0") DMA_I("%4", "%7", "%0", "1024") DMA_I("%5", "%7", "%0", "2048") DMA_I("%6", "%7", "%0", "3072")
#define DMA_XN "s_add_u32 m0, m0, 0x1000\n\ts_add_u32 %0, %0, 0x8000\n\t"
DI void tile_x(int w, unsigned lds, const int* v, rsrc_t rs, int so) {
  asm volatile("s_mov_b32 m0, %2\n\ts_cmp_lg_u32 %1, 4\n\ts_cbranch_scc1 1f\n\t" DMA_X4 DMA_XN DMA_X4 DMA_XN DMA_X4 DMA_XN DMA_X4 "1:"
               : "+&s"(so) : "s"(w), "s"(lds), "v"(v[0]), "v"(v[1]), "v"(v[2]), "v"(v[3]), "s"(rs) : "memory", "scc");
}
#define DMA_W6 DMA_I("%3", "%9", "%0", "0") DMA_I("%4", "%9", "%0", "1024") DMA_I("%5", "%9", "%0", "2048") "s_add_u32 m0, m0, %12\n\ts_nop 0\n\t" \
               DMA_I("%6", "%10", "%0", "0") DMA_I("%7", "%10", "%0", "1024") DMA_I("%8", "%10", "%0", "2048")
#define DMA_WN "s_sub_u32 m0, m0, %13\n\ts_add_u32 %0, %0, %11\n\t"
DI void tile_w(int w, unsigned lds, const int* vg, const int* vu, rsrc_t rg, rsrc_t ru, int so, int step) {
  asm volatile("s_mov_b32 m0, %2\n\ts_cmp_lg_u32 %1, 4\n\ts_cbranch_scc1 1f\n\t" DMA_W6 DMA_WN DMA_W6 DMA_WN DMA_W6 DMA_WN DMA_W6 "1:"
               : "+&s"(so) : "s"(w), "s"(lds), "v"(vg[0]), "v"(vg[1]), "v"(vg[2]), "v"(vu[0]), "v"(vu[1]), "v"(vu[2]), "s"(rg), "s"(ru), "s"(step),
                 "n"(UO - GO), "n"(UO - GO - 3072) : "memory", "scc");
}
DI void touch(int w, unsigned lds, int v, rsrc_t rs, int so) {
  asm volatile("s_mov_b32 m0, %1\n\ts_cmp_lg_u32 %0, 5\n\ts_cbranch_scc1 1f\n\tbuffer_load_dword %2, %3, %4 offen lds\n1:"
               ::"s"(w), "s"(lds), "v"(v), "s"(rs), "s"(so) : "memory", "scc");
}
DI void stws(v4u v, rsrc_t rs, int vo, int so) {
  __builtin_amdgcn_raw_buffer_store_b128(v, rs, vo, so, 16);
  asm volatile("s_nop 1" ::"v"(v) : "memory");
}
DI void set_flag(char* ws, int off, u64 ep) { __hip_atomic_store((AS1 u64*)(ws + off), ep, __ATOMIC_RELAXED, __HIP_MEMORY_SCOPE_AGENT); }
DI void poll(char* ws, int off, u64 ep) {
  AS1 u64* p = (AS1 u64*)(ws + off);
#pragma unroll 1
  for (int i = 0; i < SPIN; ++i) {
    u64 f = __hip_atomic_load(p, __ATOMIC_RELAXED, __HIP_MEMORY_SCOPE_AGENT);
    if (__all(f == ep)) break;
    __builtin_amdgcn_s_sleep(1);
  }
  __builtin_amdgcn_fence(__ATOMIC_ACQUIRE, "wavefront");
}
DI void lds_wait(unsigned a, unsigned want) {
#pragma unroll 1
  for (int i = 0; i < SPIN * 8; ++i) {
    unsigned f = __hip_atomic_load((AS3 unsigned*)a, __ATOMIC_RELAXED, __HIP_MEMORY_SCOPE_WORKGROUP);
    if (__builtin_amdgcn_readfirstlane(f) >= want) break;
    __builtin_amdgcn_s_sleep(1);
  }
  asm volatile("" ::: "memory");
}
DI void lds_inc(unsigned a, int lane) {
  if (lane == 0) __hip_atomic_fetch_add((AS3 unsigned*)a, 1u, __ATOMIC_RELAXED, __HIP_MEMORY_SCOPE_WORKGROUP);
}
DI bool lds_last(unsigned a, int lane) {
  unsigned o = 0;
  if (lane == 0) o = __hip_atomic_fetch_add((AS3 unsigned*)a, 1u, __ATOMIC_RELAXED, __HIP_MEMORY_SCOPE_WORKGROUP);
  return __builtin_amdgcn_readfirstlane(o) == 3;
}
DI float exp_acc(float x) {
  const float H = 1.44269502162933349609375f, Lo = 1.925963033500011079e-8f;
  float t = x * H, e = __builtin_fmaf(x, Lo, __builtin_fmaf(x, H, -t));
  float r = __builtin_amdgcn_exp2f(t);
  return __builtin_fmaf(r, e * 0.693147182464599609375f, r);
}
template <int RA, int RB> DI void cstep(f32x2& za, f32x2 wa, f32x2& zb, f32x2 wb) {
  f32x2 ta, tb; unsigned ca, cb;
  asm volatile("v_pk_mul_f32 %0, %4, %5 op_sel_hi:[0,1]\n\tv_pk_mul_f32 %1, %6, %7 op_sel_hi:[0,1]\n\t"
               "v_cvt_pk_bf16_f32 %2, %8, %9\n\tv_cvt_pk_bf16_f32 %3, %10, %11\n\t"
               "v_pk_fma_f32 %0, %4, %5, %0 op_sel:[1,1,0] op_sel_hi:[1,0,1] neg_lo:[1,0,0]\n\tds_write_addtid_b32 %2 offset:%12\n\t"
               "v_pk_fma_f32 %1, %6, %7, %1 op_sel:[1,1,0] op_sel_hi:[1,0,1] neg_lo:[1,0,0]\n\tds_write_addtid_b32 %3 offset:%13"
               : "=&v"(ta), "=&v"(tb), "=&v"(ca), "=&v"(cb)
               : "v"(za), "v"(wa), "v"(zb), "v"(wb), "v"(za.x), "v"(za.y), "v"(zb.x), "v"(zb.y), "n"(KSI + RA * SCR), "n"(KSI + RB * SCR) : "memory");
  za = ta; zb = tb;
}
template <int RA, int RB> DI void cput(f32x2 za, f32x2 zb) {
  asm volatile("ds_write_addtid_b32 %0 offset:%2\n\tds_write_addtid_b32 %1 offset:%3"
               ::"v"(pk(za.x, za.y)), "v"(pk(zb.x, zb.y)), "n"(KSI + RA * SCR), "n"(KSI + RB * SCR) : "memory");
}
DI f32x2 cmul(f32x2 x, f32x2 y) { return __builtin_elementwise_fma(x.yy, f32x2{-y.y, y.x}, x.xx * y); }
template <int I, int N, class Fn> DI void sfor(Fn f) { if constexpr (I < N) { f(std::integral_constant<int, I>{}); sfor<I + 1, N>(f); } }
constexpr int wslot_lds(int s) {
  return s < 10 ? 2 * STG + 4096 * s : s < 12 ? W2 + 4096 * (s - 10) : s < 22 ? 4096 * (s - 12) : s < 32 ? STG + 4096 * (s - 22) : s < 42 ? 2 * STG + 4096 * (s - 32) : W2;
}
constexpr int wslot_q(int s) { return s < 15 ? 3 * s : s < 29 ? 3 * (s - 15) + 1 : 3 * (s - 29) + 2; }

#define STW()
#define ST(i)
#define BAR(i) bar()

DI char* ptr(unsigned lo, unsigned hi) { return (char*)(((u64)hi << 32) | lo); }

__global__ __launch_bounds__(512, 2) void gated_mix_fused(
    const void* a_x, const void* a_wg, const void* a_wu, const void* a_gw, const void* a_logA, const void* a_Aim, const void* a_Cr,
    const void* a_Ci, const void* a_logdt, const void* a_wd, void* a_ws, const void* a_dskip, void* a_out) {
  __shared__ __attribute__((aligned(16))) char smem[163840];
  v16u ka; v8u kb; v2u kc; u64 pc0;
  asm volatile("s_load_dwordx16 %0, %4, 0x0\n\ts_load_dwordx8 %1, %4, 0x40\n\ts_load_dwordx2 %2, %4, 0x60\n\ts_getpc_b64 %3"
               : "=&s"(ka), "=&s"(kb), "=&s"(kc), "=&s"(pc0) : "s"(__builtin_amdgcn_kernarg_segment_ptr()));
  fence();
  const unsigned lb = (unsigned)(size_t)(AS3 char*)smem;
  const int b = blockIdx.x, xcd = b & 7, r = b >> 3;
  const int w = __builtin_amdgcn_readfirstlane(threadIdx.x >> 6), lane = threadIdx.x & 63;
  touch(w, lb + DUM, (lane * 32 + r) * 128, mk((const void*)(pc0 & ~255ull), CODE_LINES * 128), 0);
  fence();
  const int pi_ = r & 7, pj = r >> 3, xc = xcd >> 1, rp = (xcd & 1) * 8 + pi_;
  int k0 = (2 * pi_ + 4 * pj + xc + (xcd & 1)) & 7;
  const int cta = 7 * (xc >> 1) + 4 * (xc & 1) + pj, ctb = 7 * (xc >> 1) + 3, ctc = 14 + ((xc >> 1) ^ (pi_ & 1));
  const int ctd = (pi_ >> 2) == (xc & 1) ? ctb : ctc;
  int ct = pj < 3 ? cta : ctd;
  int vx[4], vg[3], vu[3];
  vx[0] = (rp * 128 + (lane >> 3)) * 1024 + (((lane & 7) ^ (lane >> 4)) * 16);
  vx[1] = (vx[0] ^ 64) + 7168; vx[2] = vx[0] + 2 * 7168; vx[3] = vx[1] + 2 * 7168;
#pragma unroll
  for (int i = 0; i < 3; ++i) vg[i] = (((i * 64 + lane) * 171) >> 11) * (F * 2 - 192) + (lane * 16 + ct * 192);
  int vp = (b * 256 + (int)threadIdx.x) * 2, vt = (2 * b + ((w >> 1) & 1)) * 2;
  asm volatile("s_waitcnt lgkmcnt(0)" : "+s"(ka), "+s"(kb), "+s"(kc), "+s"(k0), "+s"(ct), "+v"(vx[0]), "+v"(vx[1]), "+v"(vx[2]), "+v"(vx[3]),
               "+v"(vg[0]), "+v"(vg[1]), "+v"(vg[2]), "+v"(vp), "+v"(vt));
  char* const ws = ptr(kb[4], kb[5]);
  unsigned pa, pi, pr, pc, pt;
  asm volatile("s_cmp_gt_u32 %12, 3\n\ts_cbranch_scc1 1f\n\tglobal_load_ushort %0, %6, %11\n\tglobal_load_ushort %1, %5, %7\n\tglobal_load_ushort %2, %5, %8\n\t"
               "global_load_ushort %3, %5, %9\n\tglobal_load_ushort %4, %5, %10\n1:"
               : "=&v"(pt), "=&v"(pa), "=&v"(pi), "=&v"(pr), "=&v"(pc)
               : "v"(vp), "v"(vt), "s"(ptr(ka[8], ka[9])), "s"(ptr(ka[10], ka[11])), "s"(ptr(ka[12], ka[13])), "s"(ptr(ka[14], ka[15])), "s"(ptr(kb[0], kb[1])), "s"(w)
               : "memory", "scc");
  const rsrc_t rx = mk(ptr(ka[0], ka[1]), L * D * 2);
  auto ktile = [&](int t) { return ((0x73516240 >> (4 * t)) + k0) & 7; };
  auto load_x = [&](int t) { tile_x(w, lb + (t % 3) * STG, vx, rx, ktile(t) * 128); };
  load_x(0);
  rsrc_t rg = mk(ptr(ka[2], ka[3]), D * F * 2), ru = mk(ptr(ka[4], ka[5]), D * F * 2);
  int wrow = F * 2;
#pragma unroll
  for (int i = 0; i < 3; ++i) vu[i] = vg[i];
  if (__builtin_expect(ct >= 14, 0)) {
    const int sp = ct & 1;
    if (sp) { rg = ru = mk(ptr(ka[6], ka[7]) - 2048, 2048 + D * 2); wrow = 2; }
#pragma unroll
    for (int i = 0; i < 3; ++i) {
      const int q = i * 64 + lane, row = (q * 171) >> 11, c = q - row * 12, odd = row & sp;
      const int o = sp ? (row - odd) * 2 + 2048 - i * 1024 : vg[i];
      vg[i] = c + odd < (sp ? 1 : 4) ? o : 0x40000000;
      vu[i] = c + (odd ^ sp) < (sp ? 1 : 4) ? o : 0x40000000;
    }
  }
  auto load_w = [&](int t) { tile_w(w, lb + (t % 3) * STG + GO, vg, vu, rg, ru, ktile(t) * 64 * wrow, 16 * wrow); };
  load_w(0);
  fence();
  ST(40);
  const int rt = xcd * 4 + (r & 3), c2 = r >> 2;
  const rsrc_t rws = mk(ws, WS_END), rd = mk(ptr(kb[2], kb[3]), F * D * 2);
  u64 ep = *(const __attribute__((address_space(4))) u64*)ws;

  if (w >= 4) {
    const int aw = w - 4;
    const int vd = (lane >> 3) * 1024 + (((lane & 7) ^ (((lane >> 4) & 1) * 4)) * 16) + c2 * 128;
    const int ch = aw >> 1, rh = aw & 1;
    if (aw < 2) {
      if (aw == 0) {
#pragma unroll 1
        for (int t = 0; t < 7; ++t) { load_x(t + 1); load_w(t + 1); asm volatile("s_waitcnt vmcnt(40)" : "+s"(ep) :: "memory"); BAR(t); }
        int v4[4];
#pragma unroll
        for (int p = 0; p < 4; ++p) v4[p] = vd + p * 7168;
#pragma unroll
        for (int s = 0; s < 8; ++s) dma4(lb + wslot_lds(s), v4, rd, wslot_q(s) * 32768);
        waitv<32>(); BAR(7);
#pragma unroll
        for (int s = 8; s < 14; ++s) dma4(lb + wslot_lds(s), v4, rd, wslot_q(s) * 32768);
        BAR(8);
      } else {
        if (lane < 16) *(AS3 unsigned*)(lb + CTL + lane * 4) = 0;
        if (ct == 15) {
#pragma unroll 1
          for (int i = 0; i < 11; ++i) touch(w, lb + DUM, lane * 128, rd, rp * 88064 + i * 8192);
        }
        const int half = pj >> 1, G = lane >> 4, q = (lane >> 2) & 3, p = lane & 3;
        asm volatile("" : "+s"(ep));
        waitl(); BAR(0);
        v4u tv[4][2];
#pragma unroll
        for (int j = 0; j < 4; ++j)
#pragma unroll
          for (int s = 0; s < 2; ++s) {
            v2u h2[2];
#pragma unroll
            for (int t = 0; t < 2; ++t) {
              const int row = 64 * (G & 1) + 16 * p + 8 * s + 4 * t + q;
              h2[t] = trd(lb + row * 128 + (((4 * half + j) ^ ((row >> 1) & 7)) << 4) + 8 * (G >> 1));
            }
            tv[j][s] = v4u{h2[0].x, h2[0].y, h2[1].x, h2[1].y};
          }
        waitl(); BAR(1);
        const int vo = (32 * k0 + 16 * half + 2 * (G >> 1)) * 8192 + rp * 512 + (lane & 31) * 16;
        const bool lo = (lane & 2) == 0;
#pragma unroll
        for (int j = 0; j < 4; ++j) {
          v4u n0, n1;
#pragma unroll
          for (int e = 0; e < 4; ++e) {
            const unsigned a = tv[j][0][e], c = tv[j][1][e];
            const unsigned as = __builtin_amdgcn_update_dpp(0u, a, 0x4E, 0xf, 0xf, true), cs = __builtin_amdgcn_update_dpp(0u, c, 0x4E, 0xf, 0xf, true);
            n0[e] = lo ? a : cs; n1[e] = lo ? as : c;
          }
          stws(n0, rws, vo, UT + j * 32768);
          stws(n1, rws, vo, UT + j * 32768 + 8192);
          if (j == 1) BAR(2);
        }
        BAR(3);
        waitv<0>();
        if (lane == 0) set_flag(ws, FL_U + ((2 * k0 + half) * 16 + rp) * 128, ep + 1);
        BAR(4); BAR(5); BAR(6);
        AS1 u64* fp = (AS1 u64*)(ws + FL_U + ((b >> 4) * 16 + (lane & 15)) * 128);
        const u64 f0 = __hip_atomic_load(fp, __ATOMIC_RELAXED, __HIP_MEMORY_SCOPE_AGENT);
        st128(lb + UB + (lane >> 5) * UCH + (lane & 31) * 16, v4u{0u, 0u, 0u, 0u});
        fence(); BAR(7); fence();
        if (!__all(f0 == ep + 1)) poll(ws, FL_U + ((b >> 4) * 16 + (lane & 15)) * 128, ep + 1);
        __builtin_amdgcn_fence(__ATOMIC_ACQUIRE, "wavefront");
        v4u uv[8];
#pragma unroll
        for (int i = 0; i < 8; ++i) uv[i] = __builtin_amdgcn_raw_buffer_load_b128(rws, lane * 16, UT + b * 8192 + i * 1024, 16);
        fence(); BAR(8); fence();
#pragma unroll
        for (int i = 0; i < 8; ++i) st128(lb + UB + (lane & 1) * UCH + 512 + (lane >> 1) * 16 + i * 512, uv[i]);
        waitl(); lds_inc(lb + CTL + 4, lane);
      }
      const int v0 = vd + aw * 16384, v1 = v0 + 7168;
#pragma unroll
      for (int s = 14; s < 32; ++s) {
        dma2(lb + wslot_lds(s) + aw * 2048, v0, v1, rd, wslot_q(s) * 32768);
        waitv<THR>();
        if (s == 14 && aw == 0) lds_inc(lb + CTL, lane);
      }
    } else {
      const int kw = aw - 2;
      int d = 2 * b + kw; asm volatile("" : "+v"(d));
      const unsigned pd = ((const AS1 u16*)ptr(kb[6], kb[7]))[d];
      const unsigned tb0 = lb + KT + kw * TBL;
      asm volatile("" : "+s"(ep));
      unsigned ix[3][11];
      auto index = [&](int it) {
        const int top = 1055 - 512 * it - 8 * lane;
        float tf = (float)top; asm volatile("" : "+v"(tf));
#pragma unroll
        for (int i = 0; i < 12; i += 2) {
          float l0 = tf - i; asm volatile("" : "+v"(l0));
          const float l1 = tf - (i + 1);
          const unsigned p = it == 1 ? pk(__builtin_fmaxf(l0, 128.f), __builtin_fmaxf(l1, 128.f)) : pk(l0, l1);
          int i0 = (p & 0xffffu) - 0x427b, i1 = (p >> 16) - 0x427b;
          if (it == 0) { i0 = top - i > 1028 ? 4 : i0; i1 = top - i - 1 > 1028 ? 4 : i1; }
          if (it == 1) { i0 = min(i0, top - i + 5); i1 = min(i1, top - i + 4); }
          if (it == 2) { i0 = max(top - i + 5, 4); i1 = max(top - i + 4, 4); }
          ix[it][i] = tb0 + 2 * i0; asm volatile("" : "+v"(ix[it][i]));
          if (i < 10) { ix[it][i + 1] = tb0 + 2 * i1; asm volatile("" : "+v"(ix[it][i + 1])); }
        }
      };
      index(0);
      fence(); BAR(0); fence();
      unsigned kp[3][2];
      { v4u u[3], v[3];
#pragma unroll
        for (int k = 0; k < 3; ++k) { u[k] = ld128(lb + KD + kw * 6144 + k * 1024 + lane * 16); v[k] = ld128(lb + KD + kw * 6144 + 3072 + k * 1024 + lane * 16); }
        index(2);
#pragma unroll
        for (int k = 0; k < 3; ++k) {
          float f[4];
#pragma unroll
          for (int e = 0; e < 4; ++e) { f[e] = __uint_as_float(u[k][e]) + __uint_as_float(v[k][e]); asm volatile("" : "+v"(f[e])); }
          if (k == 0) f[0] += lane == 0 ? lo16(pd) : 0.f;
          kp[k][0] = pk(f[0], f[1]); kp[k][1] = pk(f[2], f[3]);
        }
      }
      fence(); BAR(1); fence();
      { const int col = lane & 15, g4 = (lane >> 4) * 4;
        const unsigned t0 = tb0 + 10 + 2 * (128 * (col >> 3) + (col & 7) + 8 * g4);
        const int s1 = col < 8 ? 4 : 2, b1 = (col < 8 ? 256 + 64 * (col >> 2) + (col & 3) : 384 + 32 * ((col - 8) >> 1) + (col & 1)) + s1 * g4;
        *(AS3 u16*)(tb0 + 8) = 0;
#pragma unroll
        for (int e = 0; e < 4; ++e) {
          *(AS3 u16*)(t0 + 16 * e) = (u16)(kp[0][e >> 1] >> (16 * (e & 1)));
          *(AS3 u16*)(tb0 + 10 + 2 * (b1 + s1 * e)) = (u16)(kp[1][e >> 1] >> (16 * (e & 1)));
          *(AS3 u16*)(tb0 + 10 + 2 * min(512 + 16 * col + g4 + e, 641)) = (u16)(kp[2][e >> 1] >> (16 * (e & 1)));
          if (e == 1) { fence(); BAR(2); fence(); }
        }
      }
      fence(); BAR(3); fence();
      index(1);
      unsigned q[3][10];
#pragma unroll
      for (int it = 0; it < 3; ++it) {
        fence(); BAR(4 + it); fence();
        unsigned v[11];
#pragma unroll
        for (int i = 0; i < 11; ++i) v[i] = *(AS3 u16*)ix[it][i];
        fence();
#pragma unroll
        for (int i = 0; i < 10; ++i) { q[it][i] = v[i] | (v[i + 1] << 16); asm volatile("" : "+v"(q[it][i])); }
      }
      fence();
      BAR(7); BAR(8);
#pragma unroll
      for (int it = 0; it < 3; ++it) {
        const unsigned o = lb + KX + kw * KXC + it * 1024 + lane * 16;
        if (it < 2 || lane < 6) {
          st128(o + 3 * KXS, v4u{q[it][0], q[it][2], q[it][4], q[it][6]}); st128(o + 2 * KXS, v4u{q[it][1], q[it][3], q[it][5], q[it][7]});
          st128(o + 1 * KXS, v4u{q[it][2], q[it][4], q[it][6], q[it][8]}); st128(o, v4u{q[it][3], q[it][5], q[it][7], q[it][9]});
        }
      }
      waitl(); lds_inc(lb + CTL + 4, lane);
    }
    ST(32);
    const int vdw = vd + aw * 8192;
    f32x4 ya[8];
#pragma unroll
    for (int c = 0; c < 8; ++c) ya[c] = f32x4{0.f, 0.f, 0.f, 0.f};
    unsigned u0 = lb + UB + ch * UCH + (lane & 15) * 32 + (lane >> 4) * 16;
    unsigned u1 = u0 - rh * 256, a0 = lb + KX + ch * KXC + (lane & 3) * KXS - ((lane >> 2) & 3) * 8 + (lane >> 4) * 16 - rh * 256 + 24;
    asm volatile("" : "+v"(u1), "+v"(a0));
    v4u uf[2][8], af[2][4];
    auto afrag = [&](int p, int e) {
      const int o = 2048 - 512 * e - 64 * p;
      const v2u x0 = *(volatile AS3 v2u*)(a0 + o), x1 = *(volatile AS3 v2u*)(a0 + o + 8);
      return v4u{x0.x, x0.y, x1.x, x1.y};
    };
    lds_wait(lb + CTL + 4, 7);
    ST(33);
#pragma unroll
    for (int c = 0; c < 8; ++c) { if (c < 4) af[0][c] = afrag(0, c); uf[0][c] = ld128(u1 + 480 + 512 * c); }
    if (rh == 0) {
      const v4u a4 = afrag(0, 4);
#pragma unroll
      for (int c = 4; c < 8; ++c) ya[c] = mma16(a4, uf[0][c - 4], ya[c]);
    }
    fence();
    v4u bo, be, tf[4], so[2], se[2];
#pragma unroll
    for (int p = 0; p < 4; ++p) {
#pragma unroll
      for (int cp = 0; cp < 8; ++cp) {
        if (p < 3) { if (cp < 4) af[(p + 1) & 1][cp] = afrag(p + 1, cp); uf[(p + 1) & 1][cp] = ld128(u1 + 480 + 512 * cp - 64 * (p + 1)); }
        if (p == 2 && cp < 4) {
          const int sh = (lane & 15) - 8 * (lane >> 4) - 2 * cp + 32;
          const unsigned mo = (unsigned)((0x7full << 5 << sh) >> 32) & 3, me = (unsigned)((0x1ffull << 4 << sh) >> 32) & 3;
          bo[cp] = ((mo | (mo << 15)) & 0x10001u) * 0x3f80u; be[cp] = ((me | (me << 15)) & 0x10001u) * 0x3f80u;
        }
        if (p == 3 && cp < 4) {
          const int k = 64 + 16 * (cp - rh) + (lane & 15) - 4 * (lane >> 4);
#pragma unroll
          for (int e = 0; e < 4; ++e) {
            const unsigned t = *(AS3 unsigned*)(lb + KT + ch * TBL + 780 + 4 * (k - e));
            tf[cp][e] = cp < 2 && k - e < 64 ? 0u : t;
          }
          if (cp < 2) { so[cp] = ld128(u0 + 480 + 512 * (2 * cp + rh)); se[cp] = ld128(u0 + 464 + 512 * (2 * cp + rh)); }
        }
#pragma unroll
        for (int c = cp; c < 8 && c < cp + 4; ++c) ya[c] = mma16(af[p & 1][c - cp], uf[p & 1][cp], ya[c]);
        if (p < 2 && cp < 6 && 32 + 6 * p + cp < 43) dma1(lb + wslot_lds(32 + 6 * p + cp) + aw * 1024, vdw, rd, wslot_q(32 + 6 * p + cp) * 32768);
        fence();
      }
      if (p == 0) { waitv<6>(); lds_inc(lb + CTL + 32, lane); }
    }
#pragma unroll
    for (int m = 0; m < 2; ++m) {
      const f32x4 z = {0.f, 0.f, 0.f, 0.f}, wo = mma16(so[m], bo, z), we = mma16(se[m], be, z);
      so[m] = v4u{pk(wo[0], we[0]), pk(wo[1], we[1]), pk(wo[2], we[2]), pk(wo[3], we[3])};
    }
#pragma unroll
    for (int m = 0; m < 2; ++m)
#pragma unroll
      for (int c = 4 + 2 * m; c < 8; ++c) ya[c] = mma16(so[m], tf[c - 4 - 2 * m], ya[c]);
    ST(34);
    { const int G = lane >> 4, a = lane & 15;
      const int vo = (2 * b + ch) * 8192 + (4 * (G & 1) + (a >> 2)) * 256 + rh * 128 + (G >> 1) * 64 + (a & 3) * 16;
#pragma unroll
      for (int c = 0; c < 8; c += 2) {
        const auto s0 = __builtin_amdgcn_permlane16_swap(pk(ya[c][0], ya[c][1]), pk(ya[c + 1][0], ya[c + 1][1]), false, false);
        const auto s1 = __builtin_amdgcn_permlane16_swap(pk(ya[c][2], ya[c][3]), pk(ya[c + 1][2], ya[c + 1][3]), false, false);
        stws(v4u{s0[0], s1[0], s0[1], s1[1]}, rws, vo, AT + c * 1024);
      }
    }
    waitv<4>(); lds_inc(lb + CTL + 8, lane);
    waitv<0>();
    ST(35);
    if (lds_last(lb + CTL + 16, lane)) {
      if (lane == 0) set_flag(ws, FL_A + b * 128, ep + 1);
      __builtin_amdgcn_s_sleep(8);
      poll(ws, FL_A + (32 * c2 + (lane & 31)) * 128, ep + 1);
      lds_inc(lb + CTL + 20, lane);
    } else lds_wait(lb + CTL + 20, 1);
    ST(37);
    { v4u av[2][2];
      const int vo = (64 * c2 + 16 * aw + (lane >> 3)) * 8192 + rt * 256 + (lane & 7) * 16;
#pragma unroll
      for (int i = 0; i < 2; ++i)
#pragma unroll
        for (int h = 0; h < 2; ++h) av[i][h] = __builtin_amdgcn_raw_buffer_load_b128(rws, vo, AT + i * 65536 + h * 128, 16);
#pragma unroll
      for (int i = 0; i < 2; ++i) {
        v4u s;
#pragma unroll
        for (int e = 0; e < 4; ++e) {
          float s0 = lo16(av[i][0][e]) + lo16(av[i][1][e]), s1 = hi16(av[i][0][e]) + hi16(av[i][1][e]);
          asm volatile("" : "+v"(s0)); s[e] = pk(s0, s1);
        }
        st128(lb + UB + aw * 2048 + i * 1024 + lane * 16, s);
      }
    }
    waitl();
    BAR(9); STW();
    if (b == 0 && aw == 1) {
#pragma unroll 1
      for (int i = 0; i < 4; ++i) poll(ws, FL_U + (i * 64 + lane) * 128, ep + 1);
      if (lane == 0) set_flag(ws, 0, ep + 1);
    }
    return;
  }

  const int wn = w & 1, wm = w >> 1;
  f32x16 acc[3][2];
#pragma unroll
  for (int j = 0; j < 3; ++j)
#pragma unroll
    for (int t = 0; t < 2; ++t) {
#pragma unroll
      for (int e = 0; e < 16; ++e) acc[j][t][e] = 0.f;
      asm volatile("" : "+v"(acc[j][t]));
    }
  unsigned wb = lb + (((lane >> 4) & 1) ? UO : GO) + 192 * (8 * (lane >> 5) + ((lane >> 2) & 3)) + 32 * wn + 8 * (lane & 3);
  unsigned xb[4];
#pragma unroll
  for (int s = 0; s < 4; ++s) xb[s] = lb + (64 * wm + (lane & 31)) * 128 + (((2 * s + (lane >> 5)) ^ ((lane >> 1) & 7)) << 4);
  {
    unsigned sr = lb + KS + w * SCW + (lane & 15) * SCR + (lane >> 4) * 16, sd = lb + KD + w * 3072 + lane * 16;
    ST(41);
    asm volatile("s_mov_b32 m0, %12\n\ts_waitcnt vmcnt(0)"
                 : "+v"(pt), "+v"(pa), "+v"(pi), "+v"(pr), "+v"(pc), "+v"(sr), "+v"(sd), "+v"(wb), "+v"(xb[0]), "+v"(xb[1]), "+v"(xb[2]), "+v"(xb[3])
                 : "s"(lb + KS - KSI + w * SCW) : "memory");
    ST(42);
    const float dt = rbf(exp_acc(lo16(pt)));
    const float ma = rbf(exp_acc(lo16(pa))) * dt * -1.44269504088896340736f, ta = lo16(pi) * dt;
    const float IH = 0.15915493667125701904296875f, IL = 6.420638316725915e-9f;
    const float ha = ta * IH, la = __builtin_fmaf(ta, IL, __builtin_fmaf(ta, IH, -ha));
    auto seed = [&](float m, float sg) {
      const float xa = __builtin_amdgcn_exp2f(ma * m), fa = __builtin_fmaf(la, m, __builtin_amdgcn_fractf(ha * m));
      return f32x2{xa * __builtin_amdgcn_cosf(fa), sg * xa * __builtin_amdgcn_sinf(fa)};
    };
    const f32x2 w1 = seed(1.f, 1.f), w8 = seed(8.f, -1.f), w128 = seed(128.f, 1.f);
    const f32x2 w2 = cmul(w1, w1), w4 = cmul(w2, w2);
    f32x2 c[8], z = w8;
    c[0] = f32x2{lo16(pr), lo16(pc)} * (2.f * dt); c[1] = cmul(c[0], w128);
    f32x2 y = cmul(c[1], w128);
    asm volatile("s_nop 0" : "+v"(z), "+v"(y));
    sfor<1, 15>([&](auto I) { constexpr int i = I + 1; if constexpr (i < 8) c[i] = y; cstep<I, (i < 4 ? 8 + 4 * i : i < 8 ? 16 + 2 * i : 24 + i)>(z, w8, y, w128); });
    cput<15, 40>(z, y);
    cput<0, 25>(f32x2{1.f, 0.f}, cmul(c[4], w4));
    v4u pf[4], qf[4];
    f32x4 k0v = {0.f, 0.f, 0.f, 0.f}, k1v = k0v, k2v = k0v;
#pragma unroll
    for (int s = 0; s < 4; ++s) { pf[s] = ld128(sr + s * 64); qf[s] = ld128(sr + 32 * SCR + s * 64); }
    cput<27, 29>(cmul(c[5], w4), cmul(c[6], w4));
    z = cmul(c[2], w2); y = cmul(c[3], w2);
    asm volatile("s_nop 0" : "+v"(z), "+v"(y));
    cstep<17, 21>(z, w2, y, w2); cstep<18, 22>(z, w2, y, w2);
    cput<19, 23>(z, y);
    cput<31, 31>(cmul(c[7], w4), cmul(c[7], w4));
    fence();
#pragma unroll
    for (int s = 0; s < 4; ++s) k2v = mma16(pf[s], qf[s], k2v);
#pragma unroll
    for (int s = 0; s < 4; ++s) qf[s] = ld128(sr + 16 * SCR + s * 64);
    fence();
    z = c[0]; y = c[1];
    sfor<0, 7>([&](auto I) { cstep<I, 8 + I>(z, w1, y, w1); });
    cput<7, 15>(z, y);
    ST(43);
    v4u rf[4];
#pragma unroll
    for (int s = 0; s < 4; ++s) rf[s] = ld128(sr + s * 64);
    st128(sd + 2048, __builtin_bit_cast(v4u, k2v));
    fence();
#pragma unroll
    for (int s = 0; s < 4; ++s) k1v = mma16(pf[s], qf[s], k1v);
#pragma unroll
    for (int s = 0; s < 4; ++s) k0v = mma16(pf[s], rf[s], k0v);
    st128(sd + 1024, __builtin_bit_cast(v4u, k1v));
    st128(sd, __builtin_bit_cast(v4u, k0v));
  }
  {
    v4u A[2][3], B[2][2];
    auto rd = [&](int buf, int s) {
#pragma unroll
      for (int t = 0; t < 2; ++t) B[buf][t] = ld128(xb[s] + 4096 * t);
#pragma unroll
      for (int j = 0; j < 3; ++j) A[buf][j] = trd2(wb + 3072 * s + 64 * j, wb + 3072 * s + 768 + 64 * j);
    };
    auto mm = [&](int buf) {
#pragma unroll
      for (int j = 0; j < 3; ++j)
#pragma unroll
        for (int t = 0; t < 2; ++t) acc[j][t] = mma32(A[buf][j], B[buf][t], acc[j][t]);
#pragma unroll
      for (int i = 0; i < 4; ++i) { __builtin_amdgcn_sched_group_barrier(0x8, 1, 0); __builtin_amdgcn_sched_group_barrier(0x100, 2, 0); }
      __builtin_amdgcn_sched_group_barrier(0x8, 1, 0); __builtin_amdgcn_sched_group_barrier(0x6, 10, 0);
      __builtin_amdgcn_sched_group_barrier(0x8, 1, 0);
      fence();
    };
    fence(); asm volatile("s_waitcnt lgkmcnt(0)" : "+s"(ep) :: "memory"); BAR(0);
    rd(0, 0); fence();
    int stg = 0;
#pragma unroll 1
    for (int t = 0; t < 8; ++t) {
      rd(1, 1); mm(0); rd(0, 2); mm(1); rd(1, 3);
      const int dl = stg == 2 ? -2 * STG : STG; stg = stg == 2 ? 0 : stg + 1;
      wb += dl;
#pragma unroll
      for (int s = 0; s < 4; ++s) xb[s] += dl;
      mm(0);
      waitl(); BAR(t + 1); fence();
      rd(0, 0); mm(1);
    }
  }
  ST(32);
  const int kh = wn, tt = wm;
  const unsigned wl = kh * 2048 + (4 * (lane >> 5) + ((lane >> 2) & 3)) * 128 + ((((lane >> 3) & 1) ^ kh) * 64) + ((lane >> 4) & 1) * 32 + (lane & 1) * 16 + ((lane >> 1) & 1) * 8;
  unsigned wa[3][2];
  auto wfrag = [&](int s, int c) { const int o = wslot_lds(s), i = o / 61440; return trd2(wa[i][c] + (o - i * 61440), wa[i][c] + (o - i * 61440) + 1024); };
  v4u wr[NREG][2];
#pragma unroll
  for (int j = 0; j < 4; ++j) {
    const int unit = ct * 6 + 2 * j + wn;
    if (j == CPJ) {
#pragma unroll
      for (int i = 0; i < 3; ++i) { wa[i][0] = lb + wl + i * 61440; wa[i][1] = lb + (wl ^ 64) + i * 61440; asm volatile("" : "+v"(wa[i][0]), "+v"(wa[i][1])); }
      ST(45);
      lds_wait(lb + CTL, 1);
#pragma unroll
      for (int s = 0; s < NREG; ++s) { wr[s][0] = wfrag(s, 0); wr[s][1] = wfrag(s, 1); }
      fence();
    }
    if (j == 1) ST(44); if (j == 2) ST(46);
    if (j == 3) break;
    if (__builtin_expect(unit < NU, 1)) {
#pragma unroll
      for (int t = 0; t < 2; ++t) {
        v4u hv;
#pragma unroll
        for (int e = 0; e < 4; ++e) {
          const f32x2 g = {acc[j][t][2 * e], acc[j][t][2 * e + 1]}, u = {acc[j][t][2 * e + 8], acc[j][t][2 * e + 9]};
          const f32x2 z = g * __builtin_elementwise_fma(g * g, f32x2{-0.1029432397f, -0.1029432397f}, f32x2{-2.3022081986f, -2.3022081986f});
          const f32x2 dn = f32x2{__builtin_amdgcn_exp2f(z.x), __builtin_amdgcn_exp2f(z.y)} + 1.f;
          const f32x2 hh = g * u * f32x2{__builtin_amdgcn_rcpf(dn.x), __builtin_amdgcn_rcpf(dn.y)};
          hv[e] = pk(hh.x, hh.y);
        }
        stws(hv, rws, lane * 16, HB + ((rp * 4 + wm * 2 + t) * NU + unit) * 1024);
        fence();
      }
    } else if (j == 0 && wn == 0) {
#pragma unroll
      for (int t = 0; t < 2; ++t) {
        const float s = rbf(acc[0][t][0] + acc[0][t][9]);
        const float g = rbf(__builtin_amdgcn_rcpf(1.f + __builtin_amdgcn_exp2f(s * -1.44269504088896340736f)));
        if (lane < 32) __builtin_amdgcn_raw_buffer_store_b32(__float_as_uint(g), rws, lane * 4, GV + (rp * 128 + wm * 64 + t * 32) * 4, 16);
      }
    }
  }
  waitl(); lds_inc(lb + CTL + 4, lane);
  ST(33);
  waitv<0>();
  ST(34);
  if (lds_last(lb + CTL + 24, lane)) {
    if (lane == 0) set_flag(ws, FL_H + (rp * 16 + ct) * 128, ep + 1);
    __builtin_amdgcn_s_sleep(8);
    poll(ws, FL_H + ((rt >> 1) * 16 + (lane & 15)) * 128, ep + 1);
    lds_inc(lb + CTL + 28, lane);
  } else lds_wait(lb + CTL + 28, 1);
  ST(35);
  constexpr int WIN = WINN;
  const int hA = HB + ((rt * 2 + tt) * NU + kh) * 1024;
  v4u hf[WIN];
  auto hload = [&](int s) { hf[s % WIN] = __builtin_amdgcn_raw_buffer_load_b128(rws, lane * 16, hA + wslot_q(s) * 2048, 16); };
#pragma unroll
  for (int s = 0; s < WIN; ++s) hload(s);
  fence();
  f32x16 o0, o1;
#pragma unroll
  for (int e = 0; e < 16; ++e) { o0[e] = 0.f; o1[e] = 0.f; }
  v4u wf[3][2];
  unsigned gu = 0;
#pragma unroll
  for (int s = 0; s < 43; ++s) {
    if (s == 0) lds_wait(lb + CTL + 32, 4);
    if (s == 28) lds_wait(lb + CTL + 8, 8);
    if (s == 20) lds_inc(lb + CTL + 8, lane);
    const v4u hb = hf[s % WIN];
    o1 = mma32(s < NREG ? wr[s < NREG ? s : 0][1] : wf[s % 3][1], hb, o1);
    if (s >= NREG - 2 && s < 41) { wf[(s + 2) % 3][1] = wfrag(s + 2, 1); wf[(s + 2) % 3][0] = wfrag(s + 2, 0); }
    fence();
    o0 = mma32(s < NREG ? wr[s < NREG ? s : 0][0] : wf[s % 3][0], hb, o0);
    if (s + WIN < 43) hload(s + WIN);
    if (s + WIN == 43) gu = __builtin_amdgcn_raw_buffer_load_b32(rws, (lane & 31) * 4, GV + (rt * 64 + tt * 32) * 4, 16);
    fence();
  }
  char* op = ptr(kc[0], kc[1]) + (rt * 64 + tt * 32 + (lane & 31)) * 1024 + c2 * 128 + kh * 64 + (lane >> 5) * 16;
  const unsigned ex = lb + lane * 16;
#pragma unroll
  for (int m = 0; m < 4; ++m)
    st128(ex + w * 4096 + m * 1024, v4u{__float_as_uint(o1[4 * m]), __float_as_uint(o1[4 * m + 1]), __float_as_uint(o1[4 * m + 2]), __float_as_uint(o1[4 * m + 3])});
  const float gt = __uint_as_float(gu), gb = rbf(1.f - gt);
  const unsigned a0 = lb + UB + (32 * kh + 8 * (lane >> 5) + ((lane >> 2) & 3)) * 128 + ((lane >> 1) & 1) * 64 + (2 * tt + ((lane >> 4) & 1)) * 16 + (lane & 1) * 8;
  fence(); waitl(); BAR(9);
  v2u at[4]; v4u pp[4];
#pragma unroll
  for (int m = 0; m < 4; ++m) { pp[m] = ld128(ex + (w ^ 1) * 4096 + m * 1024); at[m] = trd(a0 + (16 * (m >> 1) + 4 * (m & 1)) * 128); }
  fence();
  unsigned ov[8];
#pragma unroll
  for (int m = 0; m < 4; ++m) {
    const float r0 = gt * lo16(at[m].x) + gb * (o0[4 * m] + __uint_as_float(pp[m].x)), r1 = gt * hi16(at[m].x) + gb * (o0[4 * m + 1] + __uint_as_float(pp[m].y));
    const float r2 = gt * lo16(at[m].y) + gb * (o0[4 * m + 2] + __uint_as_float(pp[m].z)), r3 = gt * hi16(at[m].y) + gb * (o0[4 * m + 3] + __uint_as_float(pp[m].w));
    ov[2 * m] = pk(r0, r1); ov[2 * m + 1] = pk(r2, r3);
    if (m & 1) *(AS1 v4u*)(op + 16 * (m - 1)) = v4u{ov[2 * m - 2], ov[2 * m - 1], ov[2 * m], ov[2 * m + 1]};
  }
  STW();
}

extern "C" void kernel_launch(void* const* d_in, const int* in_sizes, int n_in, void* d_out, int out_size, void* d_ws, size_t ws_size,
                              hipStream_t stream) {
  gated_mix_fused<<<256, 512, 0, stream>>>(d_in[0], d_in[8], d_in[9], d_in[11], d_in[1], d_in[2], d_in[3], d_in[4], d_in[5], d_in[10], d_ws,
                                           d_in[6], d_out);
}
```

```cpp
#include <hip/hip_runtime.h>
constexpr int UT = 262144, AT = 2359296, HB = 4456448, GV = 36864, WS_END = 10092544;

__global__ __launch_bounds__(512, 2) void gated_mix_fused(
    const void* x, unsigned nx, unsigned fx, const void* wg, unsigned ng, unsigned fg, const void* wu, unsigned nu, unsigned fu,
    const void* wd, unsigned nd, unsigned fd, void* ws, unsigned nw, unsigned fw, const void* logA, const void* Aim, const void* Cr,
    const void* Ci, const void* logdt, const void* gw, const void* dskip, void* out) {
#if defined(__HIP_DEVICE_COMPILE__)
  __shared__ __attribute__((aligned(16))) char smem[163840];
  auto kp = __builtin_amdgcn_kernarg_segment_ptr();
  unsigned bid = blockIdx.x, tid = threadIdx.x;
  asm volatile(
      "s_load_dwordx16 s[8:23], s[0:1], 0x0\n"
      "s_load_dwordx16 s[24:39], s[0:1], 0x40\n"
      "s_load_dwordx4 s[40:43], s[0:1], 0x80\n"
      "s_getpc_b64 s[4:5]\n"
      "v_readfirstlane_b32 s44, v0\n"
      "v_and_b32 v250, 63, v0\n"
      "s_lshr_b32 s44, s44, 6\n"
      "s_cmp_gt_u32 s44, 3\n"
      "s_cbranch_scc1 LA2_%=\n"
      "s_lshl_b32 s56, s2, 9\n"
      "v_lshl_add_u32 v1, v0, 1, s56\n"
      "s_and_b32 s57, s44, 2\n"
      "s_lshl2_add_u32 s57, s2, s57\n"
      "v_mov_b32 v2, s57\n"
      "s_waitcnt lgkmcnt(0)\n"
      "global_load_ushort v200, v2, s[36:37]\n"
      "global_load_ushort v201, v1, s[28:29]\n"
      "global_load_ushort v202, v1, s[30:31]\n"
      "global_load_ushort v203, v1, s[32:33]\n"
      "global_load_ushort v204, v1, s[34:35]\n"
      "s_branch LG1_%=\n"
      "LA2_%=:\n"
      "s_cmp_eq_u32 s44, 4\n"
      "s_cbranch_scc1 Lx7_%=\n"
      "s_cmp_gt_u32 s44, 5\n"
      "s_cbranch_scc1 LD6_%=\n"
      "s_and_b32 s4, s4, 0xffffff00\n"
      "s_mov_b32 s6, 36712\n"
      "s_mov_b32 s7, 0x20000\n"
      "s_lshr_b32 s56, s2, 3\n"
      "s_lshl_b32 s56, s56, 7\n"
      "s_mov_b32 m0, 0x100\n"
      "v_lshlrev_b32 v1, 12, v250\n"
      "buffer_load_dword v1, s[4:7], s56 offen lds\n"
      "s_branch LM3_%=\n"
      "Lx7_%=:\n"
      "s_bfe_u32 s45, s2, 0x30003\n"
      "s_lshr_b32 s46, s2, 6\n"
      "s_bfe_u32 s47, s2, 0x20001\n"
      "s_and_b32 s48, s2, 1\n"
      "s_lshl3_add_u32 s49, s48, s45\n"
      "s_lshl1_add_u32 s56, s46, s45\n"
      "s_add_u32 s57, s47, s48\n"
      "s_lshl1_add_u32 s50, s56, s57\n"
      "s_and_b32 s50, s50, 7\n"
      "s_lshr_b32 s56, s47, 1\n"
      "s_and_b32 s57, s47, 1\n"
      "s_mul_i32 s58, s56, 7\n"
      "s_xor_b32 s56, s56, s45\n"
      "s_or_b32 s56, s56, 14\n"
      "s_add_u32 s59, s58, 3\n"
      "s_lshr_b32 s60, s45, 2\n"
      "s_cmp_eq_u32 s60, s57\n"
      "s_cselect_b32 s56, s59, s56\n"
      "s_lshl2_add_u32 s58, s57, s58\n"
      "s_add_u32 s58, s58, s46\n"
      "s_cmp_lt_u32 s46, 3\n"
      "s_cselect_b32 s51, s58, s56\n"
      "v_mul_u32_u24 v23, 171, v250\n"
      "v_lshrrev_b32 v21, 4, v250\n"
      "v_add_u32 v24, 0, v23\n"
      "v_add_u32 v25, 10944, v23\n"
      "v_add_u32 v26, 21888, v23\n"
      "v_lshrrev_b32 v24, 11, v24\n"
      "v_lshrrev_b32 v25, 11, v25\n"
      "v_lshrrev_b32 v26, 11, v26\n"
      "v_xor_b32 v21, v250, v21\n"
      "v_lshrrev_b32 v28, 2, v24\n"
      "v_lshrrev_b32 v29, 2, v25\n"
      "v_lshrrev_b32 v30, 2, v26\n"
      "v_lshrrev_b32 v20, 3, v250\n"
      "v_and_b32 v21, 7, v21\n"
      "v_and_b32 v28, 2, v28\n"
      "v_and_b32 v29, 2, v29\n"
      "v_and_b32 v30, 2, v30\n"
      "s_lshl_b32 s56, s49, 17\n"
      "v_lshlrev_b32 v21, 4, v21\n"
      "v_lshl_add_u32 v20, v20, 10, s56\n"
      "v_xor_b32 v28, v250, v28\n"
      "v_xor_b32 v29, v250, v29\n"
      "v_xor_b32 v30, v250, v30\n"
      "s_mul_i32 s57, s51, 192\n"
      "v_or_b32 v1, v20, v21\n"
      "v_lshl_add_u32 v32, v28, 4, s57\n"
      "v_lshl_add_u32 v33, v29, 4, s57\n"
      "v_lshl_add_u32 v34, v30, 4, s57\n"
      "s_movk_i32 s58, 2560\n"
      "v_xor_b32 v22, 64, v1\n"
      "v_mad_u32_u24 v5, v24, s58, v32\n"
      "v_mad_u32_u24 v6, v25, s58, v33\n"
      "v_mad_u32_u24 v7, v26, s58, v34\n"
      "v_add_u32 v2, 7168, v22\n"
      "v_add_u32 v3, 14336, v1\n"
      "v_add_u32 v4, 21504, v22\n"
      "v_mov_b32 v8, v5\n"
      "v_mov_b32 v9, v6\n"
      "v_mov_b32 v10, v7\n"
      "s_movk_i32 s85, 2752\n"
      "s_waitcnt lgkmcnt(0)\n"
      "s_add_u32 s61, s50, 0\n"
      "s_and_b32 s61, s61, 7\n"
      "s_mov_b32 m0, 81920\n"
      "s_lshl_b32 s62, s61, 7\n"
      "buffer_load_dwordx4 v1, s[8:11], s62 offen offset:0 lds\n"
      "buffer_load_dwordx4 v2, s[8:11], s62 offen offset:1024 lds\n"
      "buffer_load_dwordx4 v3, s[8:11], s62 offen offset:2048 lds\n"
      "buffer_load_dwordx4 v4, s[8:11], s62 offen offset:3072 lds\n"
      "s_mov_b32 m0, 86016\n"
      "s_add_u32 s62, s62, 0x8000\n"
      "buffer_load_dwordx4 v1, s[8:11], s62 offen offset:0 lds\n"
      "buffer_load_dwordx4 v2, s[8:11], s62 offen offset:1024 lds\n"
      "buffer_load_dwordx4 v3, s[8:11], s62 offen offset:2048 lds\n"
      "buffer_load_dwordx4 v4, s[8:11], s62 offen offset:3072 lds\n"
      "s_mov_b32 m0, 90112\n"
      "s_add_u32 s62, s62, 0x8000\n"
      "buffer_load_dwordx4 v1, s[8:11], s62 offen offset:0 lds\n"
      "buffer_load_dwordx4 v2, s[8:11], s62 offen offset:1024 lds\n"
      "buffer_load_dwordx4 v3, s[8:11], s62 offen offset:2048 lds\n"
      "buffer_load_dwordx4 v4, s[8:11], s62 offen offset:3072 lds\n"
      "s_mov_b32 m0, 94208\n"
      "s_add_u32 s62, s62, 0x8000\n"
      "buffer_load_dwordx4 v1, s[8:11], s62 offen offset:0 lds\n"
      "buffer_load_dwordx4 v2, s[8:11], s62 offen offset:1024 lds\n"
      "buffer_load_dwordx4 v3, s[8:11], s62 offen offset:2048 lds\n"
      "buffer_load_dwordx4 v4, s[8:11], s62 offen offset:3072 lds\n"
      "s_cmp_gt_u32 s51, 13\n"
      "s_cbranch_scc1 Lsp8_%=\n"
      "Lsr9_%=:\n"
      "s_lshl_b32 s86, s85, 4\n"
      "s_lshl_b32 s64, s85, 6\n"
      "s_mov_b32 m0, 98304\n"
      "s_mul_i32 s63, s61, s64\n"
      "buffer_load_dwordx4 v5, s[12:15], s63 offen offset:0 lds\n"
      "buffer_load_dwordx4 v6, s[12:15], s63 offen offset:1024 lds\n"
      "buffer_load_dwordx4 v7, s[12:15], s63 offen offset:2048 lds\n"
      "s_add_u32 m0, m0, 12288\n"
      "s_nop 0\n"
      "buffer_load_dwordx4 v8, s[16:19], s63 offen offset:0 lds\n"
      "buffer_load_dwordx4 v9, s[16:19], s63 offen offset:1024 lds\n"
      "buffer_load_dwordx4 v10, s[16:19], s63 offen offset:2048 lds\n"
      "s_mov_b32 m0, 101376\n"
      "s_add_u32 s63, s63, s86\n"
      "buffer_load_dwordx4 v5, s[12:15], s63 offen offset:0 lds\n"
      "buffer_load_dwordx4 v6, s[12:15], s63 offen offset:1024 lds\n"
      "buffer_load_dwordx4 v7, s[12:15], s63 offen offset:2048 lds\n"
      "s_add_u32 m0, m0, 12288\n"
      "s_nop 0\n"
      "buffer_load_dwordx4 v8, s[16:19], s63 offen offset:0 lds\n"
      "buffer_load_dwordx4 v9, s[16:19], s63 offen offset:1024 lds\n"
      "buffer_load_dwordx4 v10, s[16:19], s63 offen offset:2048 lds\n"
      "s_mov_b32 m0, 104448\n"
      "s_add_u32 s63, s63, s86\n"
      "buffer_load_dwordx4 v5, s[12:15], s63 offen offset:0 lds\n"
      "buffer_load_dwordx4 v6, s[12:15], s63 offen offset:1024 lds\n"
      "buffer_load_dwordx4 v7, s[12:15], s63 offen offset:2048 lds\n"
      "s_add_u32 m0, m0, 12288\n"
      "s_nop 0\n"
      "buffer_load_dwordx4 v8, s[16:19], s63 offen offset:0 lds\n"
      "buffer_load_dwordx4 v9, s[16:19], s63 offen offset:1024 lds\n"
      "buffer_load_dwordx4 v10, s[16:19], s63 offen offset:2048 lds\n"
      "s_mov_b32 m0, 107520\n"
      "s_add_u32 s63, s63, s86\n"
      "buffer_load_dwordx4 v5, s[12:15], s63 offen offset:0 lds\n"
      "buffer_load_dwordx4 v6, s[12:15], s63 offen offset:1024 lds\n"
      "buffer_load_dwordx4 v7, s[12:15], s63 offen offset:2048 lds\n"
      "s_add_u32 m0, m0, 12288\n"
      "s_nop 0\n"
      "buffer_load_dwordx4 v8, s[16:19], s63 offen offset:0 lds\n"
      "buffer_load_dwordx4 v9, s[16:19], s63 offen offset:1024 lds\n"
      "buffer_load_dwordx4 v10, s[16:19], s63 offen offset:2048 lds\n"
      "s_and_b32 s56, s2, 7\n"
      "s_and_b32 s57, s45, 3\n"
      "s_lshl2_add_u32 s52, s56, s57\n"
      "s_lshr_b32 s53, s2, 5\n"
      "s_waitcnt lgkmcnt(0)\n"
      "s_lshr_b32 s56, s20, 7\n"
      "s_sub_u32 s56, 3, s56\n"
      "s_and_b32 s57, s47, 1\n"
      "s_xor_b32 s56, s56, s57\n"
      "s_andn2_b32 s56, 7, s56\n"
      "s_xor_b32 s53, s53, s56\n"
      "v_mov_b32 v251, 0x0\n"
      "v_mov_b32 v252, 1\n"
      "s_load_dwordx2 s[54:55], s[24:25], 0x0\n"
      "s_mov_b32 s87, 0x73625410\n"
      "s_cmp_eq_u32 s46, 1\n"
      "s_cselect_b32 s87, 0x73625410, s87\n"
      "s_cmp_eq_u32 s46, 2\n"
      "s_cselect_b32 s87, 0x73625410, s87\n"
      "s_cmp_eq_u32 s46, 3\n"
      "s_cselect_b32 s87, 0x73625410, s87\n"
      "s_lshr_b32 s61, s87, 4\n"
      "s_add_u32 s61, s61, s50\n"
      "s_and_b32 s61, s61, 7\n"
      "s_mov_b32 m0, 122880\n"
      "s_lshl_b32 s62, s61, 7\n"
      "buffer_load_dwordx4 v1, s[8:11], s62 offen offset:0 lds\n"
      "buffer_load_dwordx4 v2, s[8:11], s62 offen offset:1024 lds\n"
      "buffer_load_dwordx4 v3, s[8:11], s62 offen offset:2048 lds\n"
      "buffer_load_dwordx4 v4, s[8:11], s62 offen offset:3072 lds\n"
      "s_mov_b32 m0, 126976\n"
      "s_add_u32 s62, s62, 0x8000\n"
      "buffer_load_dwordx4 v1, s[8:11], s62 offen offset:0 lds\n"
      "buffer_load_dwordx4 v2, s[8:11], s62 offen offset:1024 lds\n"
      "buffer_load_dwordx4 v3, s[8:11], s62 offen offset:2048 lds\n"
      "buffer_load_dwordx4 v4, s[8:11], s62 offen offset:3072 lds\n"
      "s_mov_b32 m0, 131072\n"
      "s_add_u32 s62, s62, 0x8000\n"
      "buffer_load_dwordx4 v1, s[8:11], s62 offen offset:0 lds\n"
      "buffer_load_dwordx4 v2, s[8:11], s62 offen offset:1024 lds\n"
      "buffer_load_dwordx4 v3, s[8:11], s62 offen offset:2048 lds\n"
      "buffer_load_dwordx4 v4, s[8:11], s62 offen offset:3072 lds\n"
      "s_mov_b32 m0, 135168\n"
      "s_add_u32 s62, s62, 0x8000\n"
      "buffer_load_dwordx4 v1, s[8:11], s62 offen offset:0 lds\n"
      "buffer_load_dwordx4 v2, s[8:11], s62 offen offset:1024 lds\n"
      "buffer_load_dwordx4 v3, s[8:11], s62 offen offset:2048 lds\n"
      "buffer_load_dwordx4 v4, s[8:11], s62 offen offset:3072 lds\n"
      "v_lshrrev_b32 v20, 3, v250\n"
      "s_lshl_b32 s56, s53, 7\n"
      "v_and_b32 v21, 2, v20\n"
      "v_mov_b32 v11, 2.0\n"
      "v_xor_b32 v21, v250, v21\n"
      "v_and_b32 v21, 7, v21\n"
      "v_lshl_add_u32 v21, v21, 4, s56\n"
      "v_lshl_add_u32 v12, v20, 10, v21\n"
      "v_xor_b32 v21, 64, v12\n"
      "v_add_u32 v14, 14336, v12\n"
      "v_add_u32 v13, 7168, v21\n"
      "v_add_u32 v15, 14336, v13\n"
      "v_and_b32 v40, 15, v250\n"
      "v_lshrrev_b32 v41, 4, v250\n"
      "s_movk_i32 s56, 0xfe0\n"
      "s_movk_i32 s57, 0x1ff0\n"
      "v_lshlrev_b32 v42, 3, v41\n"
      "v_lshlrev_b32 v43, v40, s56\n"
      "v_lshlrev_b32 v44, v40, s57\n"
      "s_sub_u32 s80, s44, 4\n"
      "s_lshr_b32 s81, s80, 1\n"
      "v_lshrrev_b32 v43, v42, v43\n"
      "v_lshrrev_b32 v44, v42, v44\n"
      "s_and_b32 s82, s80, 1\n"
      "s_mul_i32 s58, s81, 4608\n"
      "v_bfe_u32 v200, v43, 0, 2\n"
      "v_bfe_u32 v201, v43, 2, 2\n"
      "v_bfe_u32 v202, v43, 4, 2\n"
      "v_bfe_u32 v203, v43, 6, 2\n"
      "v_bfe_u32 v204, v44, 0, 2\n"
      "v_bfe_u32 v205, v44, 2, 2\n"
      "v_bfe_u32 v206, v44, 4, 2\n"
      "v_bfe_u32 v207, v44, 6, 2\n"
      "s_add_u32 s58, s58, 512\n"
      "v_and_b32 v47, 12, v250\n"
      "s_lshl_b32 s59, s82, 8\n"
      "s_mul_i32 s60, s81, 8960\n"
      "v_mul_u32_u24 v200, 0x8001, v200\n"
      "v_mul_u32_u24 v201, 0x8001, v201\n"
      "v_mul_u32_u24 v202, 0x8001, v202\n"
      "v_mul_u32_u24 v203, 0x8001, v203\n"
      "v_mul_u32_u24 v204, 0x8001, v204\n"
      "v_mul_u32_u24 v205, 0x8001, v205\n"
      "v_mul_u32_u24 v206, 0x8001, v206\n"
      "v_mul_u32_u24 v207, 0x8001, v207\n"
      "v_lshl_add_u32 v45, v40, 1, v41\n"
      "s_add_u32 s60, s60, 9752\n"
      "v_and_b32 v46, 3, v250\n"
      "v_sub_u32 v47, v42, v47\n"
      "v_lshlrev_b32 v48, 2, v41\n"
      "s_sub_u32 s60, s60, s59\n"
      "v_and_b32 v200, 0x10001, v200\n"
      "v_and_b32 v201, 0x10001, v201\n"
      "v_and_b32 v202, 0x10001, v202\n"
      "v_and_b32 v203, 0x10001, v203\n"
      "v_and_b32 v204, 0x10001, v204\n"
      "v_and_b32 v205, 0x10001, v205\n"
      "v_and_b32 v206, 0x10001, v206\n"
      "v_and_b32 v207, 0x10001, v207\n"
      "v_lshl_add_u32 v45, v45, 4, s58\n"
      "v_mul_u32_u24 v46, 2240, v46\n"
      "v_lshl_add_u32 v47, v47, 1, s60\n"
      "v_sub_u32 v48, v40, v48\n"
      "s_lshl_b32 s65, s82, 4\n"
      "v_mul_u32_u24 v200, 0x3f80, v200\n"
      "v_mul_u32_u24 v201, 0x3f80, v201\n"
      "v_mul_u32_u24 v202, 0x3f80, v202\n"
      "v_mul_u32_u24 v203, 0x3f80, v203\n"
      "v_mul_u32_u24 v204, 0x3f80, v204\n"
      "v_mul_u32_u24 v205, 0x3f80, v205\n"
      "v_mul_u32_u24 v206, 0x3f80, v206\n"
      "v_mul_u32_u24 v207, 0x3f80, v207\n"
      "v_subrev_u32 v225, s59, v45\n"
      "v_lshl_add_u32 v224, s59, 1, v45\n"
      "v_add_u32 v226, v46, v47\n"
      "v_subrev_u32 v227, s65, v48\n"
      "s_waitcnt lgkmcnt(0)\n"
      "s_add_u32 s54, s54, 1\n"
      "s_addc_u32 s55, s55, 0\n"
      "s_waitcnt vmcnt(28)\n"
      "s_barrier\n"
      "s_mov_b32 m0, 139264\n"
      "s_mul_i32 s63, s61, s64\n"
      "buffer_load_dwordx4 v5, s[12:15], s63 offen offset:0 lds\n"
      "buffer_load_dwordx4 v6, s[12:15], s63 offen offset:1024 lds\n"
      "buffer_load_dwordx4 v7, s[12:15], s63 offen offset:2048 lds\n"
      "s_add_u32 m0, m0, 12288\n"
      "s_nop 0\n"
      "buffer_load_dwordx4 v8, s[16:19], s63 offen offset:0 lds\n"
      "buffer_load_dwordx4 v9, s[16:19], s63 offen offset:1024 lds\n"
      "buffer_load_dwordx4 v10, s[16:19], s63 offen offset:2048 lds\n"
      "s_mov_b32 m0, 142336\n"
      "s_add_u32 s63, s63, s86\n"
      "buffer_load_dwordx4 v5, s[12:15], s63 offen offset:0 lds\n"
      "buffer_load_dwordx4 v6, s[12:15], s63 offen offset:1024 lds\n"
      "buffer_load_dwordx4 v7, s[12:15], s63 offen offset:2048 lds\n"
      "s_add_u32 m0, m0, 12288\n"
      "s_nop 0\n"
      "buffer_load_dwordx4 v8, s[16:19], s63 offen offset:0 lds\n"
      "buffer_load_dwordx4 v9, s[16:19], s63 offen offset:1024 lds\n"
      "buffer_load_dwordx4 v10, s[16:19], s63 offen offset:2048 lds\n"
      "s_mov_b32 m0, 145408\n"
      "s_add_u32 s63, s63, s86\n"
      "buffer_load_dwordx4 v5, s[12:15], s63 offen offset:0 lds\n"
      "buffer_load_dwordx4 v6, s[12:15], s63 offen offset:1024 lds\n"
      "buffer_load_dwordx4 v7, s[12:15], s63 offen offset:2048 lds\n"
      "s_add_u32 m0, m0, 12288\n"
      "s_nop 0\n"
      "buffer_load_dwordx4 v8, s[16:19], s63 offen offset:0 lds\n"
      "buffer_load_dwordx4 v9, s[16:19], s63 offen offset:1024 lds\n"
      "buffer_load_dwordx4 v10, s[16:19], s63 offen offset:2048 lds\n"
      "s_mov_b32 m0, 148480\n"
      "s_add_u32 s63, s63, s86\n"
      "buffer_load_dwordx4 v5, s[12:15], s63 offen offset:0 lds\n"
      "buffer_load_dwordx4 v6, s[12:15], s63 offen offset:1024 lds\n"
      "buffer_load_dwordx4 v7, s[12:15], s63 offen offset:2048 lds\n"
      "s_add_u32 m0, m0, 12288\n"
      "s_nop 0\n"
      "buffer_load_dwordx4 v8, s[16:19], s63 offen offset:0 lds\n"
      "buffer_load_dwordx4 v9, s[16:19], s63 offen offset:1024 lds\n"
      "buffer_load_dwordx4 v10, s[16:19], s63 offen offset:2048 lds\n"
      "s_waitcnt vmcnt(40)\n"
      "s_barrier\n"
      "s_lshr_b32 s61, s87, 8\n"
      "s_add_u32 s61, s61, s50\n"
      "s_and_b32 s61, s61, 7\n"
      "s_mov_b32 m0, 40960\n"
      "s_lshl_b32 s62, s61, 7\n"
      "buffer_load_dwordx4 v1, s[8:11], s62 offen offset:0 lds\n"
      "buffer_load_dwordx4 v2, s[8:11], s62 offen offset:1024 lds\n"
      "buffer_load_dwordx4 v3, s[8:11], s62 offen offset:2048 lds\n"
      "buffer_load_dwordx4 v4, s[8:11], s62 offen offset:3072 lds\n"
      "s_mov_b32 m0, 45056\n"
      "s_add_u32 s62, s62, 0x8000\n"
      "buffer_load_dwordx4 v1, s[8:11], s62 offen offset:0 lds\n"
      "buffer_load_dwordx4 v2, s[8:11], s62 offen offset:1024 lds\n"
      "buffer_load_dwordx4 v3, s[8:11], s62 offen offset:2048 lds\n"
      "buffer_load_dwordx4 v4, s[8:11], s62 offen offset:3072 lds\n"
      "s_mov_b32 m0, 49152\n"
      "s_add_u32 s62, s62, 0x8000\n"
      "buffer_load_dwordx4 v1, s[8:11], s62 offen offset:0 lds\n"
      "buffer_load_dwordx4 v2, s[8:11], s62 offen offset:1024 lds\n"
      "buffer_load_dwordx4 v3, s[8:11], s62 offen offset:2048 lds\n"
      "buffer_load_dwordx4 v4, s[8:11], s62 offen offset:3072 lds\n"
      "s_mov_b32 m0, 53248\n"
      "s_add_u32 s62, s62, 0x8000\n"
      "buffer_load_dwordx4 v1, s[8:11], s62 offen offset:0 lds\n"
      "buffer_load_dwordx4 v2, s[8:11], s62 offen offset:1024 lds\n"
      "buffer_load_dwordx4 v3, s[8:11], s62 offen offset:2048 lds\n"
      "buffer_load_dwordx4 v4, s[8:11], s62 offen offset:3072 lds\n"
      "s_mov_b32 m0, 57344\n"
      "s_mul_i32 s63, s61, s64\n"
      "buffer_load_dwordx4 v5, s[12:15], s63 offen offset:0 lds\n"
      "buffer_load_dwordx4 v6, s[12:15], s63 offen offset:1024 lds\n"
      "buffer_load_dwordx4 v7, s[12:15], s63 offen offset:2048 lds\n"
      "s_add_u32 m0, m0, 12288\n"
      "s_nop 0\n"
      "buffer_load_dwordx4 v8, s[16:19], s63 offen offset:0 lds\n"
      "buffer_load_dwordx4 v9, s[16:19], s63 offen offset:1024 lds\n"
      "buffer_load_dwordx4 v10, s[16:19], s63 offen offset:2048 lds\n"
      "s_waitcnt vmcnt(34)\n"
      "s_barrier\n"
      "s_mov_b32 m0, 60416\n"
      "s_add_u32 s63, s63, s86\n"
      "buffer_load_dwordx4 v5, s[12:15], s63 offen offset:0 lds\n"
      "buffer_load_dwordx4 v6, s[12:15], s63 offen offset:1024 lds\n"
      "buffer_load_dwordx4 v7, s[12:15], s63 offen offset:2048 lds\n"
      "s_add_u32 m0, m0, 12288\n"
      "s_nop 0\n"
      "buffer_load_dwordx4 v8, s[16:19], s63 offen offset:0 lds\n"
      "buffer_load_dwordx4 v9, s[16:19], s63 offen offset:1024 lds\n"
      "buffer_load_dwordx4 v10, s[16:19], s63 offen offset:2048 lds\n"
      "s_mov_b32 m0, 63488\n"
      "s_add_u32 s63, s63, s86\n"
      "buffer_load_dwordx4 v5, s[12:15], s63 offen offset:0 lds\n"
      "buffer_load_dwordx4 v6, s[12:15], s63 offen offset:1024 lds\n"
      "buffer_load_dwordx4 v7, s[12:15], s63 offen offset:2048 lds\n"
      "s_add_u32 m0, m0, 12288\n"
      "s_nop 0\n"
      "buffer_load_dwordx4 v8, s[16:19], s63 offen offset:0 lds\n"
      "buffer_load_dwordx4 v9, s[16:19], s63 offen offset:1024 lds\n"
      "buffer_load_dwordx4 v10, s[16:19], s63 offen offset:2048 lds\n"
      "s_mov_b32 m0, 66560\n"
      "s_add_u32 s63, s63, s86\n"
      "buffer_load_dwordx4 v5, s[12:15], s63 offen offset:0 lds\n"
      "buffer_load_dwordx4 v6, s[12:15], s63 offen offset:1024 lds\n"
      "buffer_load_dwordx4 v7, s[12:15], s63 offen offset:2048 lds\n"
      "s_add_u32 m0, m0, 12288\n"
      "s_nop 0\n"
      "buffer_load_dwordx4 v8, s[16:19], s63 offen offset:0 lds\n"
      "buffer_load_dwordx4 v9, s[16:19], s63 offen offset:1024 lds\n"
      "buffer_load_dwordx4 v10, s[16:19], s63 offen offset:2048 lds\n"
      "s_waitcnt vmcnt(40)\n"
      "s_barrier\n"
      "s_lshr_b32 s61, s87, 12\n"
      "s_add_u32 s61, s61, s50\n"
      "s_and_b32 s61, s61, 7\n"
      "s_mov_b32 m0, 81920\n"
      "s_lshl_b32 s62, s61, 7\n"
      "buffer_load_dwordx4 v1, s[8:11], s62 offen offset:0 lds\n"
      "buffer_load_dwordx4 v2, s[8:11], s62 offen offset:1024 lds\n"
      "buffer_load_dwordx4 v3, s[8:11], s62 offen offset:2048 lds\n"
      "buffer_load_dwordx4 v4, s[8:11], s62 offen offset:3072 lds\n"
      "s_mov_b32 m0, 86016\n"
      "s_add_u32 s62, s62, 0x8000\n"
      "buffer_load_dwordx4 v1, s[8:11], s62 offen offset:0 lds\n"
      "buffer_load_dwordx4 v2, s[8:11], s62 offen offset:1024 lds\n"
      "buffer_load_dwordx4 v3, s[8:11], s62 offen offset:2048 lds\n"
      "buffer_load_dwordx4 v4, s[8:11], s62 offen offset:3072 lds\n"
      "s_mov_b32 m0, 90112\n"
      "s_add_u32 s62, s62, 0x8000\n"
      "buffer_load_dwordx4 v1, s[8:11], s62 offen offset:0 lds\n"
      "buffer_load_dwordx4 v2, s[8:11], s62 offen offset:1024 lds\n"
      "buffer_load_dwordx4 v3, s[8:11], s62 offen offset:2048 lds\n"
      "buffer_load_dwordx4 v4, s[8:11], s62 offen offset:3072 lds\n"
      "s_mov_b32 m0, 94208\n"
      "s_add_u32 s62, s62, 0x8000\n"
      "buffer_load_dwordx4 v1, s[8:11], s62 offen offset:0 lds\n"
      "buffer_load_dwordx4 v2, s[8:11], s62 offen offset:1024 lds\n"
      "buffer_load_dwordx4 v3, s[8:11], s62 offen offset:2048 lds\n"
      "buffer_load_dwordx4 v4, s[8:11], s62 offen offset:3072 lds\n"
      "s_mov_b32 m0, 98304\n"
      "s_mul_i32 s63, s61, s64\n"
      "buffer_load_dwordx4 v5, s[12:15], s63 offen offset:0 lds\n"
      "buffer_load_dwordx4 v6, s[12:15], s63 offen offset:1024 lds\n"
      "buffer_load_dwordx4 v7, s[12:15], s63 offen offset:2048 lds\n"
      "s_add_u32 m0, m0, 12288\n"
      "s_nop 0\n"
      "buffer_load_dwordx4 v8, s[16:19], s63 offen offset:0 lds\n"
      "buffer_load_dwordx4 v9, s[16:19], s63 offen offset:1024 lds\n"
      "buffer_load_dwordx4 v10, s[16:19], s63 offen offset:2048 lds\n"
      "s_waitcnt vmcnt(34)\n"
      "s_barrier\n"
      "s_mov_b32 m0, 101376\n"
      "s_add_u32 s63, s63, s86\n"
      "buffer_load_dwordx4 v5, s[12:15], s63 offen offset:0 lds\n"
      "buffer_load_dwordx4 v6, s[12:15], s63 offen offset:1024 lds\n"
      "buffer_load_dwordx4 v7, s[12:15], s63 offen offset:2048 lds\n"
      "s_add_u32 m0, m0, 12288\n"
      "s_nop 0\n"
      "buffer_load_dwordx4 v8, s[16:19], s63 offen offset:0 lds\n"
      "buffer_load_dwordx4 v9, s[16:19], s63 offen offset:1024 lds\n"
      "buffer_load_dwordx4 v10, s[16:19], s63 offen offset:2048 lds\n"
      "s_mov_b32 m0, 104448\n"
      "s_add_u32 s63, s63, s86\n"
      "buffer_load_dwordx4 v5, s[12:15], s63 offen offset:0 lds\n"
      "buffer_load_dwordx4 v6, s[12:15], s63 offen offset:1024 lds\n"
      "buffer_load_dwordx4 v7, s[12:15], s63 offen offset:2048 lds\n"
      "s_add_u32 m0, m0, 12288\n"
      "s_nop 0\n"
      "buffer_load_dwordx4 v8, s[16:19], s63 offen offset:0 lds\n"
      "buffer_load_dwordx4 v9, s[16:19], s63 offen offset:1024 lds\n"
      "buffer_load_dwordx4 v10, s[16:19], s63 offen offset:2048 lds\n"
      "s_mov_b32 m0, 107520\n"
      "s_add_u32 s63, s63, s86\n"
      "buffer_load_dwordx4 v5, s[12:15], s63 offen offset:0 lds\n"
      "buffer_load_dwordx4 v6, s[12:15], s63 offen offset:1024 lds\n"
      "buffer_load_dwordx4 v7, s[12:15], s63 offen offset:2048 lds\n"
      "s_add_u32 m0, m0, 12288\n"
      "s_nop 0\n"
      "buffer_load_dwordx4 v8, s[16:19], s63 offen offset:0 lds\n"
      "buffer_load_dwordx4 v9, s[16:19], s63 offen offset:1024 lds\n"
      "buffer_load_dwordx4 v10, s[16:19], s63 offen offset:2048 lds\n"
      "s_waitcnt vmcnt(40)\n"
      "s_barrier\n"
      "s_lshr_b32 s61, s87, 16\n"
      "s_add_u32 s61, s61, s50\n"
      "s_and_b32 s61, s61, 7\n"
      "s_mov_b32 m0, 122880\n"
      "s_lshl_b32 s62, s61, 7\n"
      "buffer_load_dwordx4 v1, s[8:11], s62 offen offset:0 lds\n"
      "buffer_load_dwordx4 v2, s[8:11], s62 offen offset:1024 lds\n"
      "buffer_load_dwordx4 v3, s[8:11], s62 offen offset:2048 lds\n"
      "buffer_load_dwordx4 v4, s[8:11], s62 offen offset:3072 lds\n"
      "s_mov_b32 m0, 126976\n"
      "s_add_u32 s62, s62, 0x8000\n"
      "buffer_load_dwordx4 v1, s[8:11], s62 offen offset:0 lds\n"
      "buffer_load_dwordx4 v2, s[8:11], s62 offen offset:1024 lds\n"
      "buffer_load_dwordx4 v3, s[8:11], s62 offen offset:2048 lds\n"
      "buffer_load_dwordx4 v4, s[8:11], s62 offen offset:3072 lds\n"
      "s_mov_b32 m0, 131072\n"
      "s_add_u32 s62, s62, 0x8000\n"
      "buffer_load_dwordx4 v1, s[8:11], s62 offen offset:0 lds\n"
      "buffer_load_dwordx4 v2, s[8:11], s62 offen offset:1024 lds\n"
      "buffer_load_dwordx4 v3, s[8:11], s62 offen offset:2048 lds\n"
      "buffer_load_dwordx4 v4, s[8:11], s62 offen offset:3072 lds\n"
      "s_mov_b32 m0, 135168\n"
      "s_add_u32 s62, s62, 0x8000\n"
      "buffer_load_dwordx4 v1, s[8:11], s62 offen offset:0 lds\n"
      "buffer_load_dwordx4 v2, s[8:11], s62 offen offset:1024 lds\n"
      "buffer_load_dwordx4 v3, s[8:11], s62 offen offset:2048 lds\n"
      "buffer_load_dwordx4 v4, s[8:11], s62 offen offset:3072 lds\n"
      "s_mov_b32 m0, 139264\n"
      "s_mul_i32 s63, s61, s64\n"
      "buffer_load_dwordx4 v5, s[12:15], s63 offen offset:0 lds\n"
      "buffer_load_dwordx4 v6, s[12:15], s63 offen offset:1024 lds\n"
      "buffer_load_dwordx4 v7, s[12:15], s63 offen offset:2048 lds\n"
      "s_add_u32 m0, m0, 12288\n"
      "s_nop 0\n"
      "buffer_load_dwordx4 v8, s[16:19], s63 offen offset:0 lds\n"
      "buffer_load_dwordx4 v9, s[16:19], s63 offen offset:1024 lds\n"
      "buffer_load_dwordx4 v10, s[16:19], s63 offen offset:2048 lds\n"
      "s_waitcnt vmcnt(34)\n"
      "s_barrier\n"
      "s_mov_b32 m0, 142336\n"
      "s_add_u32 s63, s63, s86\n"
      "buffer_load_dwordx4 v5, s[12:15], s63 offen offset:0 lds\n"
      "buffer_load_dwordx4 v6, s[12:15], s63 offen offset:1024 lds\n"
      "buffer_load_dwordx4 v7, s[12:15], s63 offen offset:2048 lds\n"
      "s_add_u32 m0, m0, 12288\n"
      "s_nop 0\n"
      "buffer_load_dwordx4 v8, s[16:19], s63 offen offset:0 lds\n"
      "buffer_load_dwordx4 v9, s[16:19], s63 offen offset:1024 lds\n"
      "buffer_load_dwordx4 v10, s[16:19], s63 offen offset:2048 lds\n"
      "s_mov_b32 m0, 145408\n"
      "s_add_u32 s63, s63, s86\n"
      "buffer_load_dwordx4 v5, s[12:15], s63 offen offset:0 lds\n"
      "buffer_load_dwordx4 v6, s[12:15], s63 offen offset:1024 lds\n"
      "buffer_load_dwordx4 v7, s[12:15], s63 offen offset:2048 lds\n"
      "s_add_u32 m0, m0, 12288\n"
      "s_nop 0\n"
      "buffer_load_dwordx4 v8, s[16:19], s63 offen offset:0 lds\n"
      "buffer_load_dwordx4 v9, s[16:19], s63 offen offset:1024 lds\n"
      "buffer_load_dwordx4 v10, s[16:19], s63 offen offset:2048 lds\n"
      "s_mov_b32 m0, 148480\n"
      "s_add_u32 s63, s63, s86\n"
      "buffer_load_dwordx4 v5, s[12:15], s63 offen offset:0 lds\n"
      "buffer_load_dwordx4 v6, s[12:15], s63 offen offset:1024 lds\n"
      "buffer_load_dwordx4 v7, s[12:15], s63 offen offset:2048 lds\n"
      "s_add_u32 m0, m0, 12288\n"
      "s_nop 0\n"
      "buffer_load_dwordx4 v8, s[16:19], s63 offen offset:0 lds\n"
      "buffer_load_dwordx4 v9, s[16:19], s63 offen offset:1024 lds\n"
      "buffer_load_dwordx4 v10, s[16:19], s63 offen offset:2048 lds\n"
      "s_waitcnt vmcnt(40)\n"
      "s_barrier\n"
      "s_lshr_b32 s61, s87, 20\n"
      "s_add_u32 s61, s61, s50\n"
      "s_and_b32 s61, s61, 7\n"
      "s_mov_b32 m0, 40960\n"
      "s_lshl_b32 s62, s61, 7\n"
      "buffer_load_dwordx4 v1, s[8:11], s62 offen offset:0 lds\n"
      "buffer_load_dwordx4 v2, s[8:11], s62 offen offset:1024 lds\n"
      "buffer_load_dwordx4 v3, s[8:11], s62 offen offset:2048 lds\n"
      "buffer_load_dwordx4 v4, s[8:11], s62 offen offset:3072 lds\n"
      "s_mov_b32 m0, 45056\n"
      "s_add_u32 s62, s62, 0x8000\n"
      "buffer_load_dwordx4 v1, s[8:11], s62 offen offset:0 lds\n"
      "buffer_load_dwordx4 v2, s[8:11], s62 offen offset:1024 lds\n"
      "buffer_load_dwordx4 v3, s[8:11], s62 offen offset:2048 lds\n"
      "buffer_load_dwordx4 v4, s[8:11], s62 offen offset:3072 lds\n"
      "s_mov_b32 m0, 49152\n"
      "s_add_u32 s62, s62, 0x8000\n"
      "buffer_load_dwordx4 v1, s[8:11], s62 offen offset:0 lds\n"
      "buffer_load_dwordx4 v2, s[8:11], s62 offen offset:1024 lds\n"
      "buffer_load_dwordx4 v3, s[8:11], s62 offen offset:2048 lds\n"
      "buffer_load_dwordx4 v4, s[8:11], s62 offen offset:3072 lds\n"
      "s_mov_b32 m0, 53248\n"
      "s_add_u32 s62, s62, 0x8000\n"
      "buffer_load_dwordx4 v1, s[8:11], s62 offen offset:0 lds\n"
      "buffer_load_dwordx4 v2, s[8:11], s62 offen offset:1024 lds\n"
      "buffer_load_dwordx4 v3, s[8:11], s62 offen offset:2048 lds\n"
      "buffer_load_dwordx4 v4, s[8:11], s62 offen offset:3072 lds\n"
      "s_mov_b32 m0, 57344\n"
      "s_mul_i32 s63, s61, s64\n"
      "buffer_load_dwordx4 v5, s[12:15], s63 offen offset:0 lds\n"
      "buffer_load_dwordx4 v6, s[12:15], s63 offen offset:1024 lds\n"
      "buffer_load_dwordx4 v7, s[12:15], s63 offen offset:2048 lds\n"
      "s_add_u32 m0, m0, 12288\n"
      "s_nop 0\n"
      "buffer_load_dwordx4 v8, s[16:19], s63 offen offset:0 lds\n"
      "buffer_load_dwordx4 v9, s[16:19], s63 offen offset:1024 lds\n"
      "buffer_load_dwordx4 v10, s[16:19], s63 offen offset:2048 lds\n"
      "s_waitcnt vmcnt(34)\n"
      "s_barrier\n"
      "s_mov_b32 m0, 60416\n"
      "s_add_u32 s63, s63, s86\n"
      "buffer_load_dwordx4 v5, s[12:15], s63 offen offset:0 lds\n"
      "buffer_load_dwordx4 v6, s[12:15], s63 offen offset:1024 lds\n"
      "buffer_load_dwordx4 v7, s[12:15], s63 offen offset:2048 lds\n"
      "s_add_u32 m0, m0, 12288\n"
      "s_nop 0\n"
      "buffer_load_dwordx4 v8, s[16:19], s63 offen offset:0 lds\n"
      "buffer_load_dwordx4 v9, s[16:19], s63 offen offset:1024 lds\n"
      "buffer_load_dwordx4 v10, s[16:19], s63 offen offset:2048 lds\n"
      "s_mov_b32 m0, 63488\n"
      "s_add_u32 s63, s63, s86\n"
      "buffer_load_dwordx4 v5, s[12:15], s63 offen offset:0 lds\n"
      "buffer_load_dwordx4 v6, s[12:15], s63 offen offset:1024 lds\n"
      "buffer_load_dwordx4 v7, s[12:15], s63 offen offset:2048 lds\n"
      "s_add_u32 m0, m0, 12288\n"
      "s_nop 0\n"
      "buffer_load_dwordx4 v8, s[16:19], s63 offen offset:0 lds\n"
      "buffer_load_dwordx4 v9, s[16:19], s63 offen offset:1024 lds\n"
      "buffer_load_dwordx4 v10, s[16:19], s63 offen offset:2048 lds\n"
      "s_mov_b32 m0, 66560\n"
      "s_add_u32 s63, s63, s86\n"
      "buffer_load_dwordx4 v5, s[12:15], s63 offen offset:0 lds\n"
      "buffer_load_dwordx4 v6, s[12:15], s63 offen offset:1024 lds\n"
      "buffer_load_dwordx4 v7, s[12:15], s63 offen offset:2048 lds\n"
      "s_add_u32 m0, m0, 12288\n"
      "s_nop 0\n"
      "buffer_load_dwordx4 v8, s[16:19], s63 offen offset:0 lds\n"
      "buffer_load_dwordx4 v9, s[16:19], s63 offen offset:1024 lds\n"
      "buffer_load_dwordx4 v10, s[16:19], s63 offen offset:2048 lds\n"
      "s_waitcnt vmcnt(40)\n"
      "s_barrier\n"
      "s_lshr_b32 s61, s87, 24\n"
      "s_add_u32 s61, s61, s50\n"
      "s_and_b32 s61, s61, 7\n"
      "s_mov_b32 m0, 81920\n"
      "s_lshl_b32 s62, s61, 7\n"
      "buffer_load_dwordx4 v1, s[8:11], s62 offen offset:0 lds\n"
      "buffer_load_dwordx4 v2, s[8:11], s62 offen offset:1024 lds\n"
      "buffer_load_dwordx4 v3, s[8:11], s62 offen offset:2048 lds\n"
      "buffer_load_dwordx4 v4, s[8:11], s62 offen offset:3072 lds\n"
      "s_mov_b32 m0, 86016\n"
      "s_add_u32 s62, s62, 0x8000\n"
      "buffer_load_dwordx4 v1, s[8:11], s62 offen offset:0 lds\n"
      "buffer_load_dwordx4 v2, s[8:11], s62 offen offset:1024 lds\n"
      "buffer_load_dwordx4 v3, s[8:11], s62 offen offset:2048 lds\n"
      "buffer_load_dwordx4 v4, s[8:11], s62 offen offset:3072 lds\n"
      "s_mov_b32 m0, 90112\n"
      "s_add_u32 s62, s62, 0x8000\n"
      "buffer_load_dwordx4 v1, s[8:11], s62 offen offset:0 lds\n"
      "buffer_load_dwordx4 v2, s[8:11], s62 offen offset:1024 lds\n"
      "buffer_load_dwordx4 v3, s[8:11], s62 offen offset:2048 lds\n"
      "buffer_load_dwordx4 v4, s[8:11], s62 offen offset:3072 lds\n"
      "s_mov_b32 m0, 94208\n"
      "s_add_u32 s62, s62, 0x8000\n"
      "buffer_load_dwordx4 v1, s[8:11], s62 offen offset:0 lds\n"
      "buffer_load_dwordx4 v2, s[8:11], s62 offen offset:1024 lds\n"
      "buffer_load_dwordx4 v3, s[8:11], s62 offen offset:2048 lds\n"
      "buffer_load_dwordx4 v4, s[8:11], s62 offen offset:3072 lds\n"
      "s_mov_b32 m0, 98304\n"
      "s_mul_i32 s63, s61, s64\n"
      "buffer_load_dwordx4 v5, s[12:15], s63 offen offset:0 lds\n"
      "buffer_load_dwordx4 v6, s[12:15], s63 offen offset:1024 lds\n"
      "buffer_load_dwordx4 v7, s[12:15], s63 offen offset:2048 lds\n"
      "s_add_u32 m0, m0, 12288\n"
      "s_nop 0\n"
      "buffer_load_dwordx4 v8, s[16:19], s63 offen offset:0 lds\n"
      "buffer_load_dwordx4 v9, s[16:19], s63 offen offset:1024 lds\n"
      "buffer_load_dwordx4 v10, s[16:19], s63 offen offset:2048 lds\n"
      "s_waitcnt vmcnt(34)\n"
      "s_barrier\n"
      "s_mov_b32 m0, 101376\n"
      "s_add_u32 s63, s63, s86\n"
      "buffer_load_dwordx4 v5, s[12:15], s63 offen offset:0 lds\n"
      "buffer_load_dwordx4 v6, s[12:15], s63 offen offset:1024 lds\n"
      "buffer_load_dwordx4 v7, s[12:15], s63 offen offset:2048 lds\n"
      "s_add_u32 m0, m0, 12288\n"
      "s_nop 0\n"
      "buffer_load_dwordx4 v8, s[16:19], s63 offen offset:0 lds\n"
      "buffer_load_dwordx4 v9, s[16:19], s63 offen offset:1024 lds\n"
      "buffer_load_dwordx4 v10, s[16:19], s63 offen offset:2048 lds\n"
      "s_mov_b32 m0, 104448\n"
      "s_add_u32 s63, s63, s86\n"
      "buffer_load_dwordx4 v5, s[12:15], s63 offen offset:0 lds\n"
      "buffer_load_dwordx4 v6, s[12:15], s63 offen offset:1024 lds\n"
      "buffer_load_dwordx4 v7, s[12:15], s63 offen offset:2048 lds\n"
      "s_add_u32 m0, m0, 12288\n"
      "s_nop 0\n"
      "buffer_load_dwordx4 v8, s[16:19], s63 offen offset:0 lds\n"
      "buffer_load_dwordx4 v9, s[16:19], s63 offen offset:1024 lds\n"
      "buffer_load_dwordx4 v10, s[16:19], s63 offen offset:2048 lds\n"
      "s_mov_b32 m0, 107520\n"
      "s_add_u32 s63, s63, s86\n"
      "buffer_load_dwordx4 v5, s[12:15], s63 offen offset:0 lds\n"
      "buffer_load_dwordx4 v6, s[12:15], s63 offen offset:1024 lds\n"
      "buffer_load_dwordx4 v7, s[12:15], s63 offen offset:2048 lds\n"
      "s_add_u32 m0, m0, 12288\n"
      "s_nop 0\n"
      "buffer_load_dwordx4 v8, s[16:19], s63 offen offset:0 lds\n"
      "buffer_load_dwordx4 v9, s[16:19], s63 offen offset:1024 lds\n"
      "buffer_load_dwordx4 v10, s[16:19], s63 offen offset:2048 lds\n"
      "s_waitcnt vmcnt(40)\n"
      "s_barrier\n"
      "s_lshr_b32 s61, s87, 28\n"
      "s_add_u32 s61, s61, s50\n"
      "s_and_b32 s61, s61, 7\n"
      "s_mov_b32 m0, 122880\n"
      "s_lshl_b32 s62, s61, 7\n"
      "buffer_load_dwordx4 v1, s[8:11], s62 offen offset:0 lds\n"
      "buffer_load_dwordx4 v2, s[8:11], s62 offen offset:1024 lds\n"
      "buffer_load_dwordx4 v3, s[8:11], s62 offen offset:2048 lds\n"
      "buffer_load_dwordx4 v4, s[8:11], s62 offen offset:3072 lds\n"
      "s_mov_b32 m0, 126976\n"
      "s_add_u32 s62, s62, 0x8000\n"
      "buffer_load_dwordx4 v1, s[8:11], s62 offen offset:0 lds\n"
      "buffer_load_dwordx4 v2, s[8:11], s62 offen offset:1024 lds\n"
      "buffer_load_dwordx4 v3, s[8:11], s62 offen offset:2048 lds\n"
      "buffer_load_dwordx4 v4, s[8:11], s62 offen offset:3072 lds\n"
      "s_mov_b32 m0, 131072\n"
      "s_add_u32 s62, s62, 0x8000\n"
      "buffer_load_dwordx4 v1, s[8:11], s62 offen offset:0 lds\n"
      "buffer_load_dwordx4 v2, s[8:11], s62 offen offset:1024 lds\n"
      "buffer_load_dwordx4 v3, s[8:11], s62 offen offset:2048 lds\n"
      "buffer_load_dwordx4 v4, s[8:11], s62 offen offset:3072 lds\n"
      "s_mov_b32 m0, 135168\n"
      "s_add_u32 s62, s62, 0x8000\n"
      "buffer_load_dwordx4 v1, s[8:11], s62 offen offset:0 lds\n"
      "buffer_load_dwordx4 v2, s[8:11], s62 offen offset:1024 lds\n"
      "buffer_load_dwordx4 v3, s[8:11], s62 offen offset:2048 lds\n"
      "buffer_load_dwordx4 v4, s[8:11], s62 offen offset:3072 lds\n"
      "s_mov_b32 m0, 139264\n"
      "s_mul_i32 s63, s61, s64\n"
      "buffer_load_dwordx4 v5, s[12:15], s63 offen offset:0 lds\n"
      "buffer_load_dwordx4 v6, s[12:15], s63 offen offset:1024 lds\n"
      "buffer_load_dwordx4 v7, s[12:15], s63 offen offset:2048 lds\n"
      "s_add_u32 m0, m0, 12288\n"
      "s_nop 0\n"
      "buffer_load_dwordx4 v8, s[16:19], s63 offen offset:0 lds\n"
      "buffer_load_dwordx4 v9, s[16:19], s63 offen offset:1024 lds\n"
      "buffer_load_dwordx4 v10, s[16:19], s63 offen offset:2048 lds\n"
      "s_waitcnt vmcnt(34)\n"
      "s_barrier\n"
      "s_mov_b32 m0, 142336\n"
      "s_add_u32 s63, s63, s86\n"
      "buffer_load_dwordx4 v5, s[12:15], s63 offen offset:0 lds\n"
      "buffer_load_dwordx4 v6, s[12:15], s63 offen offset:1024 lds\n"
      "buffer_load_dwordx4 v7, s[12:15], s63 offen offset:2048 lds\n"
      "s_add_u32 m0, m0, 12288\n"
      "s_nop 0\n"
      "buffer_load_dwordx4 v8, s[16:19], s63 offen offset:0 lds\n"
      "buffer_load_dwordx4 v9, s[16:19], s63 offen offset:1024 lds\n"
      "buffer_load_dwordx4 v10, s[16:19], s63 offen offset:2048 lds\n"
      "s_mov_b32 m0, 145408\n"
      "s_add_u32 s63, s63, s86\n"
      "buffer_load_dwordx4 v5, s[12:15], s63 offen offset:0 lds\n"
      "buffer_load_dwordx4 v6, s[12:15], s63 offen offset:1024 lds\n"
      "buffer_load_dwordx4 v7, s[12:15], s63 offen offset:2048 lds\n"
      "s_add_u32 m0, m0, 12288\n"
      "s_nop 0\n"
      "buffer_load_dwordx4 v8, s[16:19], s63 offen offset:0 lds\n"
      "buffer_load_dwordx4 v9, s[16:19], s63 offen offset:1024 lds\n"
      "buffer_load_dwordx4 v10, s[16:19], s63 offen offset:2048 lds\n"
      "s_mov_b32 m0, 148480\n"
      "s_add_u32 s63, s63, s86\n"
      "buffer_load_dwordx4 v5, s[12:15], s63 offen offset:0 lds\n"
      "buffer_load_dwordx4 v6, s[12:15], s63 offen offset:1024 lds\n"
      "buffer_load_dwordx4 v7, s[12:15], s63 offen offset:2048 lds\n"
      "s_add_u32 m0, m0, 12288\n"
      "s_nop 0\n"
      "buffer_load_dwordx4 v8, s[16:19], s63 offen offset:0 lds\n"
      "buffer_load_dwordx4 v9, s[16:19], s63 offen offset:1024 lds\n"
      "buffer_load_dwordx4 v10, s[16:19], s63 offen offset:2048 lds\n"
      "s_waitcnt vmcnt(40)\n"
      "s_barrier\n"
      "s_mov_b32 m0, 40960\n"
      "s_mov_b32 s62, 0\n"
      "buffer_load_dwordx4 v12, s[20:23], s62 offen offset:0 lds\n"
      "buffer_load_dwordx4 v13, s[20:23], s62 offen offset:1024 lds\n"
      "buffer_load_dwordx4 v14, s[20:23], s62 offen offset:2048 lds\n"
      "buffer_load_dwordx4 v15, s[20:23], s62 offen offset:3072 lds\n"
      "s_mov_b32 m0, 45056\n"
      "s_mov_b32 s62, 98304\n"
      "buffer_load_dwordx4 v12, s[20:23], s62 offen offset:0 lds\n"
      "buffer_load_dwordx4 v13, s[20:23], s62 offen offset:1024 lds\n"
      "buffer_load_dwordx4 v14, s[20:23], s62 offen offset:2048 lds\n"
      "buffer_load_dwordx4 v15, s[20:23], s62 offen offset:3072 lds\n"
      "s_mov_b32 m0, 49152\n"
      "s_mov_b32 s62, 196608\n"
      "buffer_load_dwordx4 v12, s[20:23], s62 offen offset:0 lds\n"
      "buffer_load_dwordx4 v13, s[20:23], s62 offen offset:1024 lds\n"
      "buffer_load_dwordx4 v14, s[20:23], s62 offen offset:2048 lds\n"
      "buffer_load_dwordx4 v15, s[20:23], s62 offen offset:3072 lds\n"
      "s_mov_b32 m0, 53248\n"
      "s_mov_b32 s62, 294912\n"
      "buffer_load_dwordx4 v12, s[20:23], s62 offen offset:0 lds\n"
      "buffer_load_dwordx4 v13, s[20:23], s62 offen offset:1024 lds\n"
      "buffer_load_dwordx4 v14, s[20:23], s62 offen offset:2048 lds\n"
      "buffer_load_dwordx4 v15, s[20:23], s62 offen offset:3072 lds\n"
      "s_mov_b32 m0, 57344\n"
      "s_mov_b32 s62, 393216\n"
      "buffer_load_dwordx4 v12, s[20:23], s62 offen offset:0 lds\n"
      "buffer_load_dwordx4 v13, s[20:23], s62 offen offset:1024 lds\n"
      "buffer_load_dwordx4 v14, s[20:23], s62 offen offset:2048 lds\n"
      "buffer_load_dwordx4 v15, s[20:23], s62 offen offset:3072 lds\n"
      "s_waitcnt vmcnt(32)\n"
      "s_barrier\n"
      "s_mov_b32 m0, 61440\n"
      "s_mov_b32 s62, 491520\n"
      "buffer_load_dwordx4 v12, s[20:23], s62 offen offset:0 lds\n"
      "buffer_load_dwordx4 v13, s[20:23], s62 offen offset:1024 lds\n"
      "buffer_load_dwordx4 v14, s[20:23], s62 offen offset:2048 lds\n"
      "buffer_load_dwordx4 v15, s[20:23], s62 offen offset:3072 lds\n"
      "s_mov_b32 m0, 65536\n"
      "s_mov_b32 s62, 589824\n"
      "buffer_load_dwordx4 v12, s[20:23], s62 offen offset:0 lds\n"
      "buffer_load_dwordx4 v13, s[20:23], s62 offen offset:1024 lds\n"
      "buffer_load_dwordx4 v14, s[20:23], s62 offen offset:2048 lds\n"
      "buffer_load_dwordx4 v15, s[20:23], s62 offen offset:3072 lds\n"
      "s_mov_b32 m0, 69632\n"
      "s_mov_b32 s62, 688128\n"
      "buffer_load_dwordx4 v12, s[20:23], s62 offen offset:0 lds\n"
      "buffer_load_dwordx4 v13, s[20:23], s62 offen offset:1024 lds\n"
      "buffer_load_dwordx4 v14, s[20:23], s62 offen offset:2048 lds\n"
      "buffer_load_dwordx4 v15, s[20:23], s62 offen offset:3072 lds\n"
      "s_mov_b32 m0, 73728\n"
      "s_mov_b32 s62, 786432\n"
      "buffer_load_dwordx4 v12, s[20:23], s62 offen offset:0 lds\n"
      "buffer_load_dwordx4 v13, s[20:23], s62 offen offset:1024 lds\n"
      "buffer_load_dwordx4 v14, s[20:23], s62 offen offset:2048 lds\n"
      "buffer_load_dwordx4 v15, s[20:23], s62 offen offset:3072 lds\n"
      "s_waitcnt vmcnt(36)\n"
      "s_barrier\n"
      "s_mov_b32 m0, 77824\n"
      "s_mov_b32 s62, 884736\n"
      "buffer_load_dwordx4 v12, s[20:23], s62 offen offset:0 lds\n"
      "buffer_load_dwordx4 v13, s[20:23], s62 offen offset:1024 lds\n"
      "buffer_load_dwordx4 v14, s[20:23], s62 offen offset:2048 lds\n"
      "buffer_load_dwordx4 v15, s[20:23], s62 offen offset:3072 lds\n"
      "s_mov_b32 m0, 30272\n"
      "s_mov_b32 s62, 983040\n"
      "buffer_load_dwordx4 v12, s[20:23], s62 offen offset:0 lds\n"
      "buffer_load_dwordx4 v13, s[20:23], s62 offen offset:1024 lds\n"
      "buffer_load_dwordx4 v14, s[20:23], s62 offen offset:2048 lds\n"
      "buffer_load_dwordx4 v15, s[20:23], s62 offen offset:3072 lds\n"
      "s_mov_b32 m0, 34368\n"
      "s_mov_b32 s62, 1081344\n"
      "buffer_load_dwordx4 v12, s[20:23], s62 offen offset:0 lds\n"
      "buffer_load_dwordx4 v13, s[20:23], s62 offen offset:1024 lds\n"
      "buffer_load_dwordx4 v14, s[20:23], s62 offen offset:2048 lds\n"
      "buffer_load_dwordx4 v15, s[20:23], s62 offen offset:3072 lds\n"
      "s_mov_b32 m0, 128\n"
      "s_mov_b64 exec, 1\n"
      "buffer_load_dword v11, s[8:11], 0 offen lds\n"
      "s_mov_b64 exec, -1\n"
      "s_mov_b32 m0, 81920\n"
      "s_mov_b32 s62, 1179648\n"
      "buffer_load_dwordx4 v12, s[20:23], s62 offen offset:0 lds\n"
      "buffer_load_dwordx4 v13, s[20:23], s62 offen offset:1024 lds\n"
      "buffer_load_dwordx4 v14, s[20:23], s62 offen offset:2048 lds\n"
      "buffer_load_dwordx4 v15, s[20:23], s62 offen offset:3072 lds\n"
      "s_barrier\n"
      "s_mov_b32 m0, 86016\n"
      "s_mov_b32 s62, 1277952\n"
      "buffer_load_dwordx4 v12, s[20:23], s62 offen offset:0 lds\n"
      "buffer_load_dwordx4 v13, s[20:23], s62 offen offset:1024 lds\n"
      "buffer_load_dwordx4 v14, s[20:23], s62 offen offset:2048 lds\n"
      "buffer_load_dwordx4 v15, s[20:23], s62 offen offset:3072 lds\n"
      "s_waitcnt vmcnt(20)\n"
      "s_mov_b32 m0, 90112\n"
      "s_mov_b32 s62, 32768\n"
      "buffer_load_dwordx4 v12, s[20:23], s62 offen offset:0 lds\n"
      "buffer_load_dwordx4 v13, s[20:23], s62 offen offset:1024 lds\n"
      "buffer_load_dwordx4 v14, s[20:23], s62 offen offset:2048 lds\n"
      "buffer_load_dwordx4 v15, s[20:23], s62 offen offset:3072 lds\n"
      "s_waitcnt vmcnt(20)\n"
      "s_mov_b32 m0, 94208\n"
      "s_mov_b32 s62, 131072\n"
      "buffer_load_dwordx4 v12, s[20:23], s62 offen offset:0 lds\n"
      "buffer_load_dwordx4 v13, s[20:23], s62 offen offset:1024 lds\n"
      "buffer_load_dwordx4 v14, s[20:23], s62 offen offset:2048 lds\n"
      "buffer_load_dwordx4 v15, s[20:23], s62 offen offset:3072 lds\n"
      "s_waitcnt vmcnt(20)\n"
      "s_mov_b32 m0, 98304\n"
      "s_mov_b32 s62, 229376\n"
      "buffer_load_dwordx4 v12, s[20:23], s62 offen offset:0 lds\n"
      "buffer_load_dwordx4 v13, s[20:23], s62 offen offset:1024 lds\n"
      "buffer_load_dwordx4 v14, s[20:23], s62 offen offset:2048 lds\n"
      "buffer_load_dwordx4 v15, s[20:23], s62 offen offset:3072 lds\n"
      "s_waitcnt vmcnt(20)\n"
      "s_mov_b32 m0, 102400\n"
      "s_mov_b32 s62, 327680\n"
      "buffer_load_dwordx4 v12, s[20:23], s62 offen offset:0 lds\n"
      "buffer_load_dwordx4 v13, s[20:23], s62 offen offset:1024 lds\n"
      "buffer_load_dwordx4 v14, s[20:23], s62 offen offset:2048 lds\n"
      "buffer_load_dwordx4 v15, s[20:23], s62 offen offset:3072 lds\n"
      "s_waitcnt vmcnt(20)\n"
      "s_mov_b32 m0, 106496\n"
      "s_mov_b32 s62, 425984\n"
      "buffer_load_dwordx4 v12, s[20:23], s62 offen offset:0 lds\n"
      "buffer_load_dwordx4 v13, s[20:23], s62 offen offset:1024 lds\n"
      "buffer_load_dwordx4 v14, s[20:23], s62 offen offset:2048 lds\n"
      "buffer_load_dwordx4 v15, s[20:23], s62 offen offset:3072 lds\n"
      "s_waitcnt vmcnt(20)\n"
      "s_mov_b32 m0, 110592\n"
      "s_mov_b32 s62, 524288\n"
      "buffer_load_dwordx4 v12, s[20:23], s62 offen offset:0 lds\n"
      "buffer_load_dwordx4 v13, s[20:23], s62 offen offset:1024 lds\n"
      "buffer_load_dwordx4 v14, s[20:23], s62 offen offset:2048 lds\n"
      "buffer_load_dwordx4 v15, s[20:23], s62 offen offset:3072 lds\n"
      "s_waitcnt vmcnt(20)\n"
      "s_mov_b32 m0, 114688\n"
      "s_mov_b32 s62, 622592\n"
      "buffer_load_dwordx4 v12, s[20:23], s62 offen offset:0 lds\n"
      "buffer_load_dwordx4 v13, s[20:23], s62 offen offset:1024 lds\n"
      "buffer_load_dwordx4 v14, s[20:23], s62 offen offset:2048 lds\n"
      "buffer_load_dwordx4 v15, s[20:23], s62 offen offset:3072 lds\n"
      "s_waitcnt vmcnt(20)\n"
      "s_mov_b32 m0, 118784\n"
      "s_mov_b32 s62, 720896\n"
      "buffer_load_dwordx4 v12, s[20:23], s62 offen offset:0 lds\n"
      "buffer_load_dwordx4 v13, s[20:23], s62 offen offset:1024 lds\n"
      "buffer_load_dwordx4 v14, s[20:23], s62 offen offset:2048 lds\n"
      "buffer_load_dwordx4 v15, s[20:23], s62 offen offset:3072 lds\n"
      "s_waitcnt vmcnt(20)\n"
      "s_mov_b32 m0, 122880\n"
      "s_mov_b32 s62, 819200\n"
      "buffer_load_dwordx4 v12, s[20:23], s62 offen offset:0 lds\n"
      "buffer_load_dwordx4 v13, s[20:23], s62 offen offset:1024 lds\n"
      "buffer_load_dwordx4 v14, s[20:23], s62 offen offset:2048 lds\n"
      "buffer_load_dwordx4 v15, s[20:23], s62 offen offset:3072 lds\n"
      "s_waitcnt vmcnt(20)\n"
      "s_mov_b32 m0, 126976\n"
      "s_mov_b32 s62, 917504\n"
      "buffer_load_dwordx4 v12, s[20:23], s62 offen offset:0 lds\n"
      "buffer_load_dwordx4 v13, s[20:23], s62 offen offset:1024 lds\n"
      "buffer_load_dwordx4 v14, s[20:23], s62 offen offset:2048 lds\n"
      "buffer_load_dwordx4 v15, s[20:23], s62 offen offset:3072 lds\n"
      "s_waitcnt vmcnt(20)\n"
      "s_mov_b32 m0, 131072\n"
      "s_mov_b32 s62, 1015808\n"
      "buffer_load_dwordx4 v12, s[20:23], s62 offen offset:0 lds\n"
      "buffer_load_dwordx4 v13, s[20:23], s62 offen offset:1024 lds\n"
      "buffer_load_dwordx4 v14, s[20:23], s62 offen offset:2048 lds\n"
      "buffer_load_dwordx4 v15, s[20:23], s62 offen offset:3072 lds\n"
      "s_waitcnt vmcnt(20)\n"
      "s_mov_b32 m0, 135168\n"
      "s_mov_b32 s62, 1114112\n"
      "buffer_load_dwordx4 v12, s[20:23], s62 offen offset:0 lds\n"
      "buffer_load_dwordx4 v13, s[20:23], s62 offen offset:1024 lds\n"
      "buffer_load_dwordx4 v14, s[20:23], s62 offen offset:2048 lds\n"
      "buffer_load_dwordx4 v15, s[20:23], s62 offen offset:3072 lds\n"
      "s_waitcnt vmcnt(20)\n"
      "s_mov_b32 m0, 139264\n"
      "s_mov_b32 s62, 1212416\n"
      "buffer_load_dwordx4 v12, s[20:23], s62 offen offset:0 lds\n"
      "buffer_load_dwordx4 v13, s[20:23], s62 offen offset:1024 lds\n"
      "buffer_load_dwordx4 v14, s[20:23], s62 offen offset:2048 lds\n"
      "buffer_load_dwordx4 v15, s[20:23], s62 offen offset:3072 lds\n"
      "s_waitcnt vmcnt(20)\n"
      "s_mov_b32 m0, 143360\n"
      "s_mov_b32 s62, 1310720\n"
      "buffer_load_dwordx4 v12, s[20:23], s62 offen offset:0 lds\n"
      "buffer_load_dwordx4 v13, s[20:23], s62 offen offset:1024 lds\n"
      "buffer_load_dwordx4 v14, s[20:23], s62 offen offset:2048 lds\n"
      "buffer_load_dwordx4 v15, s[20:23], s62 offen offset:3072 lds\n"
      "s_mov_b32 m0, 132\n"
      "s_mov_b64 exec, 1\n"
      "buffer_load_dword v11, s[8:11], 0 offen lds\n"
      "s_mov_b64 exec, -1\n"
      "s_waitcnt vmcnt(20)\n"
      "s_branch LC5_%=\n"
      ".p2align 3\n"
      "LM3_%=:\n"
      "s_bfe_u32 s45, s2, 0x30003\n"
      "s_lshr_b32 s46, s2, 6\n"
      "s_bfe_u32 s47, s2, 0x20001\n"
      "s_and_b32 s48, s2, 1\n"
      "s_lshl3_add_u32 s49, s48, s45\n"
      "s_lshl1_add_u32 s56, s46, s45\n"
      "s_add_u32 s57, s47, s48\n"
      "s_lshl1_add_u32 s50, s56, s57\n"
      "s_and_b32 s50, s50, 7\n"
      "s_lshr_b32 s56, s47, 1\n"
      "s_and_b32 s57, s47, 1\n"
      "s_mul_i32 s58, s56, 7\n"
      "s_xor_b32 s56, s56, s45\n"
      "s_or_b32 s56, s56, 14\n"
      "s_add_u32 s59, s58, 3\n"
      "s_lshr_b32 s60, s45, 2\n"
      "s_cmp_eq_u32 s60, s57\n"
      "s_cselect_b32 s56, s59, s56\n"
      "s_lshl2_add_u32 s58, s57, s58\n"
      "s_add_u32 s58, s58, s46\n"
      "s_cmp_lt_u32 s46, 3\n"
      "s_cselect_b32 s51, s58, s56\n"
      "s_and_b32 s56, s2, 7\n"
      "s_and_b32 s57, s45, 3\n"
      "s_lshl2_add_u32 s52, s56, s57\n"
      "s_lshr_b32 s53, s2, 5\n"
      "s_waitcnt lgkmcnt(0)\n"
      "s_lshr_b32 s56, s20, 7\n"
      "s_sub_u32 s56, 3, s56\n"
      "s_and_b32 s57, s47, 1\n"
      "s_xor_b32 s56, s56, s57\n"
      "s_andn2_b32 s56, 7, s56\n"
      "s_xor_b32 s53, s53, s56\n"
      "v_mov_b32 v251, 0x0\n"
      "v_mov_b32 v252, 1\n"
      "v_lshl_add_u32 v1, v250, 2, v251\n"
      "v_lshrrev_b32 v2, 5, v250\n"
      "ds_write_b32 v1, v2\n"
      "s_waitcnt lgkmcnt(0)\n"
      "s_load_dwordx2 s[54:55], s[24:25], 0x0\n"
      "s_mul_i32 s56, s2, 5504\n"
      "v_lshlrev_b32 v1, 7, v250\n"
      "s_waitcnt lgkmcnt(0)\n"
      "s_bfm_b64 exec, 43, 0\n"
      "buffer_load_dword v1, s[20:23], s56 offen lds\n"
      "s_mov_b64 exec, -1\n"
      "v_bfe_u32 v20, v250, 4, 1\n"
      "v_and_b32 v21, 3, v250\n"
      "v_bfe_u32 v22, v250, 2, 2\n"
      "v_lshl_add_u32 v20, v20, 2, v21\n"
      "v_lshrrev_b32 v23, 5, v250\n"
      "v_lshl_add_u32 v20, v20, 4, v22\n"
      "s_lshr_b32 s66, s46, 1\n"
      "v_lshlrev_b32 v24, 3, v23\n"
      "v_lshrrev_b32 v22, 1, v22\n"
      "s_lshl_b32 s56, s66, 2\n"
      "v_lshl_add_u32 v20, v20, 7, v24\n"
      "v_xor_b32 v22, s56, v22\n"
      "s_lshl1_add_u32 s57, s50, s66\n"
      "s_lshl_b32 s58, s57, 17\n"
      "v_lshl_add_u32 v30, v22, 4, v20\n"
      "s_lshl_b32 s59, s49, 9\n"
      "v_and_b32 v25, 31, v250\n"
      "s_add_u32 s58, s58, s59\n"
      "v_add_u32 v30, 81920, v30\n"
      "v_lshl_add_u32 v25, v25, 4, s58\n"
      "s_lshl4_add_u32 s57, s57, s49\n"
      "s_lshl_b32 s57, s57, 3\n"
      "v_xor_b32 v31, 16, v30\n"
      "v_xor_b32 v32, 32, v30\n"
      "v_xor_b32 v33, 48, v30\n"
      "v_xor_b32 v34, 64, v30\n"
      "v_xor_b32 v35, 80, v30\n"
      "v_xor_b32 v36, 96, v30\n"
      "v_xor_b32 v37, 112, v30\n"
      "v_lshl_add_u32 v38, v23, 14, v25\n"
      "s_add_u32 s86, s57, 4096\n"
      "v_and_b32 v40, 15, v250\n"
      "v_lshrrev_b32 v41, 4, v250\n"
      "s_movk_i32 s56, 0xfe0\n"
      "s_movk_i32 s57, 0x1ff0\n"
      "v_lshlrev_b32 v42, 3, v41\n"
      "v_lshlrev_b32 v43, v40, s56\n"
      "v_lshlrev_b32 v44, v40, s57\n"
      "s_sub_u32 s80, s44, 4\n"
      "s_lshr_b32 s81, s80, 1\n"
      "v_lshrrev_b32 v43, v42, v43\n"
      "v_lshrrev_b32 v44, v42, v44\n"
      "s_and_b32 s82, s80, 1\n"
      "s_mul_i32 s58, s81, 4608\n"
      "v_bfe_u32 v200, v43, 0, 2\n"
      "v_bfe_u32 v201, v43, 2, 2\n"
      "v_bfe_u32 v202, v43, 4, 2\n"
      "v_bfe_u32 v203, v43, 6, 2\n"
      "v_bfe_u32 v204, v44, 0, 2\n"
      "v_bfe_u32 v205, v44, 2, 2\n"
      "v_bfe_u32 v206, v44, 4, 2\n"
      "v_bfe_u32 v207, v44, 6, 2\n"
      "s_add_u32 s58, s58, 512\n"
      "v_and_b32 v47, 12, v250\n"
      "s_lshl_b32 s59, s82, 8\n"
      "s_mul_i32 s60, s81, 8960\n"
      "v_mul_u32_u24 v200, 0x8001, v200\n"
      "v_mul_u32_u24 v201, 0x8001, v201\n"
      "v_mul_u32_u24 v202, 0x8001, v202\n"
      "v_mul_u32_u24 v203, 0x8001, v203\n"
      "v_mul_u32_u24 v204, 0x8001, v204\n"
      "v_mul_u32_u24 v205, 0x8001, v205\n"
      "v_mul_u32_u24 v206, 0x8001, v206\n"
      "v_mul_u32_u24 v207, 0x8001, v207\n"
      "v_lshl_add_u32 v45, v40, 1, v41\n"
      "s_add_u32 s60, s60, 9752\n"
      "v_and_b32 v46, 3, v250\n"
      "v_sub_u32 v47, v42, v47\n"
      "v_lshlrev_b32 v48, 2, v41\n"
      "s_sub_u32 s60, s60, s59\n"
      "v_and_b32 v200, 0x10001, v200\n"
      "v_and_b32 v201, 0x10001, v201\n"
      "v_and_b32 v202, 0x10001, v202\n"
      "v_and_b32 v203, 0x10001, v203\n"
      "v_and_b32 v204, 0x10001, v204\n"
      "v_and_b32 v205, 0x10001, v205\n"
      "v_and_b32 v206, 0x10001, v206\n"
      "v_and_b32 v207, 0x10001, v207\n"
      "v_lshl_add_u32 v45, v45, 4, s58\n"
      "v_mul_u32_u24 v46, 2240, v46\n"
      "v_lshl_add_u32 v47, v47, 1, s60\n"
      "v_sub_u32 v48, v40, v48\n"
      "s_lshl_b32 s65, s82, 4\n"
      "v_mul_u32_u24 v200, 0x3f80, v200\n"
      "v_mul_u32_u24 v201, 0x3f80, v201\n"
      "v_mul_u32_u24 v202, 0x3f80, v202\n"
      "v_mul_u32_u24 v203, 0x3f80, v203\n"
      "v_mul_u32_u24 v204, 0x3f80, v204\n"
      "v_mul_u32_u24 v205, 0x3f80, v205\n"
      "v_mul_u32_u24 v206, 0x3f80, v206\n"
      "v_mul_u32_u24 v207, 0x3f80, v207\n"
      "v_subrev_u32 v225, s59, v45\n"
      "v_lshl_add_u32 v224, s59, 1, v45\n"
      "v_add_u32 v226, v46, v47\n"
      "v_subrev_u32 v227, s65, v48\n"
      "s_lshr_b32 s56, s2, 4\n"
      "s_lshl_b32 s56, s56, 7\n"
      "s_add_u32 s56, s56, 4096\n"
      "v_and_b32 v20, 15, v250\n"
      "s_mov_b32 s59, 0\n"
      "s_add_u32 s59, s59, 7936\n"
      "v_cmp_gt_u32 vcc, 32, v250\n"
      "v_lshl_add_u32 v20, v20, 3, s56\n"
      "v_lshl_add_u32 v116, v250, 3, s59\n"
      "v_and_b32 v117, 15, v250\n"
      "v_cndmask_b32 v118, v116, v20, vcc\n"
      "v_lshrrev_b32 v116, 4, v250\n"
      "v_lshlrev_b32 v117, 4, v117\n"
      "s_lshl_b32 s60, s2, 8\n"
      "v_lshl_add_u32 v116, v116, 16, v117\n"
      "v_lshrrev_b32 v24, 5, v250\n"
      "v_add_u32 v116, s60, v116\n"
      "v_and_b32 v25, 1, v250\n"
      "s_lshl_b32 s60, s52, 16\n"
      "v_lshlrev_b32 v21, 4, v250\n"
      "s_and_b32 s57, s45, 3\n"
      "v_mul_u32_u24 v24, 4096, v24\n"
      "v_mul_u32_u24 v25, 4600, v25\n"
      "s_and_b32 s58, s2, 7\n"
      "s_mul_i32 s58, s58, 43\n"
      "v_add_u32 v24, v24, v21\n"
      "v_lshl_add_u32 v25, v250, 3, v25\n"
      "s_mul_i32 s57, s57, 352256\n"
      "s_lshr_b32 s58, s58, 3\n"
      "v_lshl_add_u32 v116, v250, 4, s60\n"
      "v_mov_b32 v120, -1\n"
      "v_mov_b32 v121, -1\n"
      "v_mov_b32 v122, -1\n"
      "v_mov_b32 v123, -1\n"
      "v_lshl_add_u32 v39, v250, 7, s57\n"
      "s_lshl_b32 s87, s58, 13\n"
      "v_add_u32 v24, 512, v24\n"
      "v_add_u32 v25, 1024, v25\n"
      "v_mov_b32 v108, 0\n"
      "v_mov_b32 v109, 0\n"
      "v_mov_b32 v110, 0\n"
      "v_mov_b32 v111, 0\n"
      "s_waitcnt lgkmcnt(0)\n"
      "s_add_u32 s54, s54, 1\n"
      "s_addc_u32 s55, s55, 0\n"
      "s_barrier\n"
      "ds_read_b64_tr_b16 v[64:65], v30 offset:0\n"
      "ds_read_b64_tr_b16 v[66:67], v32 offset:512\n"
      "ds_read_b64_tr_b16 v[68:69], v34 offset:1024\n"
      "ds_read_b64_tr_b16 v[70:71], v36 offset:1536\n"
      "ds_read_b64_tr_b16 v[72:73], v31 offset:0\n"
      "ds_read_b64_tr_b16 v[74:75], v33 offset:512\n"
      "ds_read_b64_tr_b16 v[76:77], v35 offset:1024\n"
      "ds_read_b64_tr_b16 v[78:79], v37 offset:1536\n"
      "s_barrier\n"
      "ds_read_b64_tr_b16 v[80:81], v32 offset:0\n"
      "ds_read_b64_tr_b16 v[82:83], v30 offset:512\n"
      "ds_read_b64_tr_b16 v[84:85], v36 offset:1024\n"
      "ds_read_b64_tr_b16 v[86:87], v34 offset:1536\n"
      "ds_read_b64_tr_b16 v[88:89], v33 offset:0\n"
      "ds_read_b64_tr_b16 v[90:91], v31 offset:512\n"
      "ds_read_b64_tr_b16 v[92:93], v37 offset:1024\n"
      "ds_read_b64_tr_b16 v[94:95], v35 offset:1536\n"
      "s_mov_b32 vcc_lo, 0x33333333\n"
      "s_mov_b32 vcc_hi, 0x33333333\n"
      "s_waitcnt lgkmcnt(0)\n"
      "s_barrier\n"
      "v_cndmask_b32_dpp v100, v68, v64, vcc quad_perm:[2,3,0,1] row_mask:0xf bank_mask:0xf\n"
      "v_cndmask_b32_dpp v101, v69, v65, vcc quad_perm:[2,3,0,1] row_mask:0xf bank_mask:0xf\n"
      "v_cndmask_b32_dpp v102, v70, v66, vcc quad_perm:[2,3,0,1] row_mask:0xf bank_mask:0xf\n"
      "v_cndmask_b32_dpp v103, v71, v67, vcc quad_perm:[2,3,0,1] row_mask:0xf bank_mask:0xf\n"
      "s_not_b64 vcc, vcc\n"
      "s_mov_b32 s62, 262144\n"
      "buffer_store_dwordx4 v[100:103], v38, s[24:27], s62 offen sc1\n"
      "v_cndmask_b32_dpp v104, v64, v68, vcc quad_perm:[2,3,0,1] row_mask:0xf bank_mask:0xf\n"
      "v_cndmask_b32_dpp v105, v65, v69, vcc quad_perm:[2,3,0,1] row_mask:0xf bank_mask:0xf\n"
      "v_cndmask_b32_dpp v106, v66, v70, vcc quad_perm:[2,3,0,1] row_mask:0xf bank_mask:0xf\n"
      "v_cndmask_b32_dpp v107, v67, v71, vcc quad_perm:[2,3,0,1] row_mask:0xf bank_mask:0xf\n"
      "s_not_b64 vcc, vcc\n"
      "s_mov_b32 s62, 270336\n"
      "buffer_store_dwordx4 v[104:107], v38, s[24:27], s62 offen sc1\n"
      "s_barrier\n"
      "v_cndmask_b32_dpp v100, v76, v72, vcc quad_perm:[2,3,0,1] row_mask:0xf bank_mask:0xf\n"
      "v_cndmask_b32_dpp v101, v77, v73, vcc quad_perm:[2,3,0,1] row_mask:0xf bank_mask:0xf\n"
      "v_cndmask_b32_dpp v102, v78, v74, vcc quad_perm:[2,3,0,1] row_mask:0xf bank_mask:0xf\n"
      "v_cndmask_b32_dpp v103, v79, v75, vcc quad_perm:[2,3,0,1] row_mask:0xf bank_mask:0xf\n"
      "s_not_b64 vcc, vcc\n"
      "s_mov_b32 s62, 294912\n"
      "buffer_store_dwordx4 v[100:103], v38, s[24:27], s62 offen sc1\n"
      "v_cndmask_b32_dpp v104, v72, v76, vcc quad_perm:[2,3,0,1] row_mask:0xf bank_mask:0xf\n"
      "v_cndmask_b32_dpp v105, v73, v77, vcc quad_perm:[2,3,0,1] row_mask:0xf bank_mask:0xf\n"
      "v_cndmask_b32_dpp v106, v74, v78, vcc quad_perm:[2,3,0,1] row_mask:0xf bank_mask:0xf\n"
      "v_cndmask_b32_dpp v107, v75, v79, vcc quad_perm:[2,3,0,1] row_mask:0xf bank_mask:0xf\n"
      "s_not_b64 vcc, vcc\n"
      "s_mov_b32 s62, 303104\n"
      "buffer_store_dwordx4 v[104:107], v38, s[24:27], s62 offen sc1\n"
      "s_barrier\n"
      "v_cndmask_b32_dpp v100, v84, v80, vcc quad_perm:[2,3,0,1] row_mask:0xf bank_mask:0xf\n"
      "v_cndmask_b32_dpp v101, v85, v81, vcc quad_perm:[2,3,0,1] row_mask:0xf bank_mask:0xf\n"
      "v_cndmask_b32_dpp v102, v86, v82, vcc quad_perm:[2,3,0,1] row_mask:0xf bank_mask:0xf\n"
      "v_cndmask_b32_dpp v103, v87, v83, vcc quad_perm:[2,3,0,1] row_mask:0xf bank_mask:0xf\n"
      "s_not_b64 vcc, vcc\n"
      "s_mov_b32 s62, 327680\n"
      "buffer_store_dwordx4 v[100:103], v38, s[24:27], s62 offen sc1\n"
      "v_cndmask_b32_dpp v104, v80, v84, vcc quad_perm:[2,3,0,1] row_mask:0xf bank_mask:0xf\n"
      "v_cndmask_b32_dpp v105, v81, v85, vcc quad_perm:[2,3,0,1] row_mask:0xf bank_mask:0xf\n"
      "v_cndmask_b32_dpp v106, v82, v86, vcc quad_perm:[2,3,0,1] row_mask:0xf bank_mask:0xf\n"
      "v_cndmask_b32_dpp v107, v83, v87, vcc quad_perm:[2,3,0,1] row_mask:0xf bank_mask:0xf\n"
      "s_not_b64 vcc, vcc\n"
      "s_mov_b32 s62, 335872\n"
      "buffer_store_dwordx4 v[104:107], v38, s[24:27], s62 offen sc1\n"
      "s_barrier\n"
      "v_cndmask_b32_dpp v100, v92, v88, vcc quad_perm:[2,3,0,1] row_mask:0xf bank_mask:0xf\n"
      "v_cndmask_b32_dpp v101, v93, v89, vcc quad_perm:[2,3,0,1] row_mask:0xf bank_mask:0xf\n"
      "v_cndmask_b32_dpp v102, v94, v90, vcc quad_perm:[2,3,0,1] row_mask:0xf bank_mask:0xf\n"
      "v_cndmask_b32_dpp v103, v95, v91, vcc quad_perm:[2,3,0,1] row_mask:0xf bank_mask:0xf\n"
      "s_not_b64 vcc, vcc\n"
      "s_mov_b32 s62, 360448\n"
      "buffer_store_dwordx4 v[100:103], v38, s[24:27], s62 offen sc1\n"
      "v_cndmask_b32_dpp v104, v88, v92, vcc quad_perm:[2,3,0,1] row_mask:0xf bank_mask:0xf\n"
      "v_cndmask_b32_dpp v105, v89, v93, vcc quad_perm:[2,3,0,1] row_mask:0xf bank_mask:0xf\n"
      "v_cndmask_b32_dpp v106, v90, v94, vcc quad_perm:[2,3,0,1] row_mask:0xf bank_mask:0xf\n"
      "v_cndmask_b32_dpp v107, v91, v95, vcc quad_perm:[2,3,0,1] row_mask:0xf bank_mask:0xf\n"
      "s_not_b64 vcc, vcc\n"
      "s_mov_b32 s62, 368640\n"
      "buffer_store_dwordx4 v[104:107], v38, s[24:27], s62 offen sc1\n"
      "s_barrier\n"
      "s_barrier\n"
      "s_waitcnt vmcnt(0)\n"
      "v_mov_b32 v26, s54\n"
      "v_mov_b32 v27, s55\n"
      "v_mov_b32 v28, s86\n"
      "s_mov_b64 exec, 1\n"
      "global_store_dwordx2 v28, v[26:27], s[24:25] sc1\n"
      "s_mov_b64 exec, -1\n"
      "s_lshl4_add_u32 s56, s49, s51\n"
      "s_lshl_b32 s56, s56, 9\n"
      "s_add_u32 s56, s56, 65536\n"
      "v_not_b32 v112, v26\n"
      "v_mov_b32 v113, v112\n"
      "v_mov_b32 v114, v112\n"
      "v_mov_b32 v115, v112\n"
      "s_mov_b32 exec_hi, 0\n"
      "buffer_store_dwordx4 v[112:115], v21, s[24:27], s56 offen sc1\n"
      "s_mov_b64 exec, -1\n"
      "s_cmp_lt_u32 s51, 14\n"
      "s_cbranch_scc1 Ln11_%=\n"
      "s_mov_b32 s62, 2359296\n"
      "buffer_store_dwordx4 v[120:123], v116, s[24:27], s62 offen offset:0 sc1\n"
      "buffer_store_dwordx4 v[120:123], v116, s[24:27], s62 offen offset:1024 sc1\n"
      "buffer_store_dwordx4 v[120:123], v116, s[24:27], s62 offen offset:2048 sc1\n"
      "buffer_store_dwordx4 v[120:123], v116, s[24:27], s62 offen offset:3072 sc1\n"
      "s_mov_b32 s62, 2363392\n"
      "buffer_store_dwordx4 v[120:123], v116, s[24:27], s62 offen offset:0 sc1\n"
      "buffer_store_dwordx4 v[120:123], v116, s[24:27], s62 offen offset:1024 sc1\n"
      "buffer_store_dwordx4 v[120:123], v116, s[24:27], s62 offen offset:2048 sc1\n"
      "buffer_store_dwordx4 v[120:123], v116, s[24:27], s62 offen offset:3072 sc1\n"
      "s_mov_b32 s62, 2367488\n"
      "buffer_store_dwordx4 v[120:123], v116, s[24:27], s62 offen offset:0 sc1\n"
      "buffer_store_dwordx4 v[120:123], v116, s[24:27], s62 offen offset:1024 sc1\n"
      "buffer_store_dwordx4 v[120:123], v116, s[24:27], s62 offen offset:2048 sc1\n"
      "buffer_store_dwordx4 v[120:123], v116, s[24:27], s62 offen offset:3072 sc1\n"
      "s_mov_b32 s62, 2371584\n"
      "buffer_store_dwordx4 v[120:123], v116, s[24:27], s62 offen offset:0 sc1\n"
      "buffer_store_dwordx4 v[120:123], v116, s[24:27], s62 offen offset:1024 sc1\n"
      "buffer_store_dwordx4 v[120:123], v116, s[24:27], s62 offen offset:2048 sc1\n"
      "buffer_store_dwordx4 v[120:123], v116, s[24:27], s62 offen offset:3072 sc1\n"
      "s_mov_b32 s62, 2375680\n"
      "buffer_store_dwordx4 v[120:123], v116, s[24:27], s62 offen offset:0 sc1\n"
      "buffer_store_dwordx4 v[120:123], v116, s[24:27], s62 offen offset:1024 sc1\n"
      "buffer_store_dwordx4 v[120:123], v116, s[24:27], s62 offen offset:2048 sc1\n"
      "buffer_store_dwordx4 v[120:123], v116, s[24:27], s62 offen offset:3072 sc1\n"
      "s_mov_b32 s62, 2379776\n"
      "buffer_store_dwordx4 v[120:123], v116, s[24:27], s62 offen offset:0 sc1\n"
      "buffer_store_dwordx4 v[120:123], v116, s[24:27], s62 offen offset:1024 sc1\n"
      "buffer_store_dwordx4 v[120:123], v116, s[24:27], s62 offen offset:2048 sc1\n"
      "buffer_store_dwordx4 v[120:123], v116, s[24:27], s62 offen offset:3072 sc1\n"
      "s_mov_b32 s62, 2383872\n"
      "buffer_store_dwordx4 v[120:123], v116, s[24:27], s62 offen offset:0 sc1\n"
      "buffer_store_dwordx4 v[120:123], v116, s[24:27], s62 offen offset:1024 sc1\n"
      "buffer_store_dwordx4 v[120:123], v116, s[24:27], s62 offen offset:2048 sc1\n"
      "buffer_store_dwordx4 v[120:123], v116, s[24:27], s62 offen offset:3072 sc1\n"
      "s_mov_b32 s62, 2387968\n"
      "buffer_store_dwordx4 v[120:123], v116, s[24:27], s62 offen offset:0 sc1\n"
      "buffer_store_dwordx4 v[120:123], v116, s[24:27], s62 offen offset:1024 sc1\n"
      "buffer_store_dwordx4 v[120:123], v116, s[24:27], s62 offen offset:2048 sc1\n"
      "buffer_store_dwordx4 v[120:123], v116, s[24:27], s62 offen offset:3072 sc1\n"
      "Ln11_%=:\n"
      "s_barrier\n"
      "s_cmp_lt_u32 s51, 14\n"
      "s_cbranch_scc1 Ln12_%=\n"
      "s_mov_b32 s62, 2392064\n"
      "buffer_store_dwordx4 v[120:123], v116, s[24:27], s62 offen offset:0 sc1\n"
      "buffer_store_dwordx4 v[120:123], v116, s[24:27], s62 offen offset:1024 sc1\n"
      "buffer_store_dwordx4 v[120:123], v116, s[24:27], s62 offen offset:2048 sc1\n"
      "buffer_store_dwordx4 v[120:123], v116, s[24:27], s62 offen offset:3072 sc1\n"
      "s_mov_b32 s62, 2396160\n"
      "buffer_store_dwordx4 v[120:123], v116, s[24:27], s62 offen offset:0 sc1\n"
      "buffer_store_dwordx4 v[120:123], v116, s[24:27], s62 offen offset:1024 sc1\n"
      "buffer_store_dwordx4 v[120:123], v116, s[24:27], s62 offen offset:2048 sc1\n"
      "buffer_store_dwordx4 v[120:123], v116, s[24:27], s62 offen offset:3072 sc1\n"
      "s_mov_b32 s62, 2400256\n"
      "buffer_store_dwordx4 v[120:123], v116, s[24:27], s62 offen offset:0 sc1\n"
      "buffer_store_dwordx4 v[120:123], v116, s[24:27], s62 offen offset:1024 sc1\n"
      "buffer_store_dwordx4 v[120:123], v116, s[24:27], s62 offen offset:2048 sc1\n"
      "buffer_store_dwordx4 v[120:123], v116, s[24:27], s62 offen offset:3072 sc1\n"
      "s_mov_b32 s62, 2404352\n"
      "buffer_store_dwordx4 v[120:123], v116, s[24:27], s62 offen offset:0 sc1\n"
      "buffer_store_dwordx4 v[120:123], v116, s[24:27], s62 offen offset:1024 sc1\n"
      "buffer_store_dwordx4 v[120:123], v116, s[24:27], s62 offen offset:2048 sc1\n"
      "buffer_store_dwordx4 v[120:123], v116, s[24:27], s62 offen offset:3072 sc1\n"
      "s_mov_b32 s62, 2408448\n"
      "buffer_store_dwordx4 v[120:123], v116, s[24:27], s62 offen offset:0 sc1\n"
      "buffer_store_dwordx4 v[120:123], v116, s[24:27], s62 offen offset:1024 sc1\n"
      "buffer_store_dwordx4 v[120:123], v116, s[24:27], s62 offen offset:2048 sc1\n"
      "buffer_store_dwordx4 v[120:123], v116, s[24:27], s62 offen offset:3072 sc1\n"
      "s_mov_b32 s62, 2412544\n"
      "buffer_store_dwordx4 v[120:123], v116, s[24:27], s62 offen offset:0 sc1\n"
      "buffer_store_dwordx4 v[120:123], v116, s[24:27], s62 offen offset:1024 sc1\n"
      "buffer_store_dwordx4 v[120:123], v116, s[24:27], s62 offen offset:2048 sc1\n"
      "buffer_store_dwordx4 v[120:123], v116, s[24:27], s62 offen offset:3072 sc1\n"
      "s_mov_b32 s62, 2416640\n"
      "buffer_store_dwordx4 v[120:123], v116, s[24:27], s62 offen offset:0 sc1\n"
      "buffer_store_dwordx4 v[120:123], v116, s[24:27], s62 offen offset:1024 sc1\n"
      "buffer_store_dwordx4 v[120:123], v116, s[24:27], s62 offen offset:2048 sc1\n"
      "buffer_store_dwordx4 v[120:123], v116, s[24:27], s62 offen offset:3072 sc1\n"
      "s_mov_b32 s62, 2420736\n"
      "buffer_store_dwordx4 v[120:123], v116, s[24:27], s62 offen offset:0 sc1\n"
      "buffer_store_dwordx4 v[120:123], v116, s[24:27], s62 offen offset:1024 sc1\n"
      "buffer_store_dwordx4 v[120:123], v116, s[24:27], s62 offen offset:2048 sc1\n"
      "buffer_store_dwordx4 v[120:123], v116, s[24:27], s62 offen offset:3072 sc1\n"
      "Ln12_%=:\n"
      "s_barrier\n"
      "s_barrier\n"
      "s_cmp_lt_u32 s51, 14\n"
      "s_cbranch_scc1 Ln13_%=\n"
      "s_waitcnt vmcnt(0)\n"
      "s_lshl3_add_u32 s56, s52, 8192\n"
      "v_mov_b32 v28, s56\n"
      "s_mov_b64 exec, 1\n"
      "global_store_dwordx2 v28, v[26:27], s[24:25] sc1\n"
      "s_mov_b64 exec, -1\n"
      "Ln13_%=:\n"
      "s_barrier\n"
      "s_barrier\n"
      "global_load_dwordx2 v[22:23], v20, s[24:25] sc1\n"
      "s_barrier\n"
      "s_barrier\n"
      "s_waitcnt vmcnt(0)\n"
      "v_cmp_ne_u64 vcc, s[54:55], v[22:23]\n"
      "s_cbranch_vccz Ln14_%=\n"
      "s_mov_b32 s68, 65536\n"
      "Lp15_%=:\n"
      "global_load_dwordx2 v[22:23], v20, s[24:25] offset:0 sc1\n"
      "s_sub_u32 s68, s68, 1\n"
      "s_waitcnt vmcnt(0)\n"
      "s_cbranch_scc1 Lq16_%=\n"
      "v_cmp_ne_u64 vcc, s[54:55], v[22:23]\n"
      "s_cbranch_vccz Lq16_%=\n"
      "s_sleep 1\n"
      "s_branch Lp15_%=\n"
      "Lq16_%=:\n"
      "Ln14_%=:\n"
      "s_lshl_b32 s56, s2, 13\n"
      "s_add_u32 s56, s56, 262144\n"
      "s_add_u32 s58, s56, 0x1000\n"
      "buffer_load_dwordx4 v[64:67], v21, s[24:27], s56 offen offset:0 sc1\n"
      "buffer_load_dwordx4 v[68:71], v21, s[24:27], s56 offen offset:1024 sc1\n"
      "buffer_load_dwordx4 v[72:75], v21, s[24:27], s56 offen offset:2048 sc1\n"
      "buffer_load_dwordx4 v[76:79], v21, s[24:27], s56 offen offset:3072 sc1\n"
      "buffer_load_dwordx4 v[80:83], v21, s[24:27], s58 offen offset:0 sc1\n"
      "buffer_load_dwordx4 v[84:87], v21, s[24:27], s58 offen offset:1024 sc1\n"
      "buffer_load_dwordx4 v[88:91], v21, s[24:27], s58 offen offset:2048 sc1\n"
      "buffer_load_dwordx4 v[92:95], v21, s[24:27], s58 offen offset:3072 sc1\n"
      "s_barrier\n"
      "s_barrier\n"
      "ds_write_b128 v24, v[108:111] offset:0\n"
      "s_waitcnt vmcnt(7)\n"
      "ds_write_b128 v25, v[64:67] offset:0\n"
      "s_waitcnt vmcnt(6)\n"
      "ds_write_b128 v25, v[68:71] offset:512\n"
      "s_waitcnt vmcnt(5)\n"
      "ds_write_b128 v25, v[72:75] offset:1024\n"
      "s_waitcnt vmcnt(4)\n"
      "ds_write_b128 v25, v[76:79] offset:1536\n"
      "s_waitcnt vmcnt(3)\n"
      "ds_write_b128 v25, v[80:83] offset:2048\n"
      "s_waitcnt vmcnt(2)\n"
      "ds_write_b128 v25, v[84:87] offset:2560\n"
      "s_waitcnt vmcnt(1)\n"
      "ds_write_b128 v25, v[88:91] offset:3072\n"
      "s_waitcnt vmcnt(0)\n"
      "ds_write_b128 v25, v[92:95] offset:3584\n"
      "s_mov_b32 s68, 65536\n"
      "Lp17_%=:\n"
      "global_load_dwordx2 v[22:23], v118, s[24:25] offset:0 sc1\n"
      "s_sub_u32 s68, s68, 1\n"
      "s_waitcnt vmcnt(0)\n"
      "s_cbranch_scc1 Lq18_%=\n"
      "v_cmp_ne_u64 vcc, s[54:55], v[22:23]\n"
      "s_cbranch_vccz Lq18_%=\n"
      "s_sleep 1\n"
      "s_branch Lp17_%=\n"
      "Lq18_%=:\n"
      "s_branch LC5_%=\n"
      ".p2align 3\n"
      "LK4_%=:\n"
      "s_bfe_u32 s45, s2, 0x30003\n"
      "s_lshr_b32 s46, s2, 6\n"
      "s_bfe_u32 s47, s2, 0x20001\n"
      "s_and_b32 s48, s2, 1\n"
      "s_lshl3_add_u32 s49, s48, s45\n"
      "s_lshl1_add_u32 s56, s46, s45\n"
      "s_add_u32 s57, s47, s48\n"
      "s_lshl1_add_u32 s50, s56, s57\n"
      "s_and_b32 s50, s50, 7\n"
      "s_lshr_b32 s56, s47, 1\n"
      "s_and_b32 s57, s47, 1\n"
      "s_mul_i32 s58, s56, 7\n"
      "s_xor_b32 s56, s56, s45\n"
      "s_or_b32 s56, s56, 14\n"
      "s_add_u32 s59, s58, 3\n"
      "s_lshr_b32 s60, s45, 2\n"
      "s_cmp_eq_u32 s60, s57\n"
      "s_cselect_b32 s56, s59, s56\n"
      "s_lshl2_add_u32 s58, s57, s58\n"
      "s_add_u32 s58, s58, s46\n"
      "s_cmp_lt_u32 s46, 3\n"
      "s_cselect_b32 s51, s58, s56\n"
      "s_and_b32 s56, s2, 7\n"
      "s_and_b32 s57, s45, 3\n"
      "s_lshl2_add_u32 s52, s56, s57\n"
      "s_lshr_b32 s53, s2, 5\n"
      "s_waitcnt lgkmcnt(0)\n"
      "s_lshr_b32 s56, s20, 7\n"
      "s_sub_u32 s56, 3, s56\n"
      "s_and_b32 s57, s47, 1\n"
      "s_xor_b32 s56, s56, s57\n"
      "s_andn2_b32 s56, 7, s56\n"
      "s_xor_b32 s53, s53, s56\n"
      "v_mov_b32 v251, 0x0\n"
      "v_mov_b32 v252, 1\n"
      "s_and_b32 s66, s44, 1\n"
      "s_lshl1_add_u32 s56, s2, s66\n"
      "s_lshl_b32 s56, s56, 1\n"
      "v_mov_b32 v1, s56\n"
      "s_mul_i32 s85, s66, 1312\n"
      "s_add_u32 s85, s85, 27648\n"
      "s_waitcnt lgkmcnt(0)\n"
      "global_load_ushort v2, v1, s[40:41]\n"
      "s_load_dwordx2 s[54:55], s[24:25], 0x0\n"
      "s_add_u32 s57, s85, 8\n"
      "v_mov_b32 v23, s57\n"
      "v_and_b32 v20, 15, v250\n"
      "v_and_b32 v21, 48, v250\n"
      "v_and_b32 v27, 6, v20\n"
      "v_and_b32 v28, 1, v20\n"
      "v_and_b32 v25, 4, v20\n"
      "v_and_b32 v26, 3, v20\n"
      "v_lshl_add_u32 v27, v27, 4, v28\n"
      "v_lshl_add_u32 v25, v25, 4, v26\n"
      "v_lshrrev_b32 v28, 1, v21\n"
      "v_and_b32 v22, 8, v20\n"
      "v_add3_u32 v25, v25, v21, 64\n"
      "v_add_u32 v27, v27, v28\n"
      "v_cmp_eq_u32 vcc, 0, v22\n"
      "v_add_u32 v25, 192, v25\n"
      "v_add_u32 v27, 384, v27\n"
      "v_mov_b32 v29, 8\n"
      "v_cndmask_b32 v25, v27, v25, vcc\n"
      "v_lshrrev_b32 v30, 2, v21\n"
      "s_add_u32 s56, s85, 10\n"
      "v_and_b32 v24, 7, v20\n"
      "v_lshl_add_u32 v5, v25, 1, s56\n"
      "v_cndmask_b32 v29, 4, v29, vcc\n"
      "v_lshl_add_u32 v30, v20, 4, v30\n"
      "v_lshl_add_u32 v24, v22, 4, v24\n"
      "v_add_u32 v6, v5, v29\n"
      "v_add_u32 v14, 512, v30\n"
      "v_add_u32 v15, 513, v30\n"
      "v_add_u32 v16, 514, v30\n"
      "v_add_u32 v17, 515, v30\n"
      "v_lshl_add_u32 v24, v21, 1, v24\n"
      "v_add_u32 v7, v6, v29\n"
      "v_min_u32 v14, 641, v14\n"
      "v_min_u32 v15, 641, v15\n"
      "v_min_u32 v16, 641, v16\n"
      "v_min_u32 v17, 641, v17\n"
      "v_lshl_add_u32 v4, v24, 1, s56\n"
      "v_add_u32 v8, v7, v29\n"
      "v_lshl_add_u32 v14, v14, 1, s56\n"
      "v_lshl_add_u32 v15, v15, 1, s56\n"
      "v_lshl_add_u32 v16, v16, 1, s56\n"
      "v_lshl_add_u32 v17, v17, 1, s56\n"
      "v_mov_b32 v28, 0\n"
      "v_and_b32 v9, 15, v250\n"
      "s_mul_i32 s58, s66, 8960\n"
      "s_add_u32 s58, s58, 9728\n"
      "s_mul_i32 s59, s66, 32832\n"
      "v_mul_u32_u24 v9, 288, v9\n"
      "v_and_b32 v10, 48, v250\n"
      "s_add_u32 s59, s59, 512\n"
      "v_lshl_add_u32 v3, v250, 4, s58\n"
      "v_mov_b32 v28, 0\n"
      "v_add3_u32 v9, v9, v10, s59\n"
      "s_waitcnt lgkmcnt(0)\n"
      "s_add_u32 s54, s54, 1\n"
      "s_addc_u32 s55, s55, 0\n"
      "s_barrier\n"
      "s_setprio 2\n"
      "ds_read_b128 v[32:35], v9 offset:0\n"
      "ds_read_b128 v[36:39], v9 offset:64\n"
      "ds_read_b128 v[40:43], v9 offset:128\n"
      "ds_read_b128 v[44:47], v9 offset:192\n"
      "ds_read_b128 v[48:51], v9 offset:16416\n"
      "ds_read_b128 v[52:55], v9 offset:16480\n"
      "ds_read_b128 v[56:59], v9 offset:16544\n"
      "ds_read_b128 v[60:63], v9 offset:16608\n"
      "ds_read_b128 v[104:107], v9 offset:4608\n"
      "ds_read_b128 v[108:111], v9 offset:4672\n"
      "ds_read_b128 v[112:115], v9 offset:4736\n"
      "ds_read_b128 v[116:119], v9 offset:4800\n"
      "ds_read_b128 v[120:123], v9 offset:21024\n"
      "ds_read_b128 v[124:127], v9 offset:21088\n"
      "ds_read_b128 v[128:131], v9 offset:21152\n"
      "ds_read_b128 v[132:135], v9 offset:21216\n"
      "ds_read_b128 v[136:139], v9 offset:9216\n"
      "ds_read_b128 v[140:143], v9 offset:9280\n"
      "ds_read_b128 v[144:147], v9 offset:9344\n"
      "ds_read_b128 v[148:151], v9 offset:9408\n"
      "ds_read_b128 v[152:155], v9 offset:25632\n"
      "ds_read_b128 v[156:159], v9 offset:25696\n"
      "ds_read_b128 v[160:163], v9 offset:25760\n"
      "ds_read_b128 v[164:167], v9 offset:25824\n"
      "ds_read_b128 v[168:171], v9 offset:13824\n"
      "ds_read_b128 v[172:175], v9 offset:13888\n"
      "ds_read_b128 v[176:179], v9 offset:13952\n"
      "ds_read_b128 v[180:183], v9 offset:14016\n"
      "ds_read_b128 v[184:187], v9 offset:30240\n"
      "ds_read_b128 v[188:191], v9 offset:30304\n"
      "ds_read_b128 v[192:195], v9 offset:30368\n"
      "ds_read_b128 v[196:199], v9 offset:30432\n"
      "s_setprio 0\n"
      "s_waitcnt lgkmcnt(0)\n"
      "s_barrier\n"
      "s_setprio 1\n"
      "v_mfma_f32_16x16x32_bf16 v[232:235], v[32:35], v[104:107], 0\n"
      "v_mfma_f32_16x16x32_bf16 v[232:235], v[36:39], v[108:111], v[232:235]\n"
      "v_mfma_f32_16x16x32_bf16 v[232:235], v[40:43], v[112:115], v[232:235]\n"
      "v_mfma_f32_16x16x32_bf16 v[232:235], v[44:47], v[116:119], v[232:235]\n"
      "v_mfma_f32_16x16x32_bf16 v[232:235], v[48:51], v[120:123], v[232:235]\n"
      "v_mfma_f32_16x16x32_bf16 v[232:235], v[52:55], v[124:127], v[232:235]\n"
      "v_mfma_f32_16x16x32_bf16 v[232:235], v[56:59], v[128:131], v[232:235]\n"
      "v_mfma_f32_16x16x32_bf16 v[232:235], v[60:63], v[132:135], v[232:235]\n"
      "s_setprio 0\n"
      "s_setprio 1\n"
      "v_mfma_f32_16x16x32_bf16 v[236:239], v[32:35], v[136:139], 0\n"
      "v_mfma_f32_16x16x32_bf16 v[236:239], v[36:39], v[140:143], v[236:239]\n"
      "v_mfma_f32_16x16x32_bf16 v[236:239], v[40:43], v[144:147], v[236:239]\n"
      "v_mfma_f32_16x16x32_bf16 v[236:239], v[44:47], v[148:151], v[236:239]\n"
      "v_mfma_f32_16x16x32_bf16 v[236:239], v[48:51], v[152:155], v[236:239]\n"
      "v_mfma_f32_16x16x32_bf16 v[236:239], v[52:55], v[156:159], v[236:239]\n"
      "v_mfma_f32_16x16x32_bf16 v[236:239], v[56:59], v[160:163], v[236:239]\n"
      "v_mfma_f32_16x16x32_bf16 v[236:239], v[60:63], v[164:167], v[236:239]\n"
      "s_setprio 0\n"
      "s_barrier\n"
      "s_setprio 1\n"
      "v_mfma_f32_16x16x32_bf16 v[240:243], v[32:35], v[168:171], 0\n"
      "v_mfma_f32_16x16x32_bf16 v[240:243], v[36:39], v[172:175], v[240:243]\n"
      "v_mfma_f32_16x16x32_bf16 v[240:243], v[40:43], v[176:179], v[240:243]\n"
      "v_mfma_f32_16x16x32_bf16 v[240:243], v[44:47], v[180:183], v[240:243]\n"
      "v_mfma_f32_16x16x32_bf16 v[240:243], v[48:51], v[184:187], v[240:243]\n"
      "v_mfma_f32_16x16x32_bf16 v[240:243], v[52:55], v[188:191], v[240:243]\n"
      "v_mfma_f32_16x16x32_bf16 v[240:243], v[56:59], v[192:195], v[240:243]\n"
      "v_mfma_f32_16x16x32_bf16 v[240:243], v[60:63], v[196:199], v[240:243]\n"
      "s_setprio 0\n"
      "s_waitcnt vmcnt(0)\n"
      "v_lshlrev_b32 v2, 16, v2\n"
      "v_cmp_eq_u32 vcc, 0, v250\n"
      "s_nop 1\n"
      "v_cndmask_b32 v2, 0, v2, vcc\n"
      "v_add_f32 v232, v232, v2\n"
      "v_cvt_pk_bf16_f32 v130, v232, v233\n"
      "v_cvt_pk_bf16_f32 v131, v234, v235\n"
      "v_cvt_pk_bf16_f32 v132, v236, v237\n"
      "v_cvt_pk_bf16_f32 v133, v238, v239\n"
      "v_cvt_pk_bf16_f32 v134, v240, v241\n"
      "v_cvt_pk_bf16_f32 v135, v242, v243\n"
      "s_barrier\n"
      "ds_write_b16 v23, v28\n"
      "ds_write_b16 v4, v130 offset:0\n"
      "ds_write_b16_d16_hi v4, v130 offset:16\n"
      "ds_write_b16 v4, v131 offset:32\n"
      "ds_write_b16_d16_hi v4, v131 offset:48\n"
      "ds_write_b16 v5, v132 offset:0\n"
      "ds_write_b16_d16_hi v6, v132 offset:0\n"
      "s_barrier\n"
      "ds_write_b16 v7, v133 offset:0\n"
      "ds_write_b16_d16_hi v8, v133 offset:0\n"
      "ds_write_b16 v14, v134 offset:0\n"
      "ds_write_b16_d16_hi v15, v134 offset:0\n"
      "ds_write_b16 v16, v135 offset:0\n"
      "ds_write_b16_d16_hi v17, v135 offset:0\n"
      "s_barrier\n"
      "v_lshlrev_b32 v20, 3, v250\n"
      "s_sub_u32 s56, s85, 34038\n"
      "v_sub_u32 v20, 1055, v20\n"
      "s_add_u32 s57, s85, 8\n"
      "v_cvt_f32_i32 v21, v20\n"
      "v_cmp_lt_i32 vcc, 1028, v20\n"
      "v_add_f32 v24, 0x00000000, v21\n"
      "v_add_f32 v25, 0xbf800000, v21\n"
      "v_mov_b32 v23, s57\n"
      "v_cvt_pk_bf16_f32 v24, v24, v25\n"
      "v_add_f32 v26, 0xc0000000, v21\n"
      "v_lshrrev_b32 v25, 16, v24\n"
      "v_and_b32 v24, 0xffff, v24\n"
      "v_add_f32 v27, 0xc0400000, v21\n"
      "v_lshl_add_u32 v64, v24, 1, s56\n"
      "v_cvt_pk_bf16_f32 v26, v26, v27\n"
      "v_cndmask_b32 v64, v64, v23, vcc\n"
      "v_cmp_lt_i32 vcc, 1029, v20\n"
      "v_lshl_add_u32 v65, v25, 1, s56\n"
      "v_lshrrev_b32 v27, 16, v26\n"
      "v_cndmask_b32 v65, v65, v23, vcc\n"
      "v_and_b32 v26, 0xffff, v26\n"
      "v_cmp_lt_i32 vcc, 1030, v20\n"
      "v_lshl_add_u32 v66, v26, 1, s56\n"
      "v_add_f32 v28, 0xc0800000, v21\n"
      "v_add_f32 v29, 0xc0a00000, v21\n"
      "v_cndmask_b32 v66, v66, v23, vcc\n"
      "v_cmp_lt_i32 vcc, 1031, v20\n"
      "v_cvt_pk_bf16_f32 v28, v28, v29\n"
      "v_lshl_add_u32 v67, v27, 1, s56\n"
      "v_lshrrev_b32 v29, 16, v28\n"
      "v_cndmask_b32 v67, v67, v23, vcc\n"
      "s_barrier\n"
      "v_and_b32 v28, 0xffff, v28\n"
      "v_cmp_lt_i32 vcc, 1032, v20\n"
      "v_lshl_add_u32 v68, v28, 1, s56\n"
      "v_add_f32 v30, 0xc0c00000, v21\n"
      "v_add_f32 v31, 0xc0e00000, v21\n"
      "v_cndmask_b32 v68, v68, v23, vcc\n"
      "v_cmp_lt_i32 vcc, 1033, v20\n"
      "v_cvt_pk_bf16_f32 v30, v30, v31\n"
      "v_lshl_add_u32 v69, v29, 1, s56\n"
      "v_lshrrev_b32 v31, 16, v30\n"
      "v_cndmask_b32 v69, v69, v23, vcc\n"
      "v_and_b32 v30, 0xffff, v30\n"
      "v_cmp_lt_i32 vcc, 1034, v20\n"
      "v_lshl_add_u32 v70, v30, 1, s56\n"
      "v_add_f32 v32, 0xc1000000, v21\n"
      "v_add_f32 v33, 0xc1100000, v21\n"
      "v_cndmask_b32 v70, v70, v23, vcc\n"
      "v_cmp_lt_i32 vcc, 1035, v20\n"
      "v_cvt_pk_bf16_f32 v32, v32, v33\n"
      "v_lshl_add_u32 v71, v31, 1, s56\n"
      "v_lshrrev_b32 v33, 16, v32\n"
      "v_cndmask_b32 v71, v71, v23, vcc\n"
      "v_and_b32 v32, 0xffff, v32\n"
      "v_cmp_lt_i32 vcc, 1036, v20\n"
      "v_lshl_add_u32 v72, v32, 1, s56\n"
      "v_add_f32 v34, 0xc1200000, v21\n"
      "v_add_f32 v35, 0xc1300000, v21\n"
      "v_cndmask_b32 v72, v72, v23, vcc\n"
      "v_cmp_lt_i32 vcc, 1037, v20\n"
      "v_cvt_pk_bf16_f32 v34, v34, v35\n"
      "v_lshl_add_u32 v73, v33, 1, s56\n"
      "v_lshrrev_b32 v35, 16, v34\n"
      "s_barrier\n"
      "v_cndmask_b32 v73, v73, v23, vcc\n"
      "v_and_b32 v34, 0xffff, v34\n"
      "v_cmp_lt_i32 vcc, 1038, v20\n"
      "v_lshl_add_u32 v74, v34, 1, s56\n"
      "v_lshl_add_u32 v75, v35, 1, s56\n"
      "v_cndmask_b32 v74, v74, v23, vcc\n"
      "v_cmp_lt_i32 vcc, 1039, v20\n"
      "v_lshl_add_u32 v22, v20, 1, s85\n"
      "s_nop 0\n"
      "v_cndmask_b32 v75, v75, v23, vcc\n"
      "v_lshlrev_b32 v20, 3, v250\n"
      "s_sub_u32 s56, s85, 34038\n"
      "v_sub_u32 v20, 543, v20\n"
      "s_add_u32 s57, s85, 8\n"
      "v_cvt_f32_i32 v21, v20\n"
      "v_lshl_add_u32 v22, v20, 1, s85\n"
      "v_add_f32 v24, 0x00000000, v21\n"
      "v_add_f32 v25, 0xbf800000, v21\n"
      "v_add_f32 v26, 0xc0000000, v21\n"
      "v_add_f32 v27, 0xc0400000, v21\n"
      "v_add_f32 v28, 0xc0800000, v21\n"
      "v_add_f32 v29, 0xc0a00000, v21\n"
      "v_add_f32 v30, 0xc0c00000, v21\n"
      "v_add_f32 v31, 0xc0e00000, v21\n"
      "v_add_f32 v32, 0xc1000000, v21\n"
      "v_add_f32 v33, 0xc1100000, v21\n"
      "v_add_f32 v34, 0xc1200000, v21\n"
      "v_add_f32 v35, 0xc1300000, v21\n"
      "v_max_f32 v24, 0x43000000, v24\n"
      "v_max_f32 v25, 0x43000000, v25\n"
      "v_max_f32 v26, 0x43000000, v26\n"
      "v_max_f32 v27, 0x43000000, v27\n"
      "v_max_f32 v28, 0x43000000, v28\n"
      "s_barrier\n"
      "v_max_f32 v29, 0x43000000, v29\n"
      "v_max_f32 v30, 0x43000000, v30\n"
      "v_max_f32 v31, 0x43000000, v31\n"
      "v_max_f32 v32, 0x43000000, v32\n"
      "v_max_f32 v33, 0x43000000, v33\n"
      "v_max_f32 v34, 0x43000000, v34\n"
      "v_max_f32 v35, 0x43000000, v35\n"
      "v_cvt_pk_bf16_f32 v24, v24, v25\n"
      "v_cvt_pk_bf16_f32 v26, v26, v27\n"
      "v_cvt_pk_bf16_f32 v28, v28, v29\n"
      "v_cvt_pk_bf16_f32 v30, v30, v31\n"
      "v_cvt_pk_bf16_f32 v32, v32, v33\n"
      "v_cvt_pk_bf16_f32 v34, v34, v35\n"
      "v_lshrrev_b32 v25, 16, v24\n"
      "v_lshrrev_b32 v27, 16, v26\n"
      "v_lshrrev_b32 v29, 16, v28\n"
      "v_lshrrev_b32 v31, 16, v30\n"
      "v_lshrrev_b32 v33, 16, v32\n"
      "v_lshrrev_b32 v35, 16, v34\n"
      "v_and_b32 v24, 0xffff, v24\n"
      "v_and_b32 v26, 0xffff, v26\n"
      "v_and_b32 v28, 0xffff, v28\n"
      "v_and_b32 v30, 0xffff, v30\n"
      "v_and_b32 v32, 0xffff, v32\n"
      "v_and_b32 v34, 0xffff, v34\n"
      "v_lshl_add_u32 v76, v24, 1, s56\n"
      "v_lshl_add_u32 v77, v25, 1, s56\n"
      "v_lshl_add_u32 v78, v26, 1, s56\n"
      "v_lshl_add_u32 v79, v27, 1, s56\n"
      "v_lshl_add_u32 v80, v28, 1, s56\n"
      "v_lshl_add_u32 v81, v29, 1, s56\n"
      "v_lshl_add_u32 v82, v30, 1, s56\n"
      "s_barrier\n"
      "v_lshl_add_u32 v83, v31, 1, s56\n"
      "v_lshl_add_u32 v84, v32, 1, s56\n"
      "v_lshl_add_u32 v85, v33, 1, s56\n"
      "v_lshl_add_u32 v86, v34, 1, s56\n"
      "v_lshl_add_u32 v87, v35, 1, s56\n"
      "v_add_u32 v24, 10, v22\n"
      "v_add_u32 v25, 8, v22\n"
      "v_add_u32 v26, 6, v22\n"
      "v_add_u32 v27, 4, v22\n"
      "v_add_u32 v28, 2, v22\n"
      "v_add_u32 v29, 0, v22\n"
      "v_add_u32 v30, -2, v22\n"
      "v_add_u32 v31, -4, v22\n"
      "v_add_u32 v32, -6, v22\n"
      "v_add_u32 v33, -8, v22\n"
      "v_add_u32 v34, -10, v22\n"
      "v_add_u32 v35, -12, v22\n"
      "v_mov_b32 v23, s57\n"
      "v_min_i32 v76, v76, v24\n"
      "v_min_i32 v77, v77, v25\n"
      "v_min_i32 v78, v78, v26\n"
      "v_min_i32 v79, v79, v27\n"
      "v_min_i32 v80, v80, v28\n"
      "v_min_i32 v81, v81, v29\n"
      "v_min_i32 v82, v82, v30\n"
      "v_min_i32 v83, v83, v31\n"
      "v_min_i32 v84, v84, v32\n"
      "v_min_i32 v85, v85, v33\n"
      "v_min_i32 v86, v86, v34\n"
      "v_min_i32 v87, v87, v35\n"
      "v_lshlrev_b32 v20, 3, v250\n"
      "s_sub_u32 s56, s85, 34038\n"
      "s_barrier\n"
      "v_sub_u32 v20, 31, v20\n"
      "s_add_u32 s57, s85, 8\n"
      "v_lshl_add_u32 v22, v20, 1, s85\n"
      "v_mov_b32 v23, s57\n"
      "v_add_u32 v88, 10, v22\n"
      "v_add_u32 v89, 8, v22\n"
      "v_add_u32 v90, 6, v22\n"
      "v_add_u32 v91, 4, v22\n"
      "v_add_u32 v92, 2, v22\n"
      "v_add_u32 v93, 0, v22\n"
      "v_add_u32 v94, -2, v22\n"
      "v_add_u32 v95, -4, v22\n"
      "v_add_u32 v96, -6, v22\n"
      "v_add_u32 v97, -8, v22\n"
      "v_add_u32 v98, -10, v22\n"
      "v_add_u32 v99, -12, v22\n"
      "v_cvt_f32_i32 v21, v20\n"
      "v_max_i32 v88, v88, v23\n"
      "v_max_i32 v89, v89, v23\n"
      "v_max_i32 v90, v90, v23\n"
      "v_max_i32 v91, v91, v23\n"
      "v_max_i32 v92, v92, v23\n"
      "v_max_i32 v93, v93, v23\n"
      "v_max_i32 v94, v94, v23\n"
      "v_max_i32 v95, v95, v23\n"
      "v_max_i32 v96, v96, v23\n"
      "v_max_i32 v97, v97, v23\n"
      "v_max_i32 v98, v98, v23\n"
      "v_max_i32 v99, v99, v23\n"
      "v_and_b32 v40, 15, v250\n"
      "v_lshrrev_b32 v41, 4, v250\n"
      "s_movk_i32 s56, 0xfe0\n"
      "s_barrier\n"
      "s_movk_i32 s57, 0x1ff0\n"
      "v_lshlrev_b32 v42, 3, v41\n"
      "v_lshlrev_b32 v43, v40, s56\n"
      "v_lshlrev_b32 v44, v40, s57\n"
      "s_sub_u32 s80, s44, 4\n"
      "s_lshr_b32 s81, s80, 1\n"
      "v_lshrrev_b32 v43, v42, v43\n"
      "v_lshrrev_b32 v44, v42, v44\n"
      "s_and_b32 s82, s80, 1\n"
      "s_mul_i32 s58, s81, 4608\n"
      "v_bfe_u32 v200, v43, 0, 2\n"
      "v_bfe_u32 v201, v43, 2, 2\n"
      "v_bfe_u32 v202, v43, 4, 2\n"
      "v_bfe_u32 v203, v43, 6, 2\n"
      "v_bfe_u32 v204, v44, 0, 2\n"
      "v_bfe_u32 v205, v44, 2, 2\n"
      "v_bfe_u32 v206, v44, 4, 2\n"
      "v_bfe_u32 v207, v44, 6, 2\n"
      "s_add_u32 s58, s58, 512\n"
      "v_and_b32 v47, 12, v250\n"
      "s_lshl_b32 s59, s82, 8\n"
      "s_mul_i32 s60, s81, 8960\n"
      "v_mul_u32_u24 v200, 0x8001, v200\n"
      "v_mul_u32_u24 v201, 0x8001, v201\n"
      "v_mul_u32_u24 v202, 0x8001, v202\n"
      "v_mul_u32_u24 v203, 0x8001, v203\n"
      "v_mul_u32_u24 v204, 0x8001, v204\n"
      "v_mul_u32_u24 v205, 0x8001, v205\n"
      "v_mul_u32_u24 v206, 0x8001, v206\n"
      "v_mul_u32_u24 v207, 0x8001, v207\n"
      "v_lshl_add_u32 v45, v40, 1, v41\n"
      "s_add_u32 s60, s60, 9752\n"
      "s_barrier\n"
      "v_and_b32 v46, 3, v250\n"
      "v_sub_u32 v47, v42, v47\n"
      "v_lshlrev_b32 v48, 2, v41\n"
      "s_sub_u32 s60, s60, s59\n"
      "v_and_b32 v200, 0x10001, v200\n"
      "v_and_b32 v201, 0x10001, v201\n"
      "v_and_b32 v202, 0x10001, v202\n"
      "v_and_b32 v203, 0x10001, v203\n"
      "v_and_b32 v204, 0x10001, v204\n"
      "v_and_b32 v205, 0x10001, v205\n"
      "v_and_b32 v206, 0x10001, v206\n"
      "v_and_b32 v207, 0x10001, v207\n"
      "v_lshl_add_u32 v45, v45, 4, s58\n"
      "v_mul_u32_u24 v46, 2240, v46\n"
      "v_lshl_add_u32 v47, v47, 1, s60\n"
      "v_sub_u32 v48, v40, v48\n"
      "s_lshl_b32 s65, s82, 4\n"
      "v_mul_u32_u24 v200, 0x3f80, v200\n"
      "v_mul_u32_u24 v201, 0x3f80, v201\n"
      "v_mul_u32_u24 v202, 0x3f80, v202\n"
      "v_mul_u32_u24 v203, 0x3f80, v203\n"
      "v_mul_u32_u24 v204, 0x3f80, v204\n"
      "v_mul_u32_u24 v205, 0x3f80, v205\n"
      "v_mul_u32_u24 v206, 0x3f80, v206\n"
      "v_mul_u32_u24 v207, 0x3f80, v207\n"
      "v_subrev_u32 v225, s59, v45\n"
      "v_lshl_add_u32 v224, s59, 1, v45\n"
      "v_add_u32 v226, v46, v47\n"
      "v_subrev_u32 v227, s65, v48\n"
      "s_barrier\n"
      "ds_read_u16 v104, v64\n"
      "ds_read_u16 v105, v65\n"
      "ds_read_u16 v106, v66\n"
      "ds_read_u16 v107, v67\n"
      "ds_read_u16 v108, v68\n"
      "ds_read_u16 v109, v69\n"
      "ds_read_u16 v110, v70\n"
      "ds_read_u16 v111, v71\n"
      "ds_read_u16 v112, v72\n"
      "ds_read_u16 v113, v73\n"
      "ds_read_u16 v114, v74\n"
      "ds_read_u16 v116, v76\n"
      "ds_read_u16 v117, v77\n"
      "ds_read_u16 v118, v78\n"
      "ds_read_u16 v119, v79\n"
      "ds_read_u16 v120, v80\n"
      "ds_read_u16 v121, v81\n"
      "ds_read_u16 v122, v82\n"
      "ds_read_u16 v123, v83\n"
      "ds_read_u16 v124, v84\n"
      "ds_read_u16 v125, v85\n"
      "ds_read_u16 v126, v86\n"
      "s_waitcnt lgkmcnt(15)\n"
      "v_lshl_or_b32 v140, v108, 16, v107\n"
      "s_waitcnt lgkmcnt(15)\n"
      "v_lshl_or_b32 v141, v110, 16, v109\n"
      "s_waitcnt lgkmcnt(13)\n"
      "v_lshl_or_b32 v142, v112, 16, v111\n"
      "s_waitcnt lgkmcnt(11)\n"
      "v_lshl_or_b32 v143, v114, 16, v113\n"
      "v_lshl_or_b32 v144, v107, 16, v106\n"
      "v_lshl_or_b32 v145, v109, 16, v108\n"
      "v_lshl_or_b32 v146, v111, 16, v110\n"
      "v_lshl_or_b32 v147, v113, 16, v112\n"
      "v_lshl_or_b32 v148, v106, 16, v105\n"
      "v_lshl_or_b32 v149, v108, 16, v107\n"
      "v_lshl_or_b32 v150, v110, 16, v109\n"
      "v_lshl_or_b32 v151, v112, 16, v111\n"
      "v_lshl_or_b32 v152, v105, 16, v104\n"
      "v_lshl_or_b32 v153, v107, 16, v106\n"
      "v_lshl_or_b32 v154, v109, 16, v108\n"
      "v_lshl_or_b32 v155, v111, 16, v110\n"
      "s_barrier\n"
      "ds_read_u16 v128, v88\n"
      "ds_read_u16 v129, v89\n"
      "ds_read_u16 v130, v90\n"
      "ds_read_u16 v131, v91\n"
      "ds_read_u16 v132, v92\n"
      "ds_read_u16 v133, v93\n"
      "ds_read_u16 v134, v94\n"
      "ds_read_u16 v135, v95\n"
      "ds_read_u16 v136, v96\n"
      "ds_read_u16 v137, v97\n"
      "ds_read_u16 v138, v98\n"
      "s_waitcnt lgkmcnt(15)\n"
      "v_lshl_or_b32 v156, v120, 16, v119\n"
      "s_waitcnt lgkmcnt(15)\n"
      "v_lshl_or_b32 v157, v122, 16, v121\n"
      "s_waitcnt lgkmcnt(13)\n"
      "v_lshl_or_b32 v158, v124, 16, v123\n"
      "s_waitcnt lgkmcnt(11)\n"
      "v_lshl_or_b32 v159, v126, 16, v125\n"
      "v_lshl_or_b32 v160, v119, 16, v118\n"
      "v_lshl_or_b32 v161, v121, 16, v120\n"
      "v_lshl_or_b32 v162, v123, 16, v122\n"
      "v_lshl_or_b32 v163, v125, 16, v124\n"
      "v_lshl_or_b32 v164, v118, 16, v117\n"
      "v_lshl_or_b32 v165, v120, 16, v119\n"
      "v_lshl_or_b32 v166, v122, 16, v121\n"
      "v_lshl_or_b32 v167, v124, 16, v123\n"
      "v_lshl_or_b32 v168, v117, 16, v116\n"
      "v_lshl_or_b32 v169, v119, 16, v118\n"
      "v_lshl_or_b32 v170, v121, 16, v120\n"
      "v_lshl_or_b32 v171, v123, 16, v122\n"
      "s_barrier\n"
      "s_waitcnt lgkmcnt(6)\n"
      "v_lshl_or_b32 v172, v132, 16, v131\n"
      "s_waitcnt lgkmcnt(4)\n"
      "v_lshl_or_b32 v173, v134, 16, v133\n"
      "s_waitcnt lgkmcnt(2)\n"
      "v_lshl_or_b32 v174, v136, 16, v135\n"
      "s_waitcnt lgkmcnt(0)\n"
      "v_lshl_or_b32 v175, v138, 16, v137\n"
      "v_lshl_or_b32 v176, v131, 16, v130\n"
      "v_lshl_or_b32 v177, v133, 16, v132\n"
      "v_lshl_or_b32 v178, v135, 16, v134\n"
      "v_lshl_or_b32 v179, v137, 16, v136\n"
      "v_lshl_or_b32 v180, v130, 16, v129\n"
      "v_lshl_or_b32 v181, v132, 16, v131\n"
      "v_lshl_or_b32 v182, v134, 16, v133\n"
      "v_lshl_or_b32 v183, v136, 16, v135\n"
      "v_lshl_or_b32 v184, v129, 16, v128\n"
      "v_lshl_or_b32 v185, v131, 16, v130\n"
      "v_lshl_or_b32 v186, v133, 16, v132\n"
      "v_lshl_or_b32 v187, v135, 16, v134\n"
      "s_barrier\n"
      "ds_write_b128 v3, v[140:143] offset:0\n"
      "ds_write_b128 v3, v[144:147] offset:2240\n"
      "ds_write_b128 v3, v[148:151] offset:4480\n"
      "ds_write_b128 v3, v[152:155] offset:6720\n"
      "ds_write_b128 v3, v[156:159] offset:1024\n"
      "ds_write_b128 v3, v[160:163] offset:3264\n"
      "ds_write_b128 v3, v[164:167] offset:5504\n"
      "ds_write_b128 v3, v[168:171] offset:7744\n"
      "s_mov_b64 exec, 63\n"
      "ds_write_b128 v3, v[172:175] offset:2048\n"
      "ds_write_b128 v3, v[176:179] offset:4288\n"
      "ds_write_b128 v3, v[180:183] offset:6528\n"
      "ds_write_b128 v3, v[184:187] offset:8768\n"
      "s_mov_b64 exec, -1\n"
      ".p2align 3\n"
      "LC5_%=:\n"
      "s_mul_i32 s56, s81, 1312\n"
      "s_add_u32 s56, s56, 28672\n"
      "v_lshl_add_u32 v229, v227, 2, s56\n"
      "s_waitcnt lgkmcnt(0)\n"
      "s_barrier\n"
      "s_setprio 1\n"
      "ds_read_b64 v[96:97], v226 offset:2048\n"
      "ds_read_b64 v[98:99], v226 offset:2056\n"
      "ds_read_b128 v[32:35], v225 offset:480\n"
      "ds_read_b64 v[100:101], v226 offset:1536\n"
      "ds_read_b64 v[102:103], v226 offset:1544\n"
      "ds_read_b128 v[36:39], v225 offset:992\n"
      "ds_read_b64 v[104:105], v226 offset:1024\n"
      "ds_read_b64 v[106:107], v226 offset:1032\n"
      "ds_read_b128 v[40:43], v225 offset:1504\n"
      "ds_read_b64 v[108:109], v226 offset:512\n"
      "ds_read_b64 v[110:111], v226 offset:520\n"
      "ds_read_b128 v[44:47], v225 offset:2016\n"
      "ds_read_b128 v[48:51], v225 offset:2528\n"
      "ds_read_b128 v[52:55], v225 offset:3040\n"
      "ds_read_b128 v[56:59], v225 offset:3552\n"
      "ds_read_b128 v[60:63], v225 offset:4064\n"
      "s_waitcnt lgkmcnt(13)\n"
      "v_mfma_f32_16x16x32_bf16 v[0:3], v[96:99], v[32:35], 0\n"
      "ds_read_b64 v[128:129], v226 offset:0\n"
      "s_waitcnt lgkmcnt(12)\n"
      "v_mfma_f32_16x16x32_bf16 v[4:7], v[100:103], v[32:35], 0\n"
      "ds_read_b64 v[130:131], v226 offset:8\n"
      "s_waitcnt lgkmcnt(10)\n"
      "v_mfma_f32_16x16x32_bf16 v[8:11], v[104:107], v[32:35], 0\n"
      "ds_read_b64 v[112:113], v226 offset:1984\n"
      "s_waitcnt lgkmcnt(8)\n"
      "v_mfma_f32_16x16x32_bf16 v[12:15], v[108:111], v[32:35], 0\n"
      "ds_read_b64 v[114:115], v226 offset:1992\n"
      "v_mfma_f32_16x16x32_bf16 v[16:19], v[108:111], v[36:39], 0\n"
      "ds_read_b128 v[64:67], v225 offset:416\n"
      "v_mfma_f32_16x16x32_bf16 v[12:15], v[104:107], v[36:39], v[12:15]\n"
      "ds_read_b64 v[116:117], v226 offset:1472\n"
      "v_mfma_f32_16x16x32_bf16 v[8:11], v[100:103], v[36:39], v[8:11]\n"
      "ds_read_b64 v[118:119], v226 offset:1480\n"
      "v_mfma_f32_16x16x32_bf16 v[4:7], v[96:99], v[36:39], v[4:7]\n"
      "ds_read_b128 v[68:71], v225 offset:928\n"
      "v_mfma_f32_16x16x32_bf16 v[8:11], v[96:99], v[40:43], v[8:11]\n"
      "ds_read_b64 v[120:121], v226 offset:960\n"
      "v_mfma_f32_16x16x32_bf16 v[12:15], v[100:103], v[40:43], v[12:15]\n"
      "ds_read_b64 v[122:123], v226 offset:968\n"
      "v_mfma_f32_16x16x32_bf16 v[16:19], v[104:107], v[40:43], v[16:19]\n"
      "ds_read_b128 v[72:75], v225 offset:1440\n"
      "v_mfma_f32_16x16x32_bf16 v[20:23], v[108:111], v[40:43], 0\n"
      "ds_read_b64 v[124:125], v226 offset:448\n"
      "s_waitcnt lgkmcnt(15)\n"
      "v_mfma_f32_16x16x32_bf16 v[24:27], v[108:111], v[44:47], 0\n"
      "ds_read_b64 v[126:127], v226 offset:456\n"
      "v_mfma_f32_16x16x32_bf16 v[20:23], v[104:107], v[44:47], v[20:23]\n"
      "ds_read_b128 v[76:79], v225 offset:1952\n"
      "v_mfma_f32_16x16x32_bf16 v[16:19], v[100:103], v[44:47], v[16:19]\n"
      "ds_read_b128 v[80:83], v225 offset:2464\n"
      "v_mfma_f32_16x16x32_bf16 v[12:15], v[96:99], v[44:47], v[12:15]\n"
      "ds_read_b128 v[84:87], v225 offset:2976\n"
      "s_waitcnt lgkmcnt(15)\n"
      "v_mfma_f32_16x16x32_bf16 v[16:19], v[96:99], v[48:51], v[16:19]\n"
      "ds_read_b128 v[88:91], v225 offset:3488\n"
      "v_mfma_f32_16x16x32_bf16 v[20:23], v[100:103], v[48:51], v[20:23]\n"
      "ds_read_b128 v[92:95], v225 offset:4000\n"
      "v_mfma_f32_16x16x32_bf16 v[24:27], v[104:107], v[48:51], v[24:27]\n"
      "v_mfma_f32_16x16x32_bf16 v[28:31], v[108:111], v[48:51], 0\n"
      "s_waitcnt lgkmcnt(15)\n"
      "v_mfma_f32_16x16x32_bf16 v[28:31], v[104:107], v[52:55], v[28:31]\n"
      "v_mfma_f32_16x16x32_bf16 v[24:27], v[100:103], v[52:55], v[24:27]\n"
      "v_mfma_f32_16x16x32_bf16 v[20:23], v[96:99], v[52:55], v[20:23]\n"
      "s_waitcnt lgkmcnt(15)\n"
      "v_mfma_f32_16x16x32_bf16 v[24:27], v[96:99], v[56:59], v[24:27]\n"
      "v_mfma_f32_16x16x32_bf16 v[28:31], v[100:103], v[56:59], v[28:31]\n"
      "s_waitcnt lgkmcnt(15)\n"
      "v_mfma_f32_16x16x32_bf16 v[28:31], v[96:99], v[60:63], v[28:31]\n"
      "s_cmp_lg_u32 s82, 0\n"
      "s_cbranch_scc1 Ln19_%=\n"
      "s_waitcnt lgkmcnt(15)\n"
      "v_mfma_f32_16x16x32_bf16 v[16:19], v[128:131], v[32:35], v[16:19]\n"
      "v_mfma_f32_16x16x32_bf16 v[20:23], v[128:131], v[36:39], v[20:23]\n"
      "v_mfma_f32_16x16x32_bf16 v[24:27], v[128:131], v[40:43], v[24:27]\n"
      "v_mfma_f32_16x16x32_bf16 v[28:31], v[128:131], v[44:47], v[28:31]\n"
      "Ln19_%=:\n"
      "s_waitcnt lgkmcnt(13)\n"
      "v_mfma_f32_16x16x32_bf16 v[0:3], v[112:115], v[64:67], v[0:3]\n"
      "ds_read_b64 v[96:97], v226 offset:1920\n"
      "ds_read_b64 v[98:99], v226 offset:1928\n"
      "s_waitcnt lgkmcnt(13)\n"
      "v_mfma_f32_16x16x32_bf16 v[4:7], v[116:119], v[64:67], v[4:7]\n"
      "ds_read_b128 v[32:35], v225 offset:352\n"
      "ds_read_b64 v[100:101], v226 offset:1408\n"
      "s_waitcnt lgkmcnt(12)\n"
      "v_mfma_f32_16x16x32_bf16 v[8:11], v[120:123], v[64:67], v[8:11]\n"
      "ds_read_b64 v[102:103], v226 offset:1416\n"
      "ds_read_b128 v[36:39], v225 offset:864\n"
      "s_waitcnt lgkmcnt(11)\n"
      "v_mfma_f32_16x16x32_bf16 v[12:15], v[124:127], v[64:67], v[12:15]\n"
      "ds_read_b64 v[104:105], v226 offset:896\n"
      "ds_read_b64 v[106:107], v226 offset:904\n"
      "v_mfma_f32_16x16x32_bf16 v[16:19], v[124:127], v[68:71], v[16:19]\n"
      "ds_read_b128 v[40:43], v225 offset:1376\n"
      "ds_read_b64 v[108:109], v226 offset:384\n"
      "v_mfma_f32_16x16x32_bf16 v[12:15], v[120:123], v[68:71], v[12:15]\n"
      "ds_read_b64 v[110:111], v226 offset:392\n"
      "ds_read_b128 v[44:47], v225 offset:1888\n"
      "v_mfma_f32_16x16x32_bf16 v[8:11], v[116:119], v[68:71], v[8:11]\n"
      "ds_read_b128 v[48:51], v225 offset:2400\n"
      "ds_read_b128 v[52:55], v225 offset:2912\n"
      "v_mfma_f32_16x16x32_bf16 v[4:7], v[112:115], v[68:71], v[4:7]\n"
      "ds_read_b128 v[56:59], v225 offset:3424\n"
      "ds_read_b128 v[60:63], v225 offset:3936\n"
      "v_mfma_f32_16x16x32_bf16 v[8:11], v[112:115], v[72:75], v[8:11]\n"
      "ds_read_b32 v208, v229 offset:12\n"
      "ds_read_b32 v209, v229 offset:8\n"
      "v_mfma_f32_16x16x32_bf16 v[12:15], v[116:119], v[72:75], v[12:15]\n"
      "ds_read_b32 v210, v229 offset:4\n"
      "ds_read_b32 v211, v229 offset:0\n"
      "v_mfma_f32_16x16x32_bf16 v[16:19], v[120:123], v[72:75], v[16:19]\n"
      "ds_read_b32 v212, v229 offset:76\n"
      "ds_read_b32 v213, v229 offset:72\n"
      "v_mfma_f32_16x16x32_bf16 v[20:23], v[124:127], v[72:75], v[20:23]\n"
      "ds_read_b32 v214, v229 offset:68\n"
      "ds_read_b32 v215, v229 offset:64\n"
      "s_waitcnt lgkmcnt(15)\n"
      "v_mfma_f32_16x16x32_bf16 v[24:27], v[124:127], v[76:79], v[24:27]\n"
      "ds_read_b32 v216, v229 offset:140\n"
      "ds_read_b32 v217, v229 offset:136\n"
      "v_mfma_f32_16x16x32_bf16 v[20:23], v[120:123], v[76:79], v[20:23]\n"
      "ds_read_b32 v218, v229 offset:132\n"
      "ds_read_b32 v219, v229 offset:128\n"
      "v_mfma_f32_16x16x32_bf16 v[16:19], v[116:119], v[76:79], v[16:19]\n"
      "ds_read_b32 v220, v229 offset:204\n"
      "ds_read_b32 v221, v229 offset:200\n"
      "v_mfma_f32_16x16x32_bf16 v[12:15], v[112:115], v[76:79], v[12:15]\n"
      "ds_read_b32 v222, v229 offset:196\n"
      "ds_read_b32 v223, v229 offset:192\n"
      "s_waitcnt lgkmcnt(15)\n"
      "v_mfma_f32_16x16x32_bf16 v[16:19], v[112:115], v[80:83], v[16:19]\n"
      "v_mfma_f32_16x16x32_bf16 v[20:23], v[116:119], v[80:83], v[20:23]\n"
      "v_mfma_f32_16x16x32_bf16 v[24:27], v[120:123], v[80:83], v[24:27]\n"
      "v_mfma_f32_16x16x32_bf16 v[28:31], v[124:127], v[80:83], v[28:31]\n"
      "s_waitcnt lgkmcnt(15)\n"
      "v_mfma_f32_16x16x32_bf16 v[28:31], v[120:123], v[84:87], v[28:31]\n"
      "v_mfma_f32_16x16x32_bf16 v[24:27], v[116:119], v[84:87], v[24:27]\n"
      "v_mfma_f32_16x16x32_bf16 v[20:23], v[112:115], v[84:87], v[20:23]\n"
      "s_waitcnt lgkmcnt(15)\n"
      "v_mfma_f32_16x16x32_bf16 v[24:27], v[112:115], v[88:91], v[24:27]\n"
      "v_mfma_f32_16x16x32_bf16 v[28:31], v[116:119], v[88:91], v[28:31]\n"
      "s_waitcnt lgkmcnt(15)\n"
      "v_mfma_f32_16x16x32_bf16 v[28:31], v[112:115], v[92:95], v[28:31]\n"
      "s_waitcnt lgkmcnt(15)\n"
      "v_mfma_f32_16x16x32_bf16 v[0:3], v[96:99], v[32:35], v[0:3]\n"
      "ds_read_b64 v[112:113], v226 offset:1856\n"
      "s_waitcnt lgkmcnt(15)\n"
      "v_mfma_f32_16x16x32_bf16 v[4:7], v[100:103], v[32:35], v[4:7]\n"
      "ds_read_b64 v[114:115], v226 offset:1864\n"
      "s_waitcnt lgkmcnt(15)\n"
      "v_mfma_f32_16x16x32_bf16 v[8:11], v[104:107], v[32:35], v[8:11]\n"
      "ds_read_b128 v[64:67], v225 offset:288\n"
      "s_waitcnt lgkmcnt(15)\n"
      "v_mfma_f32_16x16x32_bf16 v[12:15], v[108:111], v[32:35], v[12:15]\n"
      "ds_read_b64 v[116:117], v226 offset:1344\n"
      "v_mfma_f32_16x16x32_bf16 v[16:19], v[108:111], v[36:39], v[16:19]\n"
      "ds_read_b64 v[118:119], v226 offset:1352\n"
      "v_mfma_f32_16x16x32_bf16 v[12:15], v[104:107], v[36:39], v[12:15]\n"
      "ds_read_b128 v[68:71], v225 offset:800\n"
      "v_mfma_f32_16x16x32_bf16 v[8:11], v[100:103], v[36:39], v[8:11]\n"
      "ds_read_b64 v[120:121], v226 offset:832\n"
      "v_mfma_f32_16x16x32_bf16 v[4:7], v[96:99], v[36:39], v[4:7]\n"
      "ds_read_b64 v[122:123], v226 offset:840\n"
      "v_mfma_f32_16x16x32_bf16 v[8:11], v[96:99], v[40:43], v[8:11]\n"
      "ds_read_b128 v[72:75], v225 offset:1312\n"
      "v_mfma_f32_16x16x32_bf16 v[12:15], v[100:103], v[40:43], v[12:15]\n"
      "ds_read_b64 v[124:125], v226 offset:320\n"
      "v_mfma_f32_16x16x32_bf16 v[16:19], v[104:107], v[40:43], v[16:19]\n"
      "ds_read_b64 v[126:127], v226 offset:328\n"
      "v_mfma_f32_16x16x32_bf16 v[20:23], v[108:111], v[40:43], v[20:23]\n"
      "ds_read_b128 v[76:79], v225 offset:1824\n"
      "s_waitcnt lgkmcnt(15)\n"
      "v_mfma_f32_16x16x32_bf16 v[24:27], v[108:111], v[44:47], v[24:27]\n"
      "ds_read_b128 v[80:83], v225 offset:2336\n"
      "v_mfma_f32_16x16x32_bf16 v[20:23], v[104:107], v[44:47], v[20:23]\n"
      "ds_read_b128 v[84:87], v225 offset:2848\n"
      "v_mfma_f32_16x16x32_bf16 v[16:19], v[100:103], v[44:47], v[16:19]\n"
      "ds_read_b128 v[88:91], v225 offset:3360\n"
      "v_mfma_f32_16x16x32_bf16 v[12:15], v[96:99], v[44:47], v[12:15]\n"
      "ds_read_b128 v[92:95], v225 offset:3872\n"
      "s_waitcnt lgkmcnt(15)\n"
      "v_mfma_f32_16x16x32_bf16 v[16:19], v[96:99], v[48:51], v[16:19]\n"
      "ds_read_b128 v[132:135], v224 offset:480\n"
      "v_mfma_f32_16x16x32_bf16 v[20:23], v[100:103], v[48:51], v[20:23]\n"
      "ds_read_b128 v[136:139], v224 offset:1504\n"
      "v_mfma_f32_16x16x32_bf16 v[24:27], v[104:107], v[48:51], v[24:27]\n"
      "ds_read_b128 v[140:143], v224 offset:464\n"
      "v_mfma_f32_16x16x32_bf16 v[28:31], v[108:111], v[48:51], v[28:31]\n"
      "ds_read_b128 v[144:147], v224 offset:1488\n"
      "s_waitcnt lgkmcnt(15)\n"
      "v_mfma_f32_16x16x32_bf16 v[28:31], v[104:107], v[52:55], v[28:31]\n"
      "v_mfma_f32_16x16x32_bf16 v[24:27], v[100:103], v[52:55], v[24:27]\n"
      "v_mfma_f32_16x16x32_bf16 v[20:23], v[96:99], v[52:55], v[20:23]\n"
      "s_waitcnt lgkmcnt(15)\n"
      "v_mfma_f32_16x16x32_bf16 v[24:27], v[96:99], v[56:59], v[24:27]\n"
      "v_mfma_f32_16x16x32_bf16 v[28:31], v[100:103], v[56:59], v[28:31]\n"
      "s_waitcnt lgkmcnt(15)\n"
      "v_mfma_f32_16x16x32_bf16 v[28:31], v[96:99], v[60:63], v[28:31]\n"
      "s_waitcnt lgkmcnt(15)\n"
      "v_mfma_f32_16x16x32_bf16 v[0:3], v[112:115], v[64:67], v[0:3]\n"
      "s_waitcnt lgkmcnt(15)\n"
      "v_mfma_f32_16x16x32_bf16 v[4:7], v[116:119], v[64:67], v[4:7]\n"
      "s_waitcnt lgkmcnt(12)\n"
      "v_mfma_f32_16x16x32_bf16 v[8:11], v[120:123], v[64:67], v[8:11]\n"
      "s_waitcnt lgkmcnt(9)\n"
      "v_mfma_f32_16x16x32_bf16 v[12:15], v[124:127], v[64:67], v[12:15]\n"
      "s_waitcnt lgkmcnt(0)\n"
      "v_mfma_f32_16x16x32_bf16 v[148:151], v[132:135], v[200:203], 0\n"
      "s_mov_b64 exec, 1\n"
      "ds_add_u32 v251, v252 offset:28\n"
      "s_mov_b64 exec, -1\n"
      "v_mfma_f32_16x16x32_bf16 v[156:159], v[136:139], v[200:203], 0\n"
      "s_mul_i32 s58, s81, 8960\n"
      "s_add_u32 s58, s58, 9728\n"
      "v_mfma_f32_16x16x32_bf16 v[152:155], v[140:143], v[204:207], 0\n"
      "s_lshl_b32 s59, s82, 12\n"
      "s_add_u32 s60, s58, s59\n"
      "v_mfma_f32_16x16x32_bf16 v[160:163], v[144:147], v[204:207], 0\n"
      "v_lshl_add_u32 v230, v250, 4, s60\n"
      "s_xor_b32 s59, s59, 0x1000\n"
      "v_mfma_f32_16x16x32_bf16 v[16:19], v[124:127], v[68:71], v[16:19]\n"
      "s_add_u32 s60, s58, s59\n"
      "v_lshl_add_u32 v231, v250, 4, s60\n"
      "v_mfma_f32_16x16x32_bf16 v[12:15], v[120:123], v[68:71], v[12:15]\n"
      "v_bfe_u32 v232, v250, 2, 3\n"
      "v_lshrrev_b32 v233, 5, v250\n"
      "v_mfma_f32_16x16x32_bf16 v[8:11], v[116:119], v[68:71], v[8:11]\n"
      "v_lshlrev_b32 v233, 6, v233\n"
      "v_lshl_add_u32 v232, v232, 16, v233\n"
      "v_mfma_f32_16x16x32_bf16 v[4:7], v[112:115], v[68:71], v[4:7]\n"
      "v_and_b32 v233, 3, v250\n"
      "v_lshl_add_u32 v232, v233, 4, v232\n"
      "v_mfma_f32_16x16x32_bf16 v[8:11], v[112:115], v[72:75], v[8:11]\n"
      "s_lshl1_add_u32 s58, s2, s81\n"
      "s_lshl_b32 s58, s58, 7\n"
      "v_mfma_f32_16x16x32_bf16 v[12:15], v[116:119], v[72:75], v[12:15]\n"
      "s_mul_i32 s65, s82, 1048576\n"
      "s_add_u32 s65, s65, s58\n"
      "v_mfma_f32_16x16x32_bf16 v[16:19], v[120:123], v[72:75], v[16:19]\n"
      "ds_read_b32 v253, v251 offset:28\n"
      "v_mfma_f32_16x16x32_bf16 v[20:23], v[124:127], v[72:75], v[20:23]\n"
      "v_cmp_le_i32 vcc, 0, v227\n"
      "v_cvt_pk_bf16_f32 v132, v148, v152\n"
      "v_mfma_f32_16x16x32_bf16 v[24:27], v[124:127], v[76:79], v[24:27]\n"
      "v_cndmask_b32 v208, 0, v208, vcc\n"
      "v_mfma_f32_16x16x32_bf16 v[20:23], v[120:123], v[76:79], v[20:23]\n"
      "v_cmp_le_i32 vcc, 1, v227\n"
      "v_cvt_pk_bf16_f32 v133, v149, v153\n"
      "v_mfma_f32_16x16x32_bf16 v[16:19], v[116:119], v[76:79], v[16:19]\n"
      "v_cndmask_b32 v209, 0, v209, vcc\n"
      "v_mfma_f32_16x16x32_bf16 v[12:15], v[112:115], v[76:79], v[12:15]\n"
      "v_cmp_le_i32 vcc, 2, v227\n"
      "v_cvt_pk_bf16_f32 v134, v150, v154\n"
      "v_mfma_f32_16x16x32_bf16 v[16:19], v[112:115], v[80:83], v[16:19]\n"
      "v_cndmask_b32 v210, 0, v210, vcc\n"
      "v_mfma_f32_16x16x32_bf16 v[20:23], v[116:119], v[80:83], v[20:23]\n"
      "v_cmp_le_i32 vcc, 3, v227\n"
      "v_cvt_pk_bf16_f32 v135, v151, v155\n"
      "v_mfma_f32_16x16x32_bf16 v[24:27], v[120:123], v[80:83], v[24:27]\n"
      "v_cndmask_b32 v211, 0, v211, vcc\n"
      "v_mfma_f32_16x16x32_bf16 v[28:31], v[124:127], v[80:83], v[28:31]\n"
      "v_cmp_le_i32 vcc, -16, v227\n"
      "v_cvt_pk_bf16_f32 v136, v156, v160\n"
      "v_mfma_f32_16x16x32_bf16 v[28:31], v[120:123], v[84:87], v[28:31]\n"
      "v_cndmask_b32 v212, 0, v212, vcc\n"
      "v_mfma_f32_16x16x32_bf16 v[24:27], v[116:119], v[84:87], v[24:27]\n"
      "v_cmp_le_i32 vcc, -15, v227\n"
      "v_cvt_pk_bf16_f32 v137, v157, v161\n"
      "v_mfma_f32_16x16x32_bf16 v[20:23], v[112:115], v[84:87], v[20:23]\n"
      "v_cndmask_b32 v213, 0, v213, vcc\n"
      "v_mfma_f32_16x16x32_bf16 v[24:27], v[112:115], v[88:91], v[24:27]\n"
      "v_cmp_le_i32 vcc, -14, v227\n"
      "v_cvt_pk_bf16_f32 v138, v158, v162\n"
      "v_mfma_f32_16x16x32_bf16 v[28:31], v[116:119], v[88:91], v[28:31]\n"
      "v_cndmask_b32 v214, 0, v214, vcc\n"
      "v_mfma_f32_16x16x32_bf16 v[28:31], v[112:115], v[92:95], v[28:31]\n"
      "v_cmp_le_i32 vcc, -13, v227\n"
      "v_cvt_pk_bf16_f32 v139, v159, v163\n"
      "s_nop 0\n"
      "v_cndmask_b32 v215, 0, v215, vcc\n"
      "v_mfma_f32_16x16x32_bf16 v[16:19], v[132:135], v[208:211], v[16:19]\n"
      "s_nop 0\n"
      "v_mfma_f32_16x16x32_bf16 v[20:23], v[132:135], v[212:215], v[20:23]\n"
      "v_mfma_f32_16x16x32_bf16 v[24:27], v[132:135], v[216:219], v[24:27]\n"
      "v_mfma_f32_16x16x32_bf16 v[28:31], v[132:135], v[220:223], v[28:31]\n"
      "v_mfma_f32_16x16x32_bf16 v[24:27], v[136:139], v[208:211], v[24:27]\n"
      "v_mfma_f32_16x16x32_bf16 v[28:31], v[136:139], v[212:215], v[28:31]\n"
      "s_waitcnt lgkmcnt(0)\n"
      "v_readfirstlane_b32 s67, v253\n"
      "s_cmp_lt_u32 s67, 4\n"
      "s_cbranch_scc1 Lw20_%=\n"
      "Lr21_%=:\n"
      "s_cmp_lg_u32 s82, 0\n"
      "s_cbranch_scc1 Lh23_%=\n"
      "ds_write_b128 v230, v[16:19] offset:0\n"
      "ds_write_b128 v230, v[20:23] offset:1024\n"
      "ds_write_b128 v230, v[24:27] offset:2048\n"
      "ds_write_b128 v230, v[28:31] offset:3072\n"
      "s_branch Lj24_%=\n"
      "Lh23_%=:\n"
      "ds_write_b128 v230, v[0:3] offset:0\n"
      "ds_write_b128 v230, v[4:7] offset:1024\n"
      "ds_write_b128 v230, v[8:11] offset:2048\n"
      "ds_write_b128 v230, v[12:15] offset:3072\n"
      "v_mov_b64 v[0:1], v[16:17]\n"
      "v_mov_b64 v[2:3], v[18:19]\n"
      "v_mov_b64 v[4:5], v[20:21]\n"
      "v_mov_b64 v[6:7], v[22:23]\n"
      "v_mov_b64 v[8:9], v[24:25]\n"
      "v_mov_b64 v[10:11], v[26:27]\n"
      "v_mov_b64 v[12:13], v[28:29]\n"
      "v_mov_b64 v[14:15], v[30:31]\n"
      "Lj24_%=:\n"
      "s_waitcnt lgkmcnt(0)\n"
      "s_mov_b64 exec, 1\n"
      "ds_add_u32 v251, v252 offset:32\n"
      "s_mov_b64 exec, -1\n"
      "ds_read_b32 v253, v251 offset:32\n"
      "s_waitcnt lgkmcnt(0)\n"
      "v_readfirstlane_b32 s67, v253\n"
      "s_cmp_lt_u32 s67, 4\n"
      "s_cbranch_scc1 Lw25_%=\n"
      "Lr26_%=:\n"
      "ds_read_b128 v[32:35], v231 offset:0\n"
      "ds_read_b128 v[36:39], v231 offset:1024\n"
      "ds_read_b128 v[40:43], v231 offset:2048\n"
      "ds_read_b128 v[44:47], v231 offset:3072\n"
      "s_waitcnt lgkmcnt(3)\n"
      "v_add_f32 v0, v0, v32\n"
      "v_add_f32 v1, v1, v33\n"
      "v_add_f32 v2, v2, v34\n"
      "v_add_f32 v3, v3, v35\n"
      "s_waitcnt lgkmcnt(2)\n"
      "v_add_f32 v4, v4, v36\n"
      "v_add_f32 v5, v5, v37\n"
      "v_add_f32 v6, v6, v38\n"
      "v_add_f32 v7, v7, v39\n"
      "v_cvt_pk_bf16_f32 v64, v0, v1\n"
      "v_cvt_pk_bf16_f32 v65, v2, v3\n"
      "v_cvt_pk_bf16_f32 v66, v4, v5\n"
      "v_cvt_pk_bf16_f32 v67, v6, v7\n"
      "v_min_u32 v64, -2, v64\n"
      "v_min_u32 v65, -2, v65\n"
      "v_min_u32 v66, -2, v66\n"
      "v_min_u32 v67, -2, v67\n"
      "s_add_u32 s56, s65, 2359296\n"
      "v_permlane16_swap_b32 v64, v66\n"
      "v_permlane16_swap_b32 v65, v67\n"
      "buffer_store_dwordx4 v[64:67], v232, s[24:27], s56 offen sc1\n"
      "s_waitcnt lgkmcnt(1)\n"
      "v_add_f32 v8, v8, v40\n"
      "v_add_f32 v9, v9, v41\n"
      "v_add_f32 v10, v10, v42\n"
      "v_add_f32 v11, v11, v43\n"
      "s_waitcnt lgkmcnt(0)\n"
      "v_add_f32 v12, v12, v44\n"
      "v_add_f32 v13, v13, v45\n"
      "v_add_f32 v14, v14, v46\n"
      "v_add_f32 v15, v15, v47\n"
      "v_cvt_pk_bf16_f32 v68, v8, v9\n"
      "v_cvt_pk_bf16_f32 v69, v10, v11\n"
      "v_cvt_pk_bf16_f32 v70, v12, v13\n"
      "v_cvt_pk_bf16_f32 v71, v14, v15\n"
      "v_min_u32 v68, -2, v68\n"
      "v_min_u32 v69, -2, v69\n"
      "v_min_u32 v70, -2, v70\n"
      "v_min_u32 v71, -2, v71\n"
      "s_add_u32 s56, s65, 2883584\n"
      "v_permlane16_swap_b32 v68, v70\n"
      "v_permlane16_swap_b32 v69, v71\n"
      "buffer_store_dwordx4 v[68:71], v232, s[24:27], s56 offen sc1\n"
      "s_lshl2_add_u32 s56, s53, s80\n"
      "v_and_b32 v23, 56, v250\n"
      "v_and_b32 v24, 3, v250\n"
      "s_lshl_b32 s56, s56, 11\n"
      "s_lshl_b32 s58, s52, 16\n"
      "v_lshl_add_u32 v23, v24, 1, v23\n"
      "s_add_u32 s56, s56, s58\n"
      "v_bfe_u32 v24, v250, 2, 1\n"
      "s_add_u32 s56, s56, 2359296\n"
      "v_add_u32 v23, v23, v24\n"
      "s_lshl_b32 s63, s80, 11\n"
      "s_add_u32 s63, s63, 512\n"
      "v_lshlrev_b32 v22, 4, v250\n"
      "v_lshl_add_u32 v23, v23, 4, s63\n"
      "s_setprio 0\n"
      "ds_read_b32 v253, v251 offset:36\n"
      "s_waitcnt lgkmcnt(0)\n"
      "v_readfirstlane_b32 s67, v253\n"
      "s_cmp_lt_u32 s67, 4\n"
      "s_cbranch_scc1 Lw28_%=\n"
      "Lr29_%=:\n"
      "s_mov_b32 s68, 65536\n"
      "La31_%=:\n"
      "buffer_load_dwordx4 v[32:35], v22, s[24:27], s56 offen offset:0 sc1\n"
      "buffer_load_dwordx4 v[36:39], v22, s[24:27], s56 offen offset:1024 sc1\n"
      "s_sub_u32 s68, s68, 1\n"
      "s_waitcnt vmcnt(0)\n"
      "s_cbranch_scc1 Lb32_%=\n"
      "v_max3_u32 v40, v32, v33, v34\n"
      "v_max3_u32 v41, v35, v36, v37\n"
      "v_max3_u32 v40, v40, v38, v39\n"
      "v_max_u32 v40, v40, v41\n"
      "v_cmp_eq_u32 vcc, -1, v40\n"
      "s_cbranch_vccz Lb32_%=\n"
      "s_sleep 4\n"
      "s_branch La31_%=\n"
      "Lb32_%=:\n"
      "ds_write_b128 v23, v[32:35] offset:0\n"
      "ds_write_b128 v23, v[36:39] offset:1024\n"
      "s_waitcnt lgkmcnt(0)\n"
      "s_barrier\n"
      "s_lshl3_add_u32 s56, s2, s44\n"
      "s_cmp_eq_u32 s56, 5\n"
      "s_cbranch_scc1 Ln33_%=\n"
      "s_endpgm\n"
      "Ln33_%=:\n"
      "v_lshlrev_b32 v20, 3, v250\n"
      "v_add_u32 v20, 4096, v20\n"
      "s_mov_b32 s68, 65536\n"
      "Lp34_%=:\n"
      "global_load_dwordx2 v[22:23], v20, s[24:25] offset:0 sc1\n"
      "s_sub_u32 s68, s68, 1\n"
      "s_waitcnt vmcnt(0)\n"
      "s_cbranch_scc1 Lq35_%=\n"
      "v_cmp_ne_u64 vcc, s[54:55], v[22:23]\n"
      "s_cbranch_vccz Lq35_%=\n"
      "s_sleep 1\n"
      "s_branch Lp34_%=\n"
      "Lq35_%=:\n"
      "v_add_u32 v20, 0x200, v20\n"
      "s_mov_b32 s68, 65536\n"
      "Lp36_%=:\n"
      "global_load_dwordx2 v[22:23], v20, s[24:25] offset:0 sc1\n"
      "s_sub_u32 s68, s68, 1\n"
      "s_waitcnt vmcnt(0)\n"
      "s_cbranch_scc1 Lq37_%=\n"
      "v_cmp_ne_u64 vcc, s[54:55], v[22:23]\n"
      "s_cbranch_vccz Lq37_%=\n"
      "s_sleep 1\n"
      "s_branch Lp36_%=\n"
      "Lq37_%=:\n"
      "v_add_u32 v20, 0x200, v20\n"
      "s_mov_b32 s68, 65536\n"
      "Lp38_%=:\n"
      "global_load_dwordx2 v[22:23], v20, s[24:25] offset:0 sc1\n"
      "s_sub_u32 s68, s68, 1\n"
      "s_waitcnt vmcnt(0)\n"
      "s_cbranch_scc1 Lq39_%=\n"
      "v_cmp_ne_u64 vcc, s[54:55], v[22:23]\n"
      "s_cbranch_vccz Lq39_%=\n"
      "s_sleep 1\n"
      "s_branch Lp38_%=\n"
      "Lq39_%=:\n"
      "v_add_u32 v20, 0x200, v20\n"
      "s_mov_b32 s68, 65536\n"
      "Lp40_%=:\n"
      "global_load_dwordx2 v[22:23], v20, s[24:25] offset:0 sc1\n"
      "s_sub_u32 s68, s68, 1\n"
      "s_waitcnt vmcnt(0)\n"
      "s_cbranch_scc1 Lq41_%=\n"
      "v_cmp_ne_u64 vcc, s[54:55], v[22:23]\n"
      "s_cbranch_vccz Lq41_%=\n"
      "s_sleep 1\n"
      "s_branch Lp40_%=\n"
      "Lq41_%=:\n"
      "v_add_u32 v20, 0x200, v20\n"
      "v_mov_b32 v24, s54\n"
      "v_mov_b32 v25, s55\n"
      "v_mov_b32 v26, 0\n"
      "s_mov_b64 exec, 1\n"
      "global_store_dwordx2 v26, v[24:25], s[24:25] sc1\n"
      "s_mov_b64 exec, -1\n"
      "s_endpgm\n"
      ".p2align 6\n"
      "LD6_%=:\n"
      "s_branch LK4_%=\n"
      "LG1_%=:\n"
      "s_bfe_u32 s45, s2, 0x30003\n"
      "s_lshr_b32 s46, s2, 6\n"
      "s_bfe_u32 s47, s2, 0x20001\n"
      "s_and_b32 s48, s2, 1\n"
      "s_lshl3_add_u32 s49, s48, s45\n"
      "s_lshl1_add_u32 s56, s46, s45\n"
      "s_add_u32 s57, s47, s48\n"
      "s_lshl1_add_u32 s50, s56, s57\n"
      "s_and_b32 s50, s50, 7\n"
      "s_lshr_b32 s56, s47, 1\n"
      "s_and_b32 s57, s47, 1\n"
      "s_mul_i32 s58, s56, 7\n"
      "s_xor_b32 s56, s56, s45\n"
      "s_or_b32 s56, s56, 14\n"
      "s_add_u32 s59, s58, 3\n"
      "s_lshr_b32 s60, s45, 2\n"
      "s_cmp_eq_u32 s60, s57\n"
      "s_cselect_b32 s56, s59, s56\n"
      "s_lshl2_add_u32 s58, s57, s58\n"
      "s_add_u32 s58, s58, s46\n"
      "s_cmp_lt_u32 s46, 3\n"
      "s_cselect_b32 s51, s58, s56\n"
      "s_and_b32 s56, s2, 7\n"
      "s_and_b32 s57, s45, 3\n"
      "s_lshl2_add_u32 s52, s56, s57\n"
      "s_lshr_b32 s53, s2, 5\n"
      "s_waitcnt lgkmcnt(0)\n"
      "s_lshr_b32 s56, s20, 7\n"
      "s_sub_u32 s56, 3, s56\n"
      "s_and_b32 s57, s47, 1\n"
      "s_xor_b32 s56, s56, s57\n"
      "s_andn2_b32 s56, 7, s56\n"
      "s_xor_b32 s53, s53, s56\n"
      "v_mov_b32 v251, 0x0\n"
      "v_mov_b32 v252, 1\n"
      "s_load_dwordx2 s[54:55], s[24:25], 0x0\n"
      "s_and_b32 s83, s44, 1\n"
      "s_lshr_b32 s84, s44, 1\n"
      "v_lshrrev_b32 v2, 4, v250\n"
      "v_and_b32 v1, 15, v250\n"
      "v_bfe_u32 v5, v250, 2, 2\n"
      "v_xor_b32 v6, s83, v2\n"
      "v_lshrrev_b32 v3, 1, v1\n"
      "s_lshl_b32 s56, s84, 13\n"
      "v_lshl_add_u32 v5, v2, 3, v5\n"
      "v_and_b32 v6, 1, v6\n"
      "v_and_b32 v7, 3, v250\n"
      "s_add_u32 s56, s56, 81920\n"
      "v_xor_b32 v3, v2, v3\n"
      "v_lshl_add_u32 v4, v1, 7, s56\n"
      "v_mul_u32_u24 v5, 192, v5\n"
      "v_lshl_add_u32 v6, v6, 2, v7\n"
      "v_lshl_add_u32 v176, v3, 4, v4\n"
      "v_lshl_add_u32 v5, v6, 3, v5\n"
      "v_xor_b32 v177, 64, v176\n"
      "v_add_u32 v182, 98304, v5\n"
      "s_mul_i32 s57, s44, 16416\n"
      "v_add_u32 v178, 40960, v176\n"
      "v_add_u32 v179, 40960, v177\n"
      "v_add_u32 v183, 40960, v182\n"
      "v_add_u32 v180, -40960, v176\n"
      "v_add_u32 v181, -40960, v177\n"
      "v_add_u32 v184, -40960, v182\n"
      "s_add_u32 m0, s57, 512\n"
      "s_mov_b32 s72, 0x3fb8aa3b\n"
      "s_mov_b32 s73, 0x3e22f983\n"
      "s_waitcnt vmcnt(0)\n"
      "v_lshlrev_b32 v200, 16, v200\n"
      "v_lshlrev_b32 v201, 16, v201\n"
      "v_mul_f32 v210, s72, v200\n"
      "v_mul_f32 v214, s72, v201\n"
      "v_fma_f32 v211, v200, s72, -v210\n"
      "v_fma_f32 v215, v201, s72, -v214\n"
      "v_fmac_f32 v211, 0x32a57060, v200\n"
      "v_fmac_f32 v215, 0x32a57060, v201\n"
      "v_exp_f32 v212, v210\n"
      "v_exp_f32 v216, v214\n"
      "v_mul_f32 v211, 0x3f317218, v211\n"
      "v_mul_f32 v215, 0x3f317218, v215\n"
      "v_fmac_f32 v212, v212, v211\n"
      "v_fmac_f32 v216, v216, v215\n"
      "v_lshlrev_b32 v202, 16, v202\n"
      "v_cvt_pk_bf16_f32 v210, v212, v216\n"
      "v_lshlrev_b32 v203, 16, v203\n"
      "v_lshlrev_b32 v218, 16, v210\n"
      "v_and_b32 v211, 0xffff0000, v210\n"
      "v_mul_f32 v202, v202, v218\n"
      "v_mul_f32 v211, v211, v218\n"
      "v_mul_f32 v220, s73, v202\n"
      "v_mul_f32 v219, 0xbfb8aa3b, v211\n"
      "v_fma_f32 v221, v202, s73, -v220\n"
      "v_mul_f32 v233, 0x43000000, v220\n"
      "v_fmac_f32 v221, 0x31dc9c88, v202\n"
      "v_fract_f32 v233, v233\n"
      "v_mul_f32 v232, 0x43000000, v219\n"
      "v_fmac_f32 v233, 0x43000000, v221\n"
      "v_exp_f32 v232, v232\n"
      "v_cos_f32 v226, v233\n"
      "v_sin_f32 v227, v233\n"
      "v_add_f32 v211, v218, v218\n"
      "v_lshlrev_b32 v204, 16, v204\n"
      "v_mul_f32 v226, v226, v232\n"
      "v_mul_f32_e64 v227, v227, v232\n"
      "v_mul_f32 v32, v211, v203\n"
      "v_mul_f32 v33, v211, v204\n"
      "v_mul_f32 v235, 0x41000000, v220\n"
      "v_pk_mul_f32 v[48:49], v[32:33], v[226:227] op_sel_hi:[0,1]\n"
      "v_fract_f32 v235, v235\n"
      "v_mul_f32 v234, 0x41000000, v219\n"
      "v_pk_fma_f32 v[48:49], v[32:33], v[226:227], v[48:49] op_sel:[1,1,0] op_sel_hi:[1,0,1] neg_lo:[1,0,0]\n"
      "v_fmac_f32 v235, 0x41000000, v221\n"
      "v_exp_f32 v234, v234\n"
      "v_pk_mul_f32 v[64:65], v[48:49], v[226:227] op_sel_hi:[0,1]\n"
      "v_cos_f32 v224, v235\n"
      "v_sin_f32 v225, v235\n"
      "v_pk_fma_f32 v[64:65], v[48:49], v[226:227], v[64:65] op_sel:[1,1,0] op_sel_hi:[1,0,1] neg_lo:[1,0,0]\n"
      "v_mul_f32 v224, v224, v234\n"
      "v_mul_f32_e64 v225, v225, -v234\n"
      "v_pk_mul_f32 v[72:73], v[64:65], v[226:227] op_sel_hi:[0,1]\n"
      "v_pk_mul_f32 v[4:5], v[224:225], v[224:225] op_sel_hi:[0,1]\n"
      "v_fract_f32 v237, v220\n"
      "v_pk_fma_f32 v[72:73], v[64:65], v[226:227], v[72:73] op_sel:[1,1,0] op_sel_hi:[1,0,1] neg_lo:[1,0,0]\n"
      "v_pk_fma_f32 v[4:5], v[224:225], v[224:225], v[4:5] op_sel:[1,1,0] op_sel_hi:[1,0,1] neg_lo:[1,0,0]\n"
      "v_add_f32 v237, v237, v221\n"
      "v_pk_mul_f32 v[80:81], v[72:73], v[226:227] op_sel_hi:[0,1]\n"
      "v_pk_mul_f32 v[6:7], v[4:5], v[224:225] op_sel_hi:[0,1]\n"
      "v_exp_f32 v236, v219\n"
      "v_pk_fma_f32 v[80:81], v[72:73], v[226:227], v[80:81] op_sel:[1,1,0] op_sel_hi:[1,0,1] neg_lo:[1,0,0]\n"
      "v_pk_fma_f32 v[6:7], v[4:5], v[224:225], v[6:7] op_sel:[1,1,0] op_sel_hi:[1,0,1] neg_lo:[1,0,0]\n"
      "v_cos_f32 v222, v237\n"
      "v_pk_mul_f32 v[84:85], v[80:81], v[226:227] op_sel_hi:[0,1]\n"
      "v_pk_mul_f32 v[8:9], v[6:7], v[224:225] op_sel_hi:[0,1]\n"
      "v_sin_f32 v223, v237\n"
      "v_pk_fma_f32 v[84:85], v[80:81], v[226:227], v[84:85] op_sel:[1,1,0] op_sel_hi:[1,0,1] neg_lo:[1,0,0]\n"
      "v_pk_fma_f32 v[8:9], v[6:7], v[224:225], v[8:9] op_sel:[1,1,0] op_sel_hi:[1,0,1] neg_lo:[1,0,0]\n"
      "v_mul_f32 v222, v222, v236\n"
      "v_pk_mul_f32 v[88:89], v[84:85], v[226:227] op_sel_hi:[0,1]\n"
      "v_pk_mul_f32 v[10:11], v[8:9], v[224:225] op_sel_hi:[0,1]\n"
      "v_mul_f32_e64 v223, v223, v236\n"
      "v_pk_fma_f32 v[88:89], v[84:85], v[226:227], v[88:89] op_sel:[1,1,0] op_sel_hi:[1,0,1] neg_lo:[1,0,0]\n"
      "v_pk_fma_f32 v[10:11], v[8:9], v[224:225], v[10:11] op_sel:[1,1,0] op_sel_hi:[1,0,1] neg_lo:[1,0,0]\n"
      "v_pk_mul_f32 v[34:35], v[32:33], v[222:223] op_sel_hi:[0,1]\n"
      "v_pk_mul_f32 v[92:93], v[88:89], v[226:227] op_sel_hi:[0,1]\n"
      "v_pk_mul_f32 v[12:13], v[10:11], v[224:225] op_sel_hi:[0,1]\n"
      "v_pk_mul_f32 v[50:51], v[48:49], v[222:223] op_sel_hi:[0,1]\n"
      "v_pk_fma_f32 v[92:93], v[88:89], v[226:227], v[92:93] op_sel:[1,1,0] op_sel_hi:[1,0,1] neg_lo:[1,0,0]\n"
      "v_pk_fma_f32 v[12:13], v[10:11], v[224:225], v[12:13] op_sel:[1,1,0] op_sel_hi:[1,0,1] neg_lo:[1,0,0]\n"
      "v_pk_fma_f32 v[34:35], v[32:33], v[222:223], v[34:35] op_sel:[1,1,0] op_sel_hi:[1,0,1] neg_lo:[1,0,0]\n"
      "v_pk_mul_f32 v[96:97], v[92:93], v[226:227] op_sel_hi:[0,1]\n"
      "v_pk_mul_f32 v[14:15], v[12:13], v[224:225] op_sel_hi:[0,1]\n"
      "v_pk_fma_f32 v[50:51], v[48:49], v[222:223], v[50:51] op_sel:[1,1,0] op_sel_hi:[1,0,1] neg_lo:[1,0,0]\n"
      "v_pk_fma_f32 v[96:97], v[92:93], v[226:227], v[96:97] op_sel:[1,1,0] op_sel_hi:[1,0,1] neg_lo:[1,0,0]\n"
      "v_pk_fma_f32 v[14:15], v[12:13], v[224:225], v[14:15] op_sel:[1,1,0] op_sel_hi:[1,0,1] neg_lo:[1,0,0]\n"
      "v_pk_mul_f32 v[36:37], v[34:35], v[222:223] op_sel_hi:[0,1]\n"
      "v_pk_mul_f32 v[98:99], v[96:97], v[226:227] op_sel_hi:[0,1]\n"
      "v_pk_mul_f32 v[16:17], v[14:15], v[224:225] op_sel_hi:[0,1]\n"
      "v_pk_mul_f32 v[52:53], v[50:51], v[222:223] op_sel_hi:[0,1]\n"
      "v_pk_fma_f32 v[98:99], v[96:97], v[226:227], v[98:99] op_sel:[1,1,0] op_sel_hi:[1,0,1] neg_lo:[1,0,0]\n"
      "v_pk_fma_f32 v[16:17], v[14:15], v[224:225], v[16:17] op_sel:[1,1,0] op_sel_hi:[1,0,1] neg_lo:[1,0,0]\n"
      "v_pk_fma_f32 v[36:37], v[34:35], v[222:223], v[36:37] op_sel:[1,1,0] op_sel_hi:[1,0,1] neg_lo:[1,0,0]\n"
      "v_pk_mul_f32 v[100:101], v[98:99], v[226:227] op_sel_hi:[0,1]\n"
      "v_pk_mul_f32 v[18:19], v[16:17], v[224:225] op_sel_hi:[0,1]\n"
      "v_pk_fma_f32 v[52:53], v[50:51], v[222:223], v[52:53] op_sel:[1,1,0] op_sel_hi:[1,0,1] neg_lo:[1,0,0]\n"
      "v_pk_fma_f32 v[100:101], v[98:99], v[226:227], v[100:101] op_sel:[1,1,0] op_sel_hi:[1,0,1] neg_lo:[1,0,0]\n"
      "v_pk_fma_f32 v[18:19], v[16:17], v[224:225], v[18:19] op_sel:[1,1,0] op_sel_hi:[1,0,1] neg_lo:[1,0,0]\n"
      "v_pk_mul_f32 v[38:39], v[36:37], v[222:223] op_sel_hi:[0,1]\n"
      "v_pk_mul_f32 v[102:103], v[100:101], v[226:227] op_sel_hi:[0,1]\n"
      "v_pk_mul_f32 v[20:21], v[18:19], v[224:225] op_sel_hi:[0,1]\n"
      "v_pk_mul_f32 v[54:55], v[52:53], v[222:223] op_sel_hi:[0,1]\n"
      "v_pk_fma_f32 v[102:103], v[100:101], v[226:227], v[102:103] op_sel:[1,1,0] op_sel_hi:[1,0,1] neg_lo:[1,0,0]\n"
      "v_pk_fma_f32 v[20:21], v[18:19], v[224:225], v[20:21] op_sel:[1,1,0] op_sel_hi:[1,0,1] neg_lo:[1,0,0]\n"
      "v_pk_fma_f32 v[38:39], v[36:37], v[222:223], v[38:39] op_sel:[1,1,0] op_sel_hi:[1,0,1] neg_lo:[1,0,0]\n"
      "v_pk_mul_f32 v[104:105], v[102:103], v[226:227] op_sel_hi:[0,1]\n"
      "v_pk_mul_f32 v[22:23], v[20:21], v[224:225] op_sel_hi:[0,1]\n"
      "v_pk_fma_f32 v[54:55], v[52:53], v[222:223], v[54:55] op_sel:[1,1,0] op_sel_hi:[1,0,1] neg_lo:[1,0,0]\n"
      "v_pk_fma_f32 v[104:105], v[102:103], v[226:227], v[104:105] op_sel:[1,1,0] op_sel_hi:[1,0,1] neg_lo:[1,0,0]\n"
      "v_pk_fma_f32 v[22:23], v[20:21], v[224:225], v[22:23] op_sel:[1,1,0] op_sel_hi:[1,0,1] neg_lo:[1,0,0]\n"
      "v_pk_mul_f32 v[228:229], v[222:223], v[222:223] op_sel_hi:[0,1]\n"
      "v_pk_mul_f32 v[106:107], v[104:105], v[226:227] op_sel_hi:[0,1]\n"
      "v_pk_mul_f32 v[24:25], v[22:23], v[224:225] op_sel_hi:[0,1]\n"
      "v_pk_mul_f32 v[40:41], v[38:39], v[222:223] op_sel_hi:[0,1]\n"
      "v_pk_mul_f32 v[56:57], v[54:55], v[222:223] op_sel_hi:[0,1]\n"
      "v_pk_fma_f32 v[106:107], v[104:105], v[226:227], v[106:107] op_sel:[1,1,0] op_sel_hi:[1,0,1] neg_lo:[1,0,0]\n"
      "v_pk_fma_f32 v[24:25], v[22:23], v[224:225], v[24:25] op_sel:[1,1,0] op_sel_hi:[1,0,1] neg_lo:[1,0,0]\n"
      "v_pk_fma_f32 v[228:229], v[222:223], v[222:223], v[228:229] op_sel:[1,1,0] op_sel_hi:[1,0,1] neg_lo:[1,0,0]\n"
      "v_pk_fma_f32 v[40:41], v[38:39], v[222:223], v[40:41] op_sel:[1,1,0] op_sel_hi:[1,0,1] neg_lo:[1,0,0]\n"
      "v_pk_fma_f32 v[56:57], v[54:55], v[222:223], v[56:57] op_sel:[1,1,0] op_sel_hi:[1,0,1] neg_lo:[1,0,0]\n"
      "v_pk_mul_f32 v[108:109], v[106:107], v[226:227] op_sel_hi:[0,1]\n"
      "v_pk_mul_f32 v[26:27], v[24:25], v[224:225] op_sel_hi:[0,1]\n"
      "v_pk_mul_f32 v[42:43], v[40:41], v[222:223] op_sel_hi:[0,1]\n"
      "v_pk_mul_f32 v[58:59], v[56:57], v[222:223] op_sel_hi:[0,1]\n"
      "v_pk_mul_f32 v[66:67], v[64:65], v[228:229] op_sel_hi:[0,1]\n"
      "v_pk_mul_f32 v[74:75], v[72:73], v[228:229] op_sel_hi:[0,1]\n"
      "v_pk_fma_f32 v[108:109], v[106:107], v[226:227], v[108:109] op_sel:[1,1,0] op_sel_hi:[1,0,1] neg_lo:[1,0,0]\n"
      "v_pk_fma_f32 v[26:27], v[24:25], v[224:225], v[26:27] op_sel:[1,1,0] op_sel_hi:[1,0,1] neg_lo:[1,0,0]\n"
      "v_pk_fma_f32 v[42:43], v[40:41], v[222:223], v[42:43] op_sel:[1,1,0] op_sel_hi:[1,0,1] neg_lo:[1,0,0]\n"
      "v_pk_fma_f32 v[58:59], v[56:57], v[222:223], v[58:59] op_sel:[1,1,0] op_sel_hi:[1,0,1] neg_lo:[1,0,0]\n"
      "v_pk_fma_f32 v[66:67], v[64:65], v[228:229], v[66:67] op_sel:[1,1,0] op_sel_hi:[1,0,1] neg_lo:[1,0,0]\n"
      "v_pk_fma_f32 v[74:75], v[72:73], v[228:229], v[74:75] op_sel:[1,1,0] op_sel_hi:[1,0,1] neg_lo:[1,0,0]\n"
      "v_pk_mul_f32 v[110:111], v[108:109], v[226:227] op_sel_hi:[0,1]\n"
      "v_pk_mul_f32 v[28:29], v[26:27], v[224:225] op_sel_hi:[0,1]\n"
      "v_pk_mul_f32 v[230:231], v[228:229], v[228:229] op_sel_hi:[0,1]\n"
      "v_pk_mul_f32 v[44:45], v[42:43], v[222:223] op_sel_hi:[0,1]\n"
      "v_pk_mul_f32 v[60:61], v[58:59], v[222:223] op_sel_hi:[0,1]\n"
      "v_pk_mul_f32 v[68:69], v[66:67], v[228:229] op_sel_hi:[0,1]\n"
      "v_pk_mul_f32 v[76:77], v[74:75], v[228:229] op_sel_hi:[0,1]\n"
      "v_pk_fma_f32 v[110:111], v[108:109], v[226:227], v[110:111] op_sel:[1,1,0] op_sel_hi:[1,0,1] neg_lo:[1,0,0]\n"
      "v_pk_fma_f32 v[28:29], v[26:27], v[224:225], v[28:29] op_sel:[1,1,0] op_sel_hi:[1,0,1] neg_lo:[1,0,0]\n"
      "v_pk_fma_f32 v[230:231], v[228:229], v[228:229], v[230:231] op_sel:[1,1,0] op_sel_hi:[1,0,1] neg_lo:[1,0,0]\n"
      "v_pk_fma_f32 v[44:45], v[42:43], v[222:223], v[44:45] op_sel:[1,1,0] op_sel_hi:[1,0,1] neg_lo:[1,0,0]\n"
      "v_pk_fma_f32 v[60:61], v[58:59], v[222:223], v[60:61] op_sel:[1,1,0] op_sel_hi:[1,0,1] neg_lo:[1,0,0]\n"
      "v_pk_fma_f32 v[68:69], v[66:67], v[228:229], v[68:69] op_sel:[1,1,0] op_sel_hi:[1,0,1] neg_lo:[1,0,0]\n"
      "v_pk_fma_f32 v[76:77], v[74:75], v[228:229], v[76:77] op_sel:[1,1,0] op_sel_hi:[1,0,1] neg_lo:[1,0,0]\n"
      "v_pk_mul_f32 v[112:113], v[110:111], v[226:227] op_sel_hi:[0,1]\n"
      "v_pk_mul_f32 v[30:31], v[28:29], v[224:225] op_sel_hi:[0,1]\n"
      "v_pk_mul_f32 v[46:47], v[44:45], v[222:223] op_sel_hi:[0,1]\n"
      "v_pk_mul_f32 v[62:63], v[60:61], v[222:223] op_sel_hi:[0,1]\n"
      "v_pk_mul_f32 v[70:71], v[68:69], v[228:229] op_sel_hi:[0,1]\n"
      "v_pk_mul_f32 v[78:79], v[76:77], v[228:229] op_sel_hi:[0,1]\n"
      "v_pk_mul_f32 v[82:83], v[80:81], v[230:231] op_sel_hi:[0,1]\n"
      "v_pk_mul_f32 v[86:87], v[84:85], v[230:231] op_sel_hi:[0,1]\n"
      "v_pk_mul_f32 v[90:91], v[88:89], v[230:231] op_sel_hi:[0,1]\n"
      "v_pk_mul_f32 v[94:95], v[92:93], v[230:231] op_sel_hi:[0,1]\n"
      "v_pk_fma_f32 v[112:113], v[110:111], v[226:227], v[112:113] op_sel:[1,1,0] op_sel_hi:[1,0,1] neg_lo:[1,0,0]\n"
      "v_pk_fma_f32 v[30:31], v[28:29], v[224:225], v[30:31] op_sel:[1,1,0] op_sel_hi:[1,0,1] neg_lo:[1,0,0]\n"
      "v_pk_fma_f32 v[46:47], v[44:45], v[222:223], v[46:47] op_sel:[1,1,0] op_sel_hi:[1,0,1] neg_lo:[1,0,0]\n"
      "v_pk_fma_f32 v[62:63], v[60:61], v[222:223], v[62:63] op_sel:[1,1,0] op_sel_hi:[1,0,1] neg_lo:[1,0,0]\n"
      "v_pk_fma_f32 v[70:71], v[68:69], v[228:229], v[70:71] op_sel:[1,1,0] op_sel_hi:[1,0,1] neg_lo:[1,0,0]\n"
      "v_pk_fma_f32 v[78:79], v[76:77], v[228:229], v[78:79] op_sel:[1,1,0] op_sel_hi:[1,0,1] neg_lo:[1,0,0]\n"
      "v_pk_fma_f32 v[82:83], v[80:81], v[230:231], v[82:83] op_sel:[1,1,0] op_sel_hi:[1,0,1] neg_lo:[1,0,0]\n"
      "v_pk_fma_f32 v[86:87], v[84:85], v[230:231], v[86:87] op_sel:[1,1,0] op_sel_hi:[1,0,1] neg_lo:[1,0,0]\n"
      "v_pk_fma_f32 v[90:91], v[88:89], v[230:231], v[90:91] op_sel:[1,1,0] op_sel_hi:[1,0,1] neg_lo:[1,0,0]\n"
      "v_pk_fma_f32 v[94:95], v[92:93], v[230:231], v[94:95] op_sel:[1,1,0] op_sel_hi:[1,0,1] neg_lo:[1,0,0]\n"
      "v_mov_b32 v116, 0x3f80\n"
      "v_cvt_pk_bf16_f32 v117, v224, v225\n"
      "v_cvt_pk_bf16_f32 v118, v4, v5\n"
      "v_cvt_pk_bf16_f32 v119, v6, v7\n"
      "v_cvt_pk_bf16_f32 v120, v8, v9\n"
      "v_cvt_pk_bf16_f32 v121, v10, v11\n"
      "v_cvt_pk_bf16_f32 v122, v12, v13\n"
      "v_cvt_pk_bf16_f32 v123, v14, v15\n"
      "v_cvt_pk_bf16_f32 v124, v16, v17\n"
      "v_cvt_pk_bf16_f32 v125, v18, v19\n"
      "v_cvt_pk_bf16_f32 v126, v20, v21\n"
      "v_cvt_pk_bf16_f32 v127, v22, v23\n"
      "v_cvt_pk_bf16_f32 v128, v24, v25\n"
      "v_cvt_pk_bf16_f32 v129, v26, v27\n"
      "v_cvt_pk_bf16_f32 v130, v28, v29\n"
      "v_cvt_pk_bf16_f32 v131, v30, v31\n"
      "v_cvt_pk_bf16_f32 v132, v32, v33\n"
      "v_cvt_pk_bf16_f32 v133, v34, v35\n"
      "v_cvt_pk_bf16_f32 v134, v36, v37\n"
      "v_cvt_pk_bf16_f32 v135, v38, v39\n"
      "v_cvt_pk_bf16_f32 v136, v40, v41\n"
      "v_cvt_pk_bf16_f32 v137, v42, v43\n"
      "v_cvt_pk_bf16_f32 v138, v44, v45\n"
      "v_cvt_pk_bf16_f32 v139, v46, v47\n"
      "v_cvt_pk_bf16_f32 v140, v48, v49\n"
      "v_cvt_pk_bf16_f32 v141, v50, v51\n"
      "v_cvt_pk_bf16_f32 v142, v52, v53\n"
      "v_cvt_pk_bf16_f32 v143, v54, v55\n"
      "v_cvt_pk_bf16_f32 v144, v56, v57\n"
      "v_cvt_pk_bf16_f32 v145, v58, v59\n"
      "v_cvt_pk_bf16_f32 v146, v60, v61\n"
      "v_cvt_pk_bf16_f32 v147, v62, v63\n"
      "v_cvt_pk_bf16_f32 v148, v64, v65\n"
      "v_cvt_pk_bf16_f32 v149, v66, v67\n"
      "v_cvt_pk_bf16_f32 v150, v68, v69\n"
      "v_cvt_pk_bf16_f32 v151, v70, v71\n"
      "v_cvt_pk_bf16_f32 v152, v72, v73\n"
      "v_cvt_pk_bf16_f32 v153, v74, v75\n"
      "v_cvt_pk_bf16_f32 v154, v76, v77\n"
      "v_cvt_pk_bf16_f32 v155, v78, v79\n"
      "v_cvt_pk_bf16_f32 v156, v80, v81\n"
      "v_cvt_pk_bf16_f32 v157, v82, v83\n"
      "v_cvt_pk_bf16_f32 v158, v84, v85\n"
      "v_cvt_pk_bf16_f32 v159, v86, v87\n"
      "v_cvt_pk_bf16_f32 v160, v88, v89\n"
      "v_cvt_pk_bf16_f32 v161, v90, v91\n"
      "v_cvt_pk_bf16_f32 v162, v92, v93\n"
      "v_cvt_pk_bf16_f32 v163, v94, v95\n"
      "v_cvt_pk_bf16_f32 v164, v96, v97\n"
      "v_cvt_pk_bf16_f32 v165, v98, v99\n"
      "v_cvt_pk_bf16_f32 v166, v100, v101\n"
      "v_cvt_pk_bf16_f32 v167, v102, v103\n"
      "v_cvt_pk_bf16_f32 v168, v104, v105\n"
      "v_cvt_pk_bf16_f32 v169, v106, v107\n"
      "v_cvt_pk_bf16_f32 v170, v108, v109\n"
      "v_cvt_pk_bf16_f32 v171, v110, v111\n"
      "v_cvt_pk_bf16_f32 v172, v112, v113\n"
      "ds_write_addtid_b32 v116 offset:0\n"
      "ds_write_addtid_b32 v117 offset:288\n"
      "ds_write_addtid_b32 v118 offset:576\n"
      "ds_write_addtid_b32 v119 offset:864\n"
      "ds_write_addtid_b32 v120 offset:1152\n"
      "ds_write_addtid_b32 v121 offset:1440\n"
      "ds_write_addtid_b32 v122 offset:1728\n"
      "ds_write_addtid_b32 v123 offset:2016\n"
      "ds_write_addtid_b32 v124 offset:2304\n"
      "ds_write_addtid_b32 v125 offset:2592\n"
      "ds_write_addtid_b32 v126 offset:2880\n"
      "ds_write_addtid_b32 v127 offset:3168\n"
      "ds_write_addtid_b32 v128 offset:3456\n"
      "ds_write_addtid_b32 v129 offset:3744\n"
      "ds_write_addtid_b32 v130 offset:4032\n"
      "ds_write_addtid_b32 v131 offset:4320\n"
      "ds_write_addtid_b32 v132 offset:4608\n"
      "ds_write_addtid_b32 v133 offset:4896\n"
      "ds_write_addtid_b32 v134 offset:5184\n"
      "ds_write_addtid_b32 v135 offset:5472\n"
      "ds_write_addtid_b32 v136 offset:5760\n"
      "ds_write_addtid_b32 v137 offset:6048\n"
      "ds_write_addtid_b32 v138 offset:6336\n"
      "ds_write_addtid_b32 v139 offset:6624\n"
      "ds_write_addtid_b32 v140 offset:6912\n"
      "ds_write_addtid_b32 v141 offset:7200\n"
      "ds_write_addtid_b32 v142 offset:7488\n"
      "ds_write_addtid_b32 v143 offset:7776\n"
      "ds_write_addtid_b32 v144 offset:8064\n"
      "ds_write_addtid_b32 v145 offset:8352\n"
      "ds_write_addtid_b32 v146 offset:8640\n"
      "ds_write_addtid_b32 v147 offset:8928\n"
      "ds_write_addtid_b32 v148 offset:9216\n"
      "ds_write_addtid_b32 v149 offset:9504\n"
      "ds_write_addtid_b32 v150 offset:9792\n"
      "ds_write_addtid_b32 v151 offset:10080\n"
      "ds_write_addtid_b32 v152 offset:10368\n"
      "ds_write_addtid_b32 v153 offset:10656\n"
      "ds_write_addtid_b32 v154 offset:10944\n"
      "ds_write_addtid_b32 v155 offset:11232\n"
      "ds_write_addtid_b32 v156 offset:11520\n"
      "ds_write_addtid_b32 v157 offset:11808\n"
      "ds_write_addtid_b32 v158 offset:12096\n"
      "ds_write_addtid_b32 v159 offset:12384\n"
      "ds_write_addtid_b32 v160 offset:12672\n"
      "ds_write_addtid_b32 v161 offset:12960\n"
      "ds_write_addtid_b32 v162 offset:13248\n"
      "ds_write_addtid_b32 v163 offset:13536\n"
      "ds_write_addtid_b32 v164 offset:13824\n"
      "ds_write_addtid_b32 v165 offset:14112\n"
      "ds_write_addtid_b32 v166 offset:14400\n"
      "ds_write_addtid_b32 v167 offset:14688\n"
      "ds_write_addtid_b32 v168 offset:14976\n"
      "ds_write_addtid_b32 v169 offset:15264\n"
      "ds_write_addtid_b32 v170 offset:15552\n"
      "ds_write_addtid_b32 v171 offset:15840\n"
      "ds_write_addtid_b32 v172 offset:16128\n"
      "s_waitcnt lgkmcnt(0)\n"
      "s_add_u32 s54, s54, 1\n"
      "s_addc_u32 s55, s55, 0\n"
      "v_mov_b32 v246, s54\n"
      "v_mov_b32 v247, s55\n"
      "s_barrier\n"
      "s_cmp_gt_u32 s51, 13\n"
      "s_cbranch_scc1 Ll42_%=\n"
      ".p2align 3\n"
      "ds_read_b64_tr_b16 v[96:97], v182 offset:0\n"
      "ds_read_b64_tr_b16 v[98:99], v182 offset:768\n"
      "ds_read_b128 v[120:123], v176 offset:0\n"
      "ds_read_b128 v[124:127], v176 offset:2048\n"
      "ds_read_b128 v[128:131], v176 offset:4096\n"
      "ds_read_b128 v[132:135], v176 offset:6144\n"
      "ds_read_b64_tr_b16 v[100:101], v182 offset:12288\n"
      "ds_read_b64_tr_b16 v[102:103], v182 offset:13056\n"
      "ds_read_b64_tr_b16 v[104:105], v182 offset:64\n"
      "ds_read_b64_tr_b16 v[106:107], v182 offset:832\n"
      "ds_read_b64_tr_b16 v[108:109], v182 offset:12352\n"
      "ds_read_b64_tr_b16 v[110:111], v182 offset:13120\n"
      "ds_read_b64_tr_b16 v[112:113], v182 offset:128\n"
      "ds_read_b64_tr_b16 v[114:115], v182 offset:896\n"
      "ds_read_b64_tr_b16 v[116:117], v182 offset:12416\n"
      "ds_read_b64_tr_b16 v[118:119], v182 offset:13184\n"
      "s_waitcnt lgkmcnt(13)\n"
      "v_mfma_f32_16x16x32_bf16 v[0:3], v[96:99], v[120:123], 0\n"
      "s_nop 2\n"
      "s_waitcnt lgkmcnt(12)\n"
      "v_mfma_f32_16x16x32_bf16 v[4:7], v[96:99], v[124:127], 0\n"
      "s_nop 2\n"
      "s_waitcnt lgkmcnt(11)\n"
      "v_mfma_f32_16x16x32_bf16 v[8:11], v[96:99], v[128:131], 0\n"
      "s_nop 2\n"
      "s_waitcnt lgkmcnt(10)\n"
      "v_mfma_f32_16x16x32_bf16 v[12:15], v[96:99], v[132:135], 0\n"
      "s_nop 2\n"
      "s_barrier\n"
      "s_waitcnt lgkmcnt(8)\n"
      "v_mfma_f32_16x16x32_bf16 v[28:31], v[100:103], v[132:135], 0\n"
      "s_nop 2\n"
      "ds_read_b64_tr_b16 v[136:137], v182 offset:6144\n"
      "v_mfma_f32_16x16x32_bf16 v[24:27], v[100:103], v[128:131], 0\n"
      "s_nop 2\n"
      "ds_read_b64_tr_b16 v[138:139], v182 offset:6912\n"
      "v_mfma_f32_16x16x32_bf16 v[20:23], v[100:103], v[124:127], 0\n"
      "s_nop 2\n"
      "ds_read_b128 v[160:163], v177 offset:0\n"
      "v_mfma_f32_16x16x32_bf16 v[16:19], v[100:103], v[120:123], 0\n"
      "s_nop 2\n"
      "ds_read_b128 v[164:167], v177 offset:2048\n"
      "s_waitcnt lgkmcnt(10)\n"
      "v_mfma_f32_16x16x32_bf16 v[32:35], v[104:107], v[120:123], 0\n"
      "s_nop 2\n"
      "ds_read_b128 v[168:171], v177 offset:4096\n"
      "v_mfma_f32_16x16x32_bf16 v[36:39], v[104:107], v[124:127], 0\n"
      "s_nop 2\n"
      "ds_read_b128 v[172:175], v177 offset:6144\n"
      "v_mfma_f32_16x16x32_bf16 v[40:43], v[104:107], v[128:131], 0\n"
      "s_nop 2\n"
      "ds_read_b64_tr_b16 v[140:141], v182 offset:18432\n"
      "v_mfma_f32_16x16x32_bf16 v[44:47], v[104:107], v[132:135], 0\n"
      "s_nop 2\n"
      "ds_read_b64_tr_b16 v[142:143], v182 offset:19200\n"
      "s_waitcnt lgkmcnt(12)\n"
      "v_mfma_f32_16x16x32_bf16 v[60:63], v[108:111], v[132:135], 0\n"
      "s_nop 2\n"
      "ds_read_b64_tr_b16 v[144:145], v182 offset:6208\n"
      "v_mfma_f32_16x16x32_bf16 v[56:59], v[108:111], v[128:131], 0\n"
      "s_nop 2\n"
      "ds_read_b64_tr_b16 v[146:147], v182 offset:6976\n"
      "v_mfma_f32_16x16x32_bf16 v[52:55], v[108:111], v[124:127], 0\n"
      "s_nop 2\n"
      "ds_read_b64_tr_b16 v[148:149], v182 offset:18496\n"
      "v_mfma_f32_16x16x32_bf16 v[48:51], v[108:111], v[120:123], 0\n"
      "s_nop 2\n"
      "ds_read_b64_tr_b16 v[150:151], v182 offset:19264\n"
      "s_waitcnt lgkmcnt(14)\n"
      "v_mfma_f32_16x16x32_bf16 v[64:67], v[112:115], v[120:123], 0\n"
      "s_nop 2\n"
      "ds_read_b64_tr_b16 v[152:153], v182 offset:6272\n"
      "v_mfma_f32_16x16x32_bf16 v[68:71], v[112:115], v[124:127], 0\n"
      "s_nop 2\n"
      "ds_read_b64_tr_b16 v[154:155], v182 offset:7040\n"
      "v_mfma_f32_16x16x32_bf16 v[72:75], v[112:115], v[128:131], 0\n"
      "s_nop 2\n"
      "ds_read_b64_tr_b16 v[156:157], v182 offset:18560\n"
      "v_mfma_f32_16x16x32_bf16 v[76:79], v[112:115], v[132:135], 0\n"
      "s_nop 2\n"
      "ds_read_b64_tr_b16 v[158:159], v182 offset:19328\n"
      "s_waitcnt lgkmcnt(15)\n"
      "v_mfma_f32_16x16x32_bf16 v[92:95], v[116:119], v[132:135], 0\n"
      "s_nop 2\n"
      "v_mfma_f32_16x16x32_bf16 v[88:91], v[116:119], v[128:131], 0\n"
      "s_nop 2\n"
      "v_mfma_f32_16x16x32_bf16 v[84:87], v[116:119], v[124:127], 0\n"
      "s_nop 2\n"
      "v_mfma_f32_16x16x32_bf16 v[80:83], v[116:119], v[120:123], 0\n"
      "s_nop 2\n"
      "s_waitcnt lgkmcnt(13)\n"
      "v_mfma_f32_16x16x32_bf16 v[0:3], v[136:139], v[160:163], v[0:3]\n"
      "s_nop 2\n"
      "s_waitcnt lgkmcnt(12)\n"
      "v_mfma_f32_16x16x32_bf16 v[4:7], v[136:139], v[164:167], v[4:7]\n"
      "s_nop 2\n"
      "s_waitcnt lgkmcnt(11)\n"
      "v_mfma_f32_16x16x32_bf16 v[8:11], v[136:139], v[168:171], v[8:11]\n"
      "s_nop 2\n"
      "s_waitcnt lgkmcnt(10)\n"
      "v_mfma_f32_16x16x32_bf16 v[12:15], v[136:139], v[172:175], v[12:15]\n"
      "s_nop 2\n"
      "s_barrier\n"
      "s_waitcnt lgkmcnt(8)\n"
      "v_mfma_f32_16x16x32_bf16 v[28:31], v[140:143], v[172:175], v[28:31]\n"
      "s_nop 2\n"
      "ds_read_b64_tr_b16 v[96:97], v183 offset:0\n"
      "v_mfma_f32_16x16x32_bf16 v[24:27], v[140:143], v[168:171], v[24:27]\n"
      "s_nop 2\n"
      "ds_read_b64_tr_b16 v[98:99], v183 offset:768\n"
      "v_mfma_f32_16x16x32_bf16 v[20:23], v[140:143], v[164:167], v[20:23]\n"
      "s_nop 2\n"
      "ds_read_b128 v[120:123], v178 offset:0\n"
      "v_mfma_f32_16x16x32_bf16 v[16:19], v[140:143], v[160:163], v[16:19]\n"
      "s_nop 2\n"
      "ds_read_b128 v[124:127], v178 offset:2048\n"
      "s_waitcnt lgkmcnt(10)\n"
      "v_mfma_f32_16x16x32_bf16 v[32:35], v[144:147], v[160:163], v[32:35]\n"
      "s_nop 2\n"
      "ds_read_b128 v[128:131], v178 offset:4096\n"
      "v_mfma_f32_16x16x32_bf16 v[36:39], v[144:147], v[164:167], v[36:39]\n"
      "s_nop 2\n"
      "ds_read_b128 v[132:135], v178 offset:6144\n"
      "v_mfma_f32_16x16x32_bf16 v[40:43], v[144:147], v[168:171], v[40:43]\n"
      "s_nop 2\n"
      "ds_read_b64_tr_b16 v[100:101], v183 offset:12288\n"
      "v_mfma_f32_16x16x32_bf16 v[44:47], v[144:147], v[172:175], v[44:47]\n"
      "s_nop 2\n"
      "ds_read_b64_tr_b16 v[102:103], v183 offset:13056\n"
      "s_waitcnt lgkmcnt(12)\n"
      "v_mfma_f32_16x16x32_bf16 v[60:63], v[148:151], v[172:175], v[60:63]\n"
      "s_nop 2\n"
      "ds_read_b64_tr_b16 v[104:105], v183 offset:64\n"
      "v_mfma_f32_16x16x32_bf16 v[56:59], v[148:151], v[168:171], v[56:59]\n"
      "s_nop 2\n"
      "ds_read_b64_tr_b16 v[106:107], v183 offset:832\n"
      "v_mfma_f32_16x16x32_bf16 v[52:55], v[148:151], v[164:167], v[52:55]\n"
      "s_nop 2\n"
      "ds_read_b64_tr_b16 v[108:109], v183 offset:12352\n"
      "v_mfma_f32_16x16x32_bf16 v[48:51], v[148:151], v[160:163], v[48:51]\n"
      "s_nop 2\n"
      "ds_read_b64_tr_b16 v[110:111], v183 offset:13120\n"
      "s_waitcnt lgkmcnt(14)\n"
      "v_mfma_f32_16x16x32_bf16 v[64:67], v[152:155], v[160:163], v[64:67]\n"
      "s_nop 2\n"
      "ds_read_b64_tr_b16 v[112:113], v183 offset:128\n"
      "v_mfma_f32_16x16x32_bf16 v[68:71], v[152:155], v[164:167], v[68:71]\n"
      "s_nop 2\n"
      "ds_read_b64_tr_b16 v[114:115], v183 offset:896\n"
      "v_mfma_f32_16x16x32_bf16 v[72:75], v[152:155], v[168:171], v[72:75]\n"
      "s_nop 2\n"
      "ds_read_b64_tr_b16 v[116:117], v183 offset:12416\n"
      "v_mfma_f32_16x16x32_bf16 v[76:79], v[152:155], v[172:175], v[76:79]\n"
      "s_nop 2\n"
      "ds_read_b64_tr_b16 v[118:119], v183 offset:13184\n"
      "s_waitcnt lgkmcnt(15)\n"
      "v_mfma_f32_16x16x32_bf16 v[92:95], v[156:159], v[172:175], v[92:95]\n"
      "s_nop 2\n"
      "v_mfma_f32_16x16x32_bf16 v[88:91], v[156:159], v[168:171], v[88:91]\n"
      "s_nop 2\n"
      "v_mfma_f32_16x16x32_bf16 v[84:87], v[156:159], v[164:167], v[84:87]\n"
      "s_nop 2\n"
      "v_mfma_f32_16x16x32_bf16 v[80:83], v[156:159], v[160:163], v[80:83]\n"
      "s_nop 2\n"
      "s_waitcnt lgkmcnt(13)\n"
      "v_mfma_f32_16x16x32_bf16 v[0:3], v[96:99], v[120:123], v[0:3]\n"
      "s_nop 2\n"
      "s_waitcnt lgkmcnt(12)\n"
      "v_mfma_f32_16x16x32_bf16 v[4:7], v[96:99], v[124:127], v[4:7]\n"
      "s_nop 2\n"
      "s_waitcnt lgkmcnt(11)\n"
      "v_mfma_f32_16x16x32_bf16 v[8:11], v[96:99], v[128:131], v[8:11]\n"
      "s_nop 2\n"
      "s_waitcnt lgkmcnt(10)\n"
      "v_mfma_f32_16x16x32_bf16 v[12:15], v[96:99], v[132:135], v[12:15]\n"
      "s_nop 2\n"
      "s_barrier\n"
      "s_waitcnt lgkmcnt(8)\n"
      "v_mfma_f32_16x16x32_bf16 v[28:31], v[100:103], v[132:135], v[28:31]\n"
      "s_nop 2\n"
      "ds_read_b64_tr_b16 v[136:137], v183 offset:6144\n"
      "s_lshl_b32 s56, s83, 9\n"
      "v_mfma_f32_16x16x32_bf16 v[24:27], v[100:103], v[128:131], v[24:27]\n"
      "s_nop 2\n"
      "ds_read_b64_tr_b16 v[138:139], v183 offset:6912\n"
      "s_lshl1_add_u32 s57, s49, s84\n"
      "v_mfma_f32_16x16x32_bf16 v[20:23], v[100:103], v[124:127], v[20:23]\n"
      "s_nop 2\n"
      "ds_read_b128 v[160:163], v179 offset:0\n"
      "s_mul_i32 s57, s57, 172\n"
      "v_mfma_f32_16x16x32_bf16 v[16:19], v[100:103], v[120:123], v[16:19]\n"
      "s_nop 2\n"
      "ds_read_b128 v[164:167], v179 offset:2048\n"
      "s_mul_i32 s58, s51, 3\n"
      "s_waitcnt lgkmcnt(10)\n"
      "v_mfma_f32_16x16x32_bf16 v[32:35], v[104:107], v[120:123], v[32:35]\n"
      "s_nop 2\n"
      "ds_read_b128 v[168:171], v179 offset:4096\n"
      "s_add_u32 s57, s57, s58\n"
      "v_mfma_f32_16x16x32_bf16 v[36:39], v[104:107], v[124:127], v[36:39]\n"
      "s_nop 2\n"
      "ds_read_b128 v[172:175], v179 offset:6144\n"
      "s_lshl_b32 s57, s57, 10\n"
      "v_mfma_f32_16x16x32_bf16 v[40:43], v[104:107], v[128:131], v[40:43]\n"
      "s_nop 2\n"
      "ds_read_b64_tr_b16 v[140:141], v183 offset:18432\n"
      "v_lshrrev_b32 v213, 4, v250\n"
      "v_mfma_f32_16x16x32_bf16 v[44:47], v[104:107], v[132:135], v[44:47]\n"
      "s_nop 2\n"
      "ds_read_b64_tr_b16 v[142:143], v183 offset:19200\n"
      "v_bfe_u32 v214, v250, 2, 2\n"
      "s_waitcnt lgkmcnt(12)\n"
      "v_mfma_f32_16x16x32_bf16 v[60:63], v[108:111], v[132:135], v[60:63]\n"
      "s_nop 2\n"
      "ds_read_b64_tr_b16 v[144:145], v183 offset:6208\n"
      "s_add_u32 s65, s57, 4456448\n"
      "v_mfma_f32_16x16x32_bf16 v[56:59], v[108:111], v[128:131], v[56:59]\n"
      "s_nop 2\n"
      "ds_read_b64_tr_b16 v[146:147], v183 offset:6976\n"
      "v_lshl_add_u32 v215, v213, 3, v214\n"
      "v_mfma_f32_16x16x32_bf16 v[52:55], v[108:111], v[124:127], v[52:55]\n"
      "s_nop 2\n"
      "ds_read_b64_tr_b16 v[148:149], v183 offset:18496\n"
      "s_lshl_b32 s59, s83, 12\n"
      "v_mfma_f32_16x16x32_bf16 v[48:51], v[108:111], v[120:123], v[48:51]\n"
      "s_nop 2\n"
      "ds_read_b64_tr_b16 v[150:151], v183 offset:19264\n"
      "v_bfe_u32 v211, v250, 4, 1\n"
      "s_waitcnt lgkmcnt(14)\n"
      "v_mfma_f32_16x16x32_bf16 v[64:67], v[112:115], v[120:123], v[64:67]\n"
      "s_nop 2\n"
      "ds_read_b64_tr_b16 v[152:153], v183 offset:6272\n"
      "v_bfe_u32 v214, v250, 3, 2\n"
      "v_mfma_f32_16x16x32_bf16 v[68:71], v[112:115], v[124:127], v[68:71]\n"
      "s_nop 2\n"
      "ds_read_b64_tr_b16 v[154:155], v183 offset:7040\n"
      "s_add_u32 s59, s59, 40960\n"
      "v_mfma_f32_16x16x32_bf16 v[72:75], v[112:115], v[128:131], v[72:75]\n"
      "s_nop 2\n"
      "ds_read_b64_tr_b16 v[156:157], v183 offset:18560\n"
      "s_lshl2_add_u32 s66, s52, s44\n"
      "v_mfma_f32_16x16x32_bf16 v[76:79], v[112:115], v[132:135], v[76:79]\n"
      "s_nop 2\n"
      "ds_read_b64_tr_b16 v[158:159], v183 offset:19328\n"
      "v_mul_u32_u24 v211, 44032, v211\n"
      "s_waitcnt lgkmcnt(15)\n"
      "v_mfma_f32_16x16x32_bf16 v[92:95], v[116:119], v[132:135], v[92:95]\n"
      "s_nop 2\n"
      "v_mfma_f32_16x16x32_bf16 v[88:91], v[116:119], v[128:131], v[88:91]\n"
      "s_nop 2\n"
      "v_mfma_f32_16x16x32_bf16 v[84:87], v[116:119], v[124:127], v[84:87]\n"
      "s_nop 2\n"
      "v_mfma_f32_16x16x32_bf16 v[80:83], v[116:119], v[120:123], v[80:83]\n"
      "s_nop 2\n"
      "s_waitcnt lgkmcnt(13)\n"
      "v_mfma_f32_16x16x32_bf16 v[0:3], v[136:139], v[160:163], v[0:3]\n"
      "s_nop 2\n"
      "s_waitcnt lgkmcnt(12)\n"
      "v_mfma_f32_16x16x32_bf16 v[4:7], v[136:139], v[164:167], v[4:7]\n"
      "s_nop 2\n"
      "s_waitcnt lgkmcnt(11)\n"
      "v_mfma_f32_16x16x32_bf16 v[8:11], v[136:139], v[168:171], v[8:11]\n"
      "s_nop 2\n"
      "s_waitcnt lgkmcnt(10)\n"
      "v_mfma_f32_16x16x32_bf16 v[12:15], v[136:139], v[172:175], v[12:15]\n"
      "s_nop 2\n"
      "s_barrier\n"
      "s_waitcnt lgkmcnt(8)\n"
      "v_mfma_f32_16x16x32_bf16 v[28:31], v[140:143], v[172:175], v[28:31]\n"
      "s_nop 2\n"
      "ds_read_b64_tr_b16 v[96:97], v184 offset:0\n"
      "v_lshrrev_b32 v212, 5, v250\n"
      "v_mfma_f32_16x16x32_bf16 v[24:27], v[140:143], v[168:171], v[24:27]\n"
      "s_nop 2\n"
      "ds_read_b64_tr_b16 v[98:99], v184 offset:768\n"
      "v_lshl_add_u32 v215, v215, 2, v214\n"
      "v_mfma_f32_16x16x32_bf16 v[20:23], v[140:143], v[164:167], v[20:23]\n"
      "s_nop 2\n"
      "ds_read_b128 v[120:123], v180 offset:0\n"
      "s_mul_i32 s60, s83, 3\n"
      "v_mfma_f32_16x16x32_bf16 v[16:19], v[140:143], v[160:163], v[16:19]\n"
      "s_nop 2\n"
      "ds_read_b128 v[124:127], v180 offset:2048\n"
      "s_mul_i32 s61, s66, 43\n"
      "s_waitcnt lgkmcnt(10)\n"
      "v_mfma_f32_16x16x32_bf16 v[32:35], v[144:147], v[160:163], v[32:35]\n"
      "s_nop 2\n"
      "ds_read_b128 v[128:131], v180 offset:4096\n"
      "v_and_b32 v214, 3, v250\n"
      "v_mfma_f32_16x16x32_bf16 v[36:39], v[144:147], v[164:167], v[36:39]\n"
      "s_nop 2\n"
      "ds_read_b128 v[132:135], v180 offset:6144\n"
      "s_add_u32 s61, s61, s60\n"
      "v_mfma_f32_16x16x32_bf16 v[40:43], v[144:147], v[168:171], v[40:43]\n"
      "s_nop 2\n"
      "ds_read_b64_tr_b16 v[100:101], v184 offset:12288\n"
      "v_lshl_add_u32 v211, v212, 8, v211\n"
      "v_mfma_f32_16x16x32_bf16 v[44:47], v[144:147], v[172:175], v[44:47]\n"
      "s_nop 2\n"
      "ds_read_b64_tr_b16 v[102:103], v184 offset:13056\n"
      "s_lshl_b32 s61, s61, 10\n"
      "s_waitcnt lgkmcnt(12)\n"
      "v_mfma_f32_16x16x32_bf16 v[60:63], v[148:151], v[172:175], v[60:63]\n"
      "s_nop 2\n"
      "ds_read_b64_tr_b16 v[104:105], v184 offset:64\n"
      "v_and_b32 v212, 15, v250\n"
      "v_mfma_f32_16x16x32_bf16 v[56:59], v[148:151], v[168:171], v[56:59]\n"
      "s_nop 2\n"
      "ds_read_b64_tr_b16 v[106:107], v184 offset:832\n"
      "v_lshl_add_u32 v215, v215, 2, v214\n"
      "v_mfma_f32_16x16x32_bf16 v[52:55], v[148:151], v[164:167], v[52:55]\n"
      "s_nop 2\n"
      "ds_read_b64_tr_b16 v[108:109], v184 offset:12352\n"
      "s_xor_b32 s62, s66, 1\n"
      "v_mfma_f32_16x16x32_bf16 v[48:51], v[148:151], v[160:163], v[48:51]\n"
      "s_nop 2\n"
      "ds_read_b64_tr_b16 v[110:111], v184 offset:13120\n"
      "s_mul_i32 s62, s62, 43\n"
      "s_waitcnt lgkmcnt(14)\n"
      "v_mfma_f32_16x16x32_bf16 v[64:67], v[152:155], v[160:163], v[64:67]\n"
      "s_nop 2\n"
      "ds_read_b64_tr_b16 v[112:113], v184 offset:128\n"
      "v_lshl_add_u32 v211, v212, 4, v211\n"
      "v_mfma_f32_16x16x32_bf16 v[68:71], v[152:155], v[164:167], v[68:71]\n"
      "s_nop 2\n"
      "ds_read_b64_tr_b16 v[114:115], v184 offset:896\n"
      "v_lshl_add_u32 v194, v215, 3, s59\n"
      "v_mfma_f32_16x16x32_bf16 v[72:75], v[152:155], v[168:171], v[72:75]\n"
      "s_nop 2\n"
      "ds_read_b64_tr_b16 v[116:117], v184 offset:12416\n"
      "s_add_u32 s62, s62, s60\n"
      "v_mfma_f32_16x16x32_bf16 v[76:79], v[152:155], v[172:175], v[76:79]\n"
      "s_nop 2\n"
      "ds_read_b64_tr_b16 v[118:119], v184 offset:13184\n"
      "s_lshl_b32 s62, s62, 10\n"
      "s_waitcnt lgkmcnt(15)\n"
      "v_mfma_f32_16x16x32_bf16 v[92:95], v[156:159], v[172:175], v[92:95]\n"
      "s_nop 2\n"
      "v_mfma_f32_16x16x32_bf16 v[88:91], v[156:159], v[168:171], v[88:91]\n"
      "s_nop 2\n"
      "v_mfma_f32_16x16x32_bf16 v[84:87], v[156:159], v[164:167], v[84:87]\n"
      "s_nop 2\n"
      "v_mfma_f32_16x16x32_bf16 v[80:83], v[156:159], v[160:163], v[80:83]\n"
      "s_nop 2\n"
      "s_waitcnt lgkmcnt(13)\n"
      "v_mfma_f32_16x16x32_bf16 v[0:3], v[96:99], v[120:123], v[0:3]\n"
      "s_nop 2\n"
      "s_waitcnt lgkmcnt(12)\n"
      "v_mfma_f32_16x16x32_bf16 v[4:7], v[96:99], v[124:127], v[4:7]\n"
      "s_nop 2\n"
      "s_waitcnt lgkmcnt(11)\n"
      "v_mfma_f32_16x16x32_bf16 v[8:11], v[96:99], v[128:131], v[8:11]\n"
      "s_nop 2\n"
      "s_waitcnt lgkmcnt(10)\n"
      "v_mfma_f32_16x16x32_bf16 v[12:15], v[96:99], v[132:135], v[12:15]\n"
      "s_nop 2\n"
      "s_barrier\n"
      "s_waitcnt lgkmcnt(8)\n"
      "v_mfma_f32_16x16x32_bf16 v[28:31], v[100:103], v[132:135], v[28:31]\n"
      "s_nop 2\n"
      "ds_read_b64_tr_b16 v[136:137], v184 offset:6144\n"
      "v_add_u32 v209, s56, v211\n"
      "v_mfma_f32_16x16x32_bf16 v[24:27], v[100:103], v[128:131], v[24:27]\n"
      "s_nop 2\n"
      "ds_read_b64_tr_b16 v[138:139], v184 offset:6912\n"
      "v_mov_b32 v192, 0xc0135761\n"
      "v_mfma_f32_16x16x32_bf16 v[20:23], v[100:103], v[124:127], v[20:23]\n"
      "s_nop 2\n"
      "ds_read_b128 v[160:163], v181 offset:0\n"
      "s_mov_b32 s70, 0xbdd2d3e8\n"
      "v_mfma_f32_16x16x32_bf16 v[16:19], v[100:103], v[120:123], v[16:19]\n"
      "s_nop 2\n"
      "ds_read_b128 v[164:167], v181 offset:2048\n"
      "v_xor_b32 v195, 32, v194\n"
      "s_waitcnt lgkmcnt(10)\n"
      "v_mfma_f32_16x16x32_bf16 v[32:35], v[104:107], v[120:123], v[32:35]\n"
      "s_nop 2\n"
      "ds_read_b128 v[168:171], v181 offset:4096\n"
      "v_xor_b32 v196, 64, v194\n"
      "v_mfma_f32_16x16x32_bf16 v[36:39], v[104:107], v[124:127], v[36:39]\n"
      "s_nop 2\n"
      "ds_read_b128 v[172:175], v181 offset:6144\n"
      "v_xor_b32 v197, 96, v194\n"
      "v_mfma_f32_16x16x32_bf16 v[40:43], v[104:107], v[128:131], v[40:43]\n"
      "s_nop 2\n"
      "ds_read_b64_tr_b16 v[140:141], v184 offset:18432\n"
      "v_lshl_add_u32 v198, v250, 4, s61\n"
      "v_mfma_f32_16x16x32_bf16 v[44:47], v[104:107], v[132:135], v[44:47]\n"
      "s_nop 2\n"
      "ds_read_b64_tr_b16 v[142:143], v184 offset:19200\n"
      "v_lshl_add_u32 v199, v250, 4, s62\n"
      "s_waitcnt lgkmcnt(12)\n"
      "v_mfma_f32_16x16x32_bf16 v[60:63], v[108:111], v[132:135], v[60:63]\n"
      "s_nop 2\n"
      "ds_read_b64_tr_b16 v[144:145], v184 offset:6208\n"
      "v_lshrrev_b32 v215, 5, v250\n"
      "v_mfma_f32_16x16x32_bf16 v[56:59], v[108:111], v[128:131], v[56:59]\n"
      "s_nop 2\n"
      "ds_read_b64_tr_b16 v[146:147], v184 offset:6976\n"
      "v_and_b32 v211, 15, v250\n"
      "v_mfma_f32_16x16x32_bf16 v[52:55], v[108:111], v[124:127], v[52:55]\n"
      "s_nop 2\n"
      "ds_read_b64_tr_b16 v[148:149], v184 offset:18496\n"
      "s_lshl_b32 s56, s66, 4\n"
      "v_mfma_f32_16x16x32_bf16 v[48:51], v[108:111], v[120:123], v[48:51]\n"
      "s_nop 2\n"
      "ds_read_b64_tr_b16 v[150:151], v184 offset:19264\n"
      "v_and_b32 v213, 3, v250\n"
      "s_waitcnt lgkmcnt(14)\n"
      "v_mfma_f32_16x16x32_bf16 v[64:67], v[112:115], v[120:123], v[64:67]\n"
      "s_nop 2\n"
      "ds_read_b64_tr_b16 v[152:153], v184 offset:6272\n"
      "v_and_b32 v214, 16, v250\n"
      "v_mfma_f32_16x16x32_bf16 v[68:71], v[112:115], v[124:127], v[68:71]\n"
      "s_nop 2\n"
      "ds_read_b64_tr_b16 v[154:155], v184 offset:7040\n"
      "v_lshlrev_b32 v215, 4, v215\n"
      "v_mfma_f32_16x16x32_bf16 v[72:75], v[112:115], v[128:131], v[72:75]\n"
      "s_nop 2\n"
      "ds_read_b64_tr_b16 v[156:157], v184 offset:18560\n"
      "s_lshl_b32 s57, s44, 12\n"
      "v_mfma_f32_16x16x32_bf16 v[76:79], v[112:115], v[132:135], v[76:79]\n"
      "s_nop 2\n"
      "ds_read_b64_tr_b16 v[158:159], v184 offset:19328\n"
      "v_add_u32 v211, s56, v211\n"
      "s_waitcnt lgkmcnt(15)\n"
      "v_mfma_f32_16x16x32_bf16 v[92:95], v[116:119], v[132:135], v[92:95]\n"
      "s_nop 2\n"
      "v_mfma_f32_16x16x32_bf16 v[88:91], v[116:119], v[128:131], v[88:91]\n"
      "s_nop 2\n"
      "v_mfma_f32_16x16x32_bf16 v[84:87], v[116:119], v[124:127], v[84:87]\n"
      "s_nop 2\n"
      "v_mfma_f32_16x16x32_bf16 v[80:83], v[116:119], v[120:123], v[80:83]\n"
      "s_nop 2\n"
      "s_waitcnt lgkmcnt(13)\n"
      "v_mfma_f32_16x16x32_bf16 v[0:3], v[136:139], v[160:163], v[0:3]\n"
      "s_nop 2\n"
      "s_waitcnt lgkmcnt(12)\n"
      "v_mfma_f32_16x16x32_bf16 v[4:7], v[136:139], v[164:167], v[4:7]\n"
      "s_nop 2\n"
      "s_waitcnt lgkmcnt(11)\n"
      "v_mfma_f32_16x16x32_bf16 v[8:11], v[136:139], v[168:171], v[8:11]\n"
      "s_nop 2\n"
      "s_waitcnt lgkmcnt(10)\n"
      "v_mfma_f32_16x16x32_bf16 v[12:15], v[136:139], v[172:175], v[12:15]\n"
      "s_nop 2\n"
      "s_barrier\n"
      "s_waitcnt lgkmcnt(8)\n"
      "v_mfma_f32_16x16x32_bf16 v[28:31], v[140:143], v[172:175], v[28:31]\n"
      "s_nop 2\n"
      "ds_read_b64_tr_b16 v[96:97], v182 offset:0\n"
      "s_add_u32 s57, s57, 81920\n"
      "v_mfma_f32_16x16x32_bf16 v[24:27], v[140:143], v[168:171], v[24:27]\n"
      "s_nop 2\n"
      "ds_read_b64_tr_b16 v[98:99], v182 offset:768\n"
      "v_lshrrev_b32 v212, 2, v250\n"
      "v_mfma_f32_16x16x32_bf16 v[20:23], v[140:143], v[164:167], v[20:23]\n"
      "s_nop 2\n"
      "ds_read_b128 v[120:123], v176 offset:0\n"
      "v_lshlrev_b32 v213, 3, v213\n"
      "v_mfma_f32_16x16x32_bf16 v[16:19], v[140:143], v[160:163], v[16:19]\n"
      "s_nop 2\n"
      "ds_read_b128 v[124:127], v176 offset:2048\n"
      "v_lshl_add_u32 v214, v214, 1, v215\n"
      "s_waitcnt lgkmcnt(10)\n"
      "v_mfma_f32_16x16x32_bf16 v[32:35], v[144:147], v[160:163], v[32:35]\n"
      "s_nop 2\n"
      "ds_read_b128 v[128:131], v176 offset:4096\n"
      "s_lshl_b32 s58, s44, 5\n"
      "v_mfma_f32_16x16x32_bf16 v[36:39], v[144:147], v[164:167], v[36:39]\n"
      "s_nop 2\n"
      "ds_read_b128 v[132:135], v176 offset:6144\n"
      "v_lshl_add_u32 v212, v212, 7, v213\n"
      "v_mfma_f32_16x16x32_bf16 v[40:43], v[144:147], v[168:171], v[40:43]\n"
      "s_nop 2\n"
      "ds_read_b64_tr_b16 v[100:101], v182 offset:12288\n"
      "s_add_u32 s58, s58, 512\n"
      "v_mfma_f32_16x16x32_bf16 v[44:47], v[144:147], v[172:175], v[44:47]\n"
      "s_nop 2\n"
      "ds_read_b64_tr_b16 v[102:103], v182 offset:13056\n"
      "v_lshl_add_u32 v214, v211, 10, v214\n"
      "s_waitcnt lgkmcnt(12)\n"
      "v_mfma_f32_16x16x32_bf16 v[60:63], v[148:151], v[172:175], v[60:63]\n"
      "s_nop 2\n"
      "ds_read_b64_tr_b16 v[104:105], v182 offset:64\n"
      "s_lshl_b32 s59, s53, 7\n"
      "v_mfma_f32_16x16x32_bf16 v[56:59], v[148:151], v[168:171], v[56:59]\n"
      "s_nop 2\n"
      "ds_read_b64_tr_b16 v[106:107], v182 offset:832\n"
      "v_lshlrev_b32 v208, 2, v211\n"
      "v_mfma_f32_16x16x32_bf16 v[52:55], v[148:151], v[164:167], v[52:55]\n"
      "s_nop 2\n"
      "ds_read_b64_tr_b16 v[108:109], v182 offset:12352\n"
      "v_lshl_add_u32 v205, v250, 4, s57\n"
      "v_mfma_f32_16x16x32_bf16 v[48:51], v[148:151], v[160:163], v[48:51]\n"
      "s_nop 2\n"
      "ds_read_b64_tr_b16 v[110:111], v182 offset:13120\n"
      "v_add_u32 v206, s58, v212\n"
      "s_waitcnt lgkmcnt(14)\n"
      "v_mfma_f32_16x16x32_bf16 v[64:67], v[152:155], v[160:163], v[64:67]\n"
      "s_nop 2\n"
      "ds_read_b64_tr_b16 v[112:113], v182 offset:128\n"
      "v_add_u32 v207, s59, v214\n"
      "v_mfma_f32_16x16x32_bf16 v[68:71], v[152:155], v[164:167], v[68:71]\n"
      "s_nop 2\n"
      "ds_read_b64_tr_b16 v[114:115], v182 offset:896\n"
      "s_mov_b32 s60, 36864\n"
      "v_mfma_f32_16x16x32_bf16 v[72:75], v[152:155], v[168:171], v[72:75]\n"
      "s_nop 2\n"
      "ds_read_b64_tr_b16 v[116:117], v182 offset:12416\n"
      "s_lshl4_add_u32 s56, s49, s51\n"
      "v_mfma_f32_16x16x32_bf16 v[76:79], v[152:155], v[172:175], v[76:79]\n"
      "s_nop 2\n"
      "ds_read_b64_tr_b16 v[118:119], v182 offset:13184\n"
      "s_lshl2_add_u32 s56, s56, s44\n"
      "s_waitcnt lgkmcnt(15)\n"
      "v_mfma_f32_16x16x32_bf16 v[92:95], v[156:159], v[172:175], v[92:95]\n"
      "s_nop 2\n"
      "v_mfma_f32_16x16x32_bf16 v[88:91], v[156:159], v[168:171], v[88:91]\n"
      "s_nop 2\n"
      "v_mfma_f32_16x16x32_bf16 v[84:87], v[156:159], v[164:167], v[84:87]\n"
      "s_nop 2\n"
      "v_mfma_f32_16x16x32_bf16 v[80:83], v[156:159], v[160:163], v[80:83]\n"
      "s_nop 2\n"
      "s_waitcnt lgkmcnt(13)\n"
      "v_mfma_f32_16x16x32_bf16 v[0:3], v[96:99], v[120:123], v[0:3]\n"
      "s_nop 2\n"
      "s_waitcnt lgkmcnt(12)\n"
      "v_mfma_f32_16x16x32_bf16 v[4:7], v[96:99], v[124:127], v[4:7]\n"
      "s_nop 2\n"
      "s_waitcnt lgkmcnt(11)\n"
      "v_mfma_f32_16x16x32_bf16 v[8:11], v[96:99], v[128:131], v[8:11]\n"
      "s_nop 2\n"
      "s_waitcnt lgkmcnt(10)\n"
      "v_mfma_f32_16x16x32_bf16 v[12:15], v[96:99], v[132:135], v[12:15]\n"
      "s_nop 2\n"
      "s_barrier\n"
      "s_waitcnt lgkmcnt(8)\n"
      "v_mfma_f32_16x16x32_bf16 v[28:31], v[100:103], v[132:135], v[28:31]\n"
      "s_nop 2\n"
      "ds_read_b64_tr_b16 v[136:137], v182 offset:6144\n"
      "s_lshl_b32 s56, s56, 7\n"
      "v_mfma_f32_16x16x32_bf16 v[24:27], v[100:103], v[128:131], v[24:27]\n"
      "s_nop 2\n"
      "ds_read_b64_tr_b16 v[138:139], v182 offset:6912\n"
      "s_add_u32 s64, s56, 65536\n"
      "v_mfma_f32_16x16x32_bf16 v[20:23], v[100:103], v[124:127], v[20:23]\n"
      "s_nop 2\n"
      "ds_read_b128 v[160:163], v177 offset:0\n"
      "s_lshr_b32 s57, s52, 1\n"
      "v_mfma_f32_16x16x32_bf16 v[16:19], v[100:103], v[120:123], v[16:19]\n"
      "s_nop 2\n"
      "ds_read_b128 v[164:167], v177 offset:2048\n"
      "s_lshl_b32 s57, s57, 13\n"
      "s_waitcnt lgkmcnt(10)\n"
      "v_mfma_f32_16x16x32_bf16 v[32:35], v[104:107], v[120:123], v[32:35]\n"
      "s_nop 2\n"
      "ds_read_b128 v[168:171], v177 offset:4096\n"
      "s_add_u32 s57, s57, 65536\n"
      "v_mfma_f32_16x16x32_bf16 v[36:39], v[104:107], v[124:127], v[36:39]\n"
      "s_nop 2\n"
      "ds_read_b128 v[172:175], v177 offset:6144\n"
      "s_and_b32 s58, s52, 1\n"
      "v_mfma_f32_16x16x32_bf16 v[40:43], v[104:107], v[128:131], v[40:43]\n"
      "s_nop 2\n"
      "ds_read_b64_tr_b16 v[140:141], v182 offset:18432\n"
      "s_lshl1_add_u32 s58, s83, s58\n"
      "v_mfma_f32_16x16x32_bf16 v[44:47], v[104:107], v[132:135], v[44:47]\n"
      "s_nop 2\n"
      "ds_read_b64_tr_b16 v[142:143], v182 offset:19200\n"
      "v_min_u32 v212, 13, v250\n"
      "s_waitcnt lgkmcnt(12)\n"
      "v_mfma_f32_16x16x32_bf16 v[60:63], v[108:111], v[132:135], v[60:63]\n"
      "s_nop 2\n"
      "ds_read_b64_tr_b16 v[144:145], v182 offset:6208\n"
      "s_lshl_b32 s58, s58, 8\n"
      "v_mfma_f32_16x16x32_bf16 v[56:59], v[108:111], v[128:131], v[56:59]\n"
      "s_nop 2\n"
      "ds_read_b64_tr_b16 v[146:147], v182 offset:6976\n"
      "v_min_u32 v214, 17, v250\n"
      "v_mfma_f32_16x16x32_bf16 v[52:55], v[108:111], v[124:127], v[52:55]\n"
      "s_nop 2\n"
      "ds_read_b64_tr_b16 v[148:149], v182 offset:18496\n"
      "s_add_u32 s58, s58, s57\n"
      "v_mfma_f32_16x16x32_bf16 v[48:51], v[108:111], v[120:123], v[48:51]\n"
      "s_nop 2\n"
      "ds_read_b64_tr_b16 v[150:151], v182 offset:19264\n"
      "v_and_b32 v213, 14, v212\n"
      "s_waitcnt lgkmcnt(14)\n"
      "v_mfma_f32_16x16x32_bf16 v[64:67], v[112:115], v[120:123], v[64:67]\n"
      "s_nop 2\n"
      "ds_read_b64_tr_b16 v[152:153], v182 offset:6272\n"
      "v_and_b32 v215, 30, v214\n"
      "v_mfma_f32_16x16x32_bf16 v[68:71], v[112:115], v[124:127], v[68:71]\n"
      "s_nop 2\n"
      "ds_read_b64_tr_b16 v[154:155], v182 offset:7040\n"
      "s_and_b32 s59, s52, 1\n"
      "v_mfma_f32_16x16x32_bf16 v[72:75], v[112:115], v[128:131], v[72:75]\n"
      "s_nop 2\n"
      "ds_read_b64_tr_b16 v[156:157], v182 offset:18560\n"
      "s_lshl_b32 s59, s59, 8\n"
      "v_mfma_f32_16x16x32_bf16 v[76:79], v[112:115], v[132:135], v[76:79]\n"
      "s_nop 2\n"
      "ds_read_b64_tr_b16 v[158:159], v182 offset:19328\n"
      "v_lshl_add_u32 v211, v250, 1, s83\n"
      "s_waitcnt lgkmcnt(15)\n"
      "v_mfma_f32_16x16x32_bf16 v[92:95], v[116:119], v[132:135], v[92:95]\n"
      "s_nop 2\n"
      "v_mfma_f32_16x16x32_bf16 v[88:91], v[116:119], v[128:131], v[88:91]\n"
      "s_nop 2\n"
      "v_mfma_f32_16x16x32_bf16 v[84:87], v[116:119], v[124:127], v[84:87]\n"
      "s_nop 2\n"
      "v_mfma_f32_16x16x32_bf16 v[80:83], v[116:119], v[120:123], v[80:83]\n"
      "s_nop 2\n"
      "s_waitcnt lgkmcnt(13)\n"
      "v_mfma_f32_16x16x32_bf16 v[0:3], v[136:139], v[160:163], v[0:3]\n"
      "s_waitcnt lgkmcnt(12)\n"
      "v_mfma_f32_16x16x32_bf16 v[4:7], v[136:139], v[164:167], v[4:7]\n"
      "s_waitcnt lgkmcnt(11)\n"
      "v_mfma_f32_16x16x32_bf16 v[8:11], v[136:139], v[168:171], v[8:11]\n"
      "s_waitcnt lgkmcnt(10)\n"
      "v_mfma_f32_16x16x32_bf16 v[12:15], v[136:139], v[172:175], v[12:15]\n"
      "s_barrier\n"
      "s_waitcnt lgkmcnt(8)\n"
      "v_mfma_f32_16x16x32_bf16 v[28:31], v[140:143], v[172:175], v[28:31]\n"
      "ds_read_b64_tr_b16 v[96:97], v183 offset:0\n"
      "v_add_u32 v220, 7, v250\n"
      "v_mfma_f32_16x16x32_bf16 v[24:27], v[140:143], v[168:171], v[24:27]\n"
      "ds_read_b64_tr_b16 v[98:99], v183 offset:768\n"
      "v_mad_u32_u24 v212, v213, 3, v212\n"
      "v_mfma_f32_16x16x32_bf16 v[20:23], v[140:143], v[164:167], v[20:23]\n"
      "ds_read_b128 v[120:123], v178 offset:0\n"
      "v_add_u32 v214, v214, v215\n"
      "v_mfma_f32_16x16x32_bf16 v[16:19], v[140:143], v[160:163], v[16:19]\n"
      "ds_read_b128 v[124:127], v178 offset:2048\n"
      "s_add_u32 s59, s59, s57\n"
      "s_waitcnt lgkmcnt(10)\n"
      "v_mfma_f32_16x16x32_bf16 v[32:35], v[144:147], v[160:163], v[32:35]\n"
      "ds_read_b128 v[128:131], v178 offset:4096\n"
      "s_add_u32 s59, s59, 3584\n"
      "v_mfma_f32_16x16x32_bf16 v[36:39], v[144:147], v[164:167], v[36:39]\n"
      "ds_read_b128 v[132:135], v178 offset:6144\n"
      "v_cmp_gt_u32 vcc, 14, v250\n"
      "v_mfma_f32_16x16x32_bf16 v[40:43], v[144:147], v[168:171], v[40:43]\n"
      "ds_read_b64_tr_b16 v[100:101], v183 offset:12288\n"
      "v_min3_u32 v211, v211, v220, 15\n"
      "v_mfma_f32_16x16x32_bf16 v[44:47], v[144:147], v[172:175], v[44:47]\n"
      "ds_read_b64_tr_b16 v[102:103], v183 offset:13056\n"
      "v_lshl_add_u32 v193, v212, 7, s58\n"
      "s_waitcnt lgkmcnt(12)\n"
      "v_mfma_f32_16x16x32_bf16 v[60:63], v[148:151], v[172:175], v[60:63]\n"
      "ds_read_b64_tr_b16 v[104:105], v183 offset:64\n"
      "v_lshl_add_u32 v214, v214, 7, s59\n"
      "v_mfma_f32_16x16x32_bf16 v[56:59], v[148:151], v[168:171], v[56:59]\n"
      "ds_read_b64_tr_b16 v[106:107], v183 offset:832\n"
      "v_lshl_add_u32 v248, v211, 9, s57\n"
      "v_mfma_f32_16x16x32_bf16 v[52:55], v[148:151], v[164:167], v[52:55]\n"
      "ds_read_b64_tr_b16 v[108:109], v183 offset:12352\n"
      "v_cndmask_b32 v193, v214, v193, vcc\n"
      "v_mfma_f32_16x16x32_bf16 v[48:51], v[148:151], v[160:163], v[48:51]\n"
      "ds_read_b64_tr_b16 v[110:111], v183 offset:13120\n"
      "v_lshrrev_b32 v230, 3, v250\n"
      "s_waitcnt lgkmcnt(14)\n"
      "v_mfma_f32_16x16x32_bf16 v[64:67], v[152:155], v[160:163], v[64:67]\n"
      "ds_read_b64_tr_b16 v[112:113], v183 offset:128\n"
      "s_lshl_b32 s56, s53, 7\n"
      "v_mfma_f32_16x16x32_bf16 v[68:71], v[152:155], v[164:167], v[68:71]\n"
      "ds_read_b64_tr_b16 v[114:115], v183 offset:896\n"
      "v_and_b32 v231, 2, v230\n"
      "v_mfma_f32_16x16x32_bf16 v[72:75], v[152:155], v[168:171], v[72:75]\n"
      "ds_read_b64_tr_b16 v[116:117], v183 offset:12416\n"
      "s_and_b32 s57, s44, 1\n"
      "v_mfma_f32_16x16x32_bf16 v[76:79], v[152:155], v[172:175], v[76:79]\n"
      "ds_read_b64_tr_b16 v[118:119], v183 offset:13184\n"
      "v_xor_b32 v231, v250, v231\n"
      "s_waitcnt lgkmcnt(15)\n"
      "v_mfma_f32_16x16x32_bf16 v[92:95], v[156:159], v[172:175], v[92:95]\n"
      "v_mfma_f32_16x16x32_bf16 v[88:91], v[156:159], v[168:171], v[88:91]\n"
      "v_mfma_f32_16x16x32_bf16 v[84:87], v[156:159], v[164:167], v[84:87]\n"
      "v_mfma_f32_16x16x32_bf16 v[80:83], v[156:159], v[160:163], v[80:83]\n"
      "s_waitcnt lgkmcnt(13)\n"
      "v_mfma_f32_16x16x32_bf16 v[0:3], v[96:99], v[120:123], v[0:3]\n"
      "s_waitcnt lgkmcnt(12)\n"
      "v_mfma_f32_16x16x32_bf16 v[4:7], v[96:99], v[124:127], v[4:7]\n"
      "s_waitcnt lgkmcnt(11)\n"
      "v_mfma_f32_16x16x32_bf16 v[8:11], v[96:99], v[128:131], v[8:11]\n"
      "s_waitcnt lgkmcnt(10)\n"
      "v_mfma_f32_16x16x32_bf16 v[12:15], v[96:99], v[132:135], v[12:15]\n"
      "s_barrier\n"
      "s_waitcnt lgkmcnt(8)\n"
      "v_mfma_f32_16x16x32_bf16 v[28:31], v[100:103], v[132:135], v[28:31]\n"
      "ds_read_b64_tr_b16 v[136:137], v183 offset:6144\n"
      "s_lshl_b32 s57, s57, 6\n"
      "v_mfma_f32_16x16x32_bf16 v[24:27], v[100:103], v[128:131], v[24:27]\n"
      "ds_read_b64_tr_b16 v[138:139], v183 offset:6912\n"
      "v_and_b32 v231, 7, v231\n"
      "v_mfma_f32_16x16x32_bf16 v[20:23], v[100:103], v[124:127], v[20:23]\n"
      "ds_read_b128 v[160:163], v179 offset:0\n"
      "s_lshl_b32 s58, s44, 13\n"
      "v_mfma_f32_16x16x32_bf16 v[16:19], v[100:103], v[120:123], v[16:19]\n"
      "ds_read_b128 v[164:167], v179 offset:2048\n"
      "v_lshl_add_u32 v231, v231, 4, s56\n"
      "s_waitcnt lgkmcnt(10)\n"
      "v_mfma_f32_16x16x32_bf16 v[32:35], v[104:107], v[120:123], v[32:35]\n"
      "ds_read_b128 v[168:171], v179 offset:4096\n"
      "v_lshl_add_u32 v220, v230, 10, v231\n"
      "v_mfma_f32_16x16x32_bf16 v[36:39], v[104:107], v[124:127], v[36:39]\n"
      "ds_read_b128 v[172:175], v179 offset:6144\n"
      "v_xor_b32 v231, 64, v220\n"
      "v_mfma_f32_16x16x32_bf16 v[40:43], v[104:107], v[128:131], v[40:43]\n"
      "ds_read_b64_tr_b16 v[140:141], v183 offset:18432\n"
      "v_xor_b32 v220, s57, v220\n"
      "v_mfma_f32_16x16x32_bf16 v[44:47], v[104:107], v[132:135], v[44:47]\n"
      "ds_read_b64_tr_b16 v[142:143], v183 offset:19200\n"
      "v_add_u32 v221, 7168, v231\n"
      "s_waitcnt lgkmcnt(12)\n"
      "v_mfma_f32_16x16x32_bf16 v[60:63], v[108:111], v[132:135], v[60:63]\n"
      "ds_read_b64_tr_b16 v[144:145], v183 offset:6208\n"
      "v_add_u32 v249, s58, v220\n"
      "v_mfma_f32_16x16x32_bf16 v[56:59], v[108:111], v[128:131], v[56:59]\n"
      "ds_read_b64_tr_b16 v[146:147], v183 offset:6976\n"
      "v_mfma_f32_16x16x32_bf16 v[52:55], v[108:111], v[124:127], v[52:55]\n"
      "ds_read_b64_tr_b16 v[148:149], v183 offset:18496\n"
      "v_mfma_f32_16x16x32_bf16 v[48:51], v[108:111], v[120:123], v[48:51]\n"
      "ds_read_b64_tr_b16 v[150:151], v183 offset:19264\n"
      "s_waitcnt lgkmcnt(14)\n"
      "v_mfma_f32_16x16x32_bf16 v[64:67], v[112:115], v[120:123], v[64:67]\n"
      "ds_read_b64_tr_b16 v[152:153], v183 offset:6272\n"
      "v_mfma_f32_16x16x32_bf16 v[68:71], v[112:115], v[124:127], v[68:71]\n"
      "ds_read_b64_tr_b16 v[154:155], v183 offset:7040\n"
      "v_mfma_f32_16x16x32_bf16 v[72:75], v[112:115], v[128:131], v[72:75]\n"
      "ds_read_b64_tr_b16 v[156:157], v183 offset:18560\n"
      "v_mfma_f32_16x16x32_bf16 v[76:79], v[112:115], v[132:135], v[76:79]\n"
      "ds_read_b64_tr_b16 v[158:159], v183 offset:19328\n"
      "s_waitcnt lgkmcnt(15)\n"
      "v_mfma_f32_16x16x32_bf16 v[92:95], v[116:119], v[132:135], v[92:95]\n"
      "v_mfma_f32_16x16x32_bf16 v[88:91], v[116:119], v[128:131], v[88:91]\n"
      "v_mfma_f32_16x16x32_bf16 v[84:87], v[116:119], v[124:127], v[84:87]\n"
      "v_mfma_f32_16x16x32_bf16 v[80:83], v[116:119], v[120:123], v[80:83]\n"
      "s_waitcnt lgkmcnt(13)\n"
      "v_mfma_f32_16x16x32_bf16 v[0:3], v[136:139], v[160:163], v[0:3]\n"
      "s_waitcnt lgkmcnt(12)\n"
      "v_mfma_f32_16x16x32_bf16 v[4:7], v[136:139], v[164:167], v[4:7]\n"
      "s_waitcnt lgkmcnt(11)\n"
      "v_mfma_f32_16x16x32_bf16 v[8:11], v[136:139], v[168:171], v[8:11]\n"
      "s_waitcnt lgkmcnt(10)\n"
      "v_mfma_f32_16x16x32_bf16 v[12:15], v[136:139], v[172:175], v[12:15]\n"
      "s_barrier\n"
      "s_waitcnt lgkmcnt(8)\n"
      "v_mfma_f32_16x16x32_bf16 v[28:31], v[140:143], v[172:175], v[28:31]\n"
      "ds_read_b64_tr_b16 v[96:97], v184 offset:0\n"
      "v_mfma_f32_16x16x32_bf16 v[24:27], v[140:143], v[168:171], v[24:27]\n"
      "ds_read_b64_tr_b16 v[98:99], v184 offset:768\n"
      "v_mfma_f32_16x16x32_bf16 v[20:23], v[140:143], v[164:167], v[20:23]\n"
      "ds_read_b128 v[120:123], v180 offset:0\n"
      "v_mfma_f32_16x16x32_bf16 v[16:19], v[140:143], v[160:163], v[16:19]\n"
      "ds_read_b128 v[124:127], v180 offset:2048\n"
      "s_waitcnt lgkmcnt(10)\n"
      "v_mfma_f32_16x16x32_bf16 v[32:35], v[144:147], v[160:163], v[32:35]\n"
      "ds_read_b128 v[128:131], v180 offset:4096\n"
      "v_mfma_f32_16x16x32_bf16 v[36:39], v[144:147], v[164:167], v[36:39]\n"
      "ds_read_b128 v[132:135], v180 offset:6144\n"
      "v_mfma_f32_16x16x32_bf16 v[40:43], v[144:147], v[168:171], v[40:43]\n"
      "ds_read_b64_tr_b16 v[100:101], v184 offset:12288\n"
      "v_mfma_f32_16x16x32_bf16 v[44:47], v[144:147], v[172:175], v[44:47]\n"
      "ds_read_b64_tr_b16 v[102:103], v184 offset:13056\n"
      "s_waitcnt lgkmcnt(12)\n"
      "v_mfma_f32_16x16x32_bf16 v[60:63], v[148:151], v[172:175], v[60:63]\n"
      "ds_read_b64_tr_b16 v[104:105], v184 offset:64\n"
      "v_mfma_f32_16x16x32_bf16 v[56:59], v[148:151], v[168:171], v[56:59]\n"
      "ds_read_b64_tr_b16 v[106:107], v184 offset:832\n"
      "v_mfma_f32_16x16x32_bf16 v[52:55], v[148:151], v[164:167], v[52:55]\n"
      "ds_read_b64_tr_b16 v[108:109], v184 offset:12352\n"
      "v_mfma_f32_16x16x32_bf16 v[48:51], v[148:151], v[160:163], v[48:51]\n"
      "ds_read_b64_tr_b16 v[110:111], v184 offset:13120\n"
      "s_waitcnt lgkmcnt(14)\n"
      "v_mfma_f32_16x16x32_bf16 v[64:67], v[152:155], v[160:163], v[64:67]\n"
      "ds_read_b64_tr_b16 v[112:113], v184 offset:128\n"
      "v_mfma_f32_16x16x32_bf16 v[68:71], v[152:155], v[164:167], v[68:71]\n"
      "ds_read_b64_tr_b16 v[114:115], v184 offset:896\n"
      "v_mfma_f32_16x16x32_bf16 v[72:75], v[152:155], v[168:171], v[72:75]\n"
      "ds_read_b64_tr_b16 v[116:117], v184 offset:12416\n"
      "v_mfma_f32_16x16x32_bf16 v[76:79], v[152:155], v[172:175], v[76:79]\n"
      "ds_read_b64_tr_b16 v[118:119], v184 offset:13184\n"
      "s_waitcnt lgkmcnt(15)\n"
      "v_mfma_f32_16x16x32_bf16 v[92:95], v[156:159], v[172:175], v[92:95]\n"
      "v_mfma_f32_16x16x32_bf16 v[88:91], v[156:159], v[168:171], v[88:91]\n"
      "v_mfma_f32_16x16x32_bf16 v[84:87], v[156:159], v[164:167], v[84:87]\n"
      "v_mfma_f32_16x16x32_bf16 v[80:83], v[156:159], v[160:163], v[80:83]\n"
      "s_waitcnt lgkmcnt(13)\n"
      "v_mfma_f32_16x16x32_bf16 v[0:3], v[96:99], v[120:123], v[0:3]\n"
      "s_waitcnt lgkmcnt(12)\n"
      "v_mfma_f32_16x16x32_bf16 v[4:7], v[96:99], v[124:127], v[4:7]\n"
      "s_waitcnt lgkmcnt(11)\n"
      "v_mfma_f32_16x16x32_bf16 v[8:11], v[96:99], v[128:131], v[8:11]\n"
      "s_waitcnt lgkmcnt(10)\n"
      "v_mfma_f32_16x16x32_bf16 v[12:15], v[96:99], v[132:135], v[12:15]\n"
      "s_barrier\n"
      "s_waitcnt lgkmcnt(8)\n"
      "v_mfma_f32_16x16x32_bf16 v[28:31], v[100:103], v[132:135], v[28:31]\n"
      "ds_read_b64_tr_b16 v[136:137], v184 offset:6144\n"
      "v_mfma_f32_16x16x32_bf16 v[24:27], v[100:103], v[128:131], v[24:27]\n"
      "ds_read_b64_tr_b16 v[138:139], v184 offset:6912\n"
      "v_mfma_f32_16x16x32_bf16 v[20:23], v[100:103], v[124:127], v[20:23]\n"
      "ds_read_b128 v[160:163], v181 offset:0\n"
      "v_mfma_f32_16x16x32_bf16 v[16:19], v[100:103], v[120:123], v[16:19]\n"
      "ds_read_b128 v[164:167], v181 offset:2048\n"
      "s_waitcnt lgkmcnt(10)\n"
      "v_mfma_f32_16x16x32_bf16 v[32:35], v[104:107], v[120:123], v[32:35]\n"
      "ds_read_b128 v[168:171], v181 offset:4096\n"
      "v_mfma_f32_16x16x32_bf16 v[36:39], v[104:107], v[124:127], v[36:39]\n"
      "ds_read_b128 v[172:175], v181 offset:6144\n"
      "v_mfma_f32_16x16x32_bf16 v[40:43], v[104:107], v[128:131], v[40:43]\n"
      "ds_read_b64_tr_b16 v[140:141], v184 offset:18432\n"
      "v_mfma_f32_16x16x32_bf16 v[44:47], v[104:107], v[132:135], v[44:47]\n"
      "ds_read_b64_tr_b16 v[142:143], v184 offset:19200\n"
      "s_waitcnt lgkmcnt(12)\n"
      "v_mfma_f32_16x16x32_bf16 v[60:63], v[108:111], v[132:135], v[60:63]\n"
      "ds_read_b64_tr_b16 v[144:145], v184 offset:6208\n"
      "v_mfma_f32_16x16x32_bf16 v[56:59], v[108:111], v[128:131], v[56:59]\n"
      "ds_read_b64_tr_b16 v[146:147], v184 offset:6976\n"
      "v_mfma_f32_16x16x32_bf16 v[52:55], v[108:111], v[124:127], v[52:55]\n"
      "ds_read_b64_tr_b16 v[148:149], v184 offset:18496\n"
      "v_mfma_f32_16x16x32_bf16 v[48:51], v[108:111], v[120:123], v[48:51]\n"
      "ds_read_b64_tr_b16 v[150:151], v184 offset:19264\n"
      "s_waitcnt lgkmcnt(14)\n"
      "v_mfma_f32_16x16x32_bf16 v[64:67], v[112:115], v[120:123], v[64:67]\n"
      "ds_read_b64_tr_b16 v[152:153], v184 offset:6272\n"
      "v_mfma_f32_16x16x32_bf16 v[68:71], v[112:115], v[124:127], v[68:71]\n"
      "ds_read_b64_tr_b16 v[154:155], v184 offset:7040\n"
      "v_mfma_f32_16x16x32_bf16 v[72:75], v[112:115], v[128:131], v[72:75]\n"
      "ds_read_b64_tr_b16 v[156:157], v184 offset:18560\n"
      "v_mfma_f32_16x16x32_bf16 v[76:79], v[112:115], v[132:135], v[76:79]\n"
      "ds_read_b64_tr_b16 v[158:159], v184 offset:19328\n"
      "s_waitcnt lgkmcnt(15)\n"
      "v_mfma_f32_16x16x32_bf16 v[92:95], v[116:119], v[132:135], v[92:95]\n"
      "v_mfma_f32_16x16x32_bf16 v[88:91], v[116:119], v[128:131], v[88:91]\n"
      "v_mfma_f32_16x16x32_bf16 v[84:87], v[116:119], v[124:127], v[84:87]\n"
      "v_mfma_f32_16x16x32_bf16 v[80:83], v[116:119], v[120:123], v[80:83]\n"
      "s_waitcnt lgkmcnt(13)\n"
      "v_mfma_f32_16x16x32_bf16 v[0:3], v[136:139], v[160:163], v[0:3]\n"
      "s_waitcnt lgkmcnt(12)\n"
      "v_mfma_f32_16x16x32_bf16 v[4:7], v[136:139], v[164:167], v[4:7]\n"
      "s_waitcnt lgkmcnt(11)\n"
      "v_mfma_f32_16x16x32_bf16 v[8:11], v[136:139], v[168:171], v[8:11]\n"
      "s_waitcnt lgkmcnt(10)\n"
      "v_mfma_f32_16x16x32_bf16 v[12:15], v[136:139], v[172:175], v[12:15]\n"
      "s_barrier\n"
      "s_waitcnt lgkmcnt(8)\n"
      "v_mfma_f32_16x16x32_bf16 v[28:31], v[140:143], v[172:175], v[28:31]\n"
      "ds_read_b64_tr_b16 v[96:97], v182 offset:0\n"
      "v_mfma_f32_16x16x32_bf16 v[24:27], v[140:143], v[168:171], v[24:27]\n"
      "ds_read_b64_tr_b16 v[98:99], v182 offset:768\n"
      "v_mfma_f32_16x16x32_bf16 v[20:23], v[140:143], v[164:167], v[20:23]\n"
      "ds_read_b128 v[120:123], v176 offset:0\n"
      "v_mfma_f32_16x16x32_bf16 v[16:19], v[140:143], v[160:163], v[16:19]\n"
      "ds_read_b128 v[124:127], v176 offset:2048\n"
      "s_waitcnt lgkmcnt(10)\n"
      "v_mfma_f32_16x16x32_bf16 v[32:35], v[144:147], v[160:163], v[32:35]\n"
      "ds_read_b128 v[128:131], v176 offset:4096\n"
      "v_mfma_f32_16x16x32_bf16 v[36:39], v[144:147], v[164:167], v[36:39]\n"
      "ds_read_b128 v[132:135], v176 offset:6144\n"
      "v_mfma_f32_16x16x32_bf16 v[40:43], v[144:147], v[168:171], v[40:43]\n"
      "ds_read_b64_tr_b16 v[100:101], v182 offset:12288\n"
      "v_mfma_f32_16x16x32_bf16 v[44:47], v[144:147], v[172:175], v[44:47]\n"
      "ds_read_b64_tr_b16 v[102:103], v182 offset:13056\n"
      "s_waitcnt lgkmcnt(12)\n"
      "v_mfma_f32_16x16x32_bf16 v[60:63], v[148:151], v[172:175], v[60:63]\n"
      "ds_read_b64_tr_b16 v[104:105], v182 offset:64\n"
      "v_mfma_f32_16x16x32_bf16 v[56:59], v[148:151], v[168:171], v[56:59]\n"
      "ds_read_b64_tr_b16 v[106:107], v182 offset:832\n"
      "v_mfma_f32_16x16x32_bf16 v[52:55], v[148:151], v[164:167], v[52:55]\n"
      "ds_read_b64_tr_b16 v[108:109], v182 offset:12352\n"
      "v_mfma_f32_16x16x32_bf16 v[48:51], v[148:151], v[160:163], v[48:51]\n"
      "ds_read_b64_tr_b16 v[110:111], v182 offset:13120\n"
      "s_waitcnt lgkmcnt(14)\n"
      "v_mfma_f32_16x16x32_bf16 v[64:67], v[152:155], v[160:163], v[64:67]\n"
      "ds_read_b64_tr_b16 v[112:113], v182 offset:128\n"
      "v_mfma_f32_16x16x32_bf16 v[68:71], v[152:155], v[164:167], v[68:71]\n"
      "ds_read_b64_tr_b16 v[114:115], v182 offset:896\n"
      "v_mfma_f32_16x16x32_bf16 v[72:75], v[152:155], v[168:171], v[72:75]\n"
      "ds_read_b64_tr_b16 v[116:117], v182 offset:12416\n"
      "v_mfma_f32_16x16x32_bf16 v[76:79], v[152:155], v[172:175], v[76:79]\n"
      "ds_read_b64_tr_b16 v[118:119], v182 offset:13184\n"
      "s_waitcnt lgkmcnt(15)\n"
      "v_mfma_f32_16x16x32_bf16 v[92:95], v[156:159], v[172:175], v[92:95]\n"
      "v_mfma_f32_16x16x32_bf16 v[88:91], v[156:159], v[168:171], v[88:91]\n"
      "v_mfma_f32_16x16x32_bf16 v[84:87], v[156:159], v[164:167], v[84:87]\n"
      "v_mfma_f32_16x16x32_bf16 v[80:83], v[156:159], v[160:163], v[80:83]\n"
      "s_waitcnt lgkmcnt(13)\n"
      "v_mfma_f32_16x16x32_bf16 v[0:3], v[96:99], v[120:123], v[0:3]\n"
      "s_waitcnt lgkmcnt(12)\n"
      "v_mfma_f32_16x16x32_bf16 v[4:7], v[96:99], v[124:127], v[4:7]\n"
      "s_waitcnt lgkmcnt(11)\n"
      "v_mfma_f32_16x16x32_bf16 v[8:11], v[96:99], v[128:131], v[8:11]\n"
      "s_waitcnt lgkmcnt(10)\n"
      "v_mfma_f32_16x16x32_bf16 v[12:15], v[96:99], v[132:135], v[12:15]\n"
      "s_barrier\n"
      "s_waitcnt lgkmcnt(8)\n"
      "v_mfma_f32_16x16x32_bf16 v[28:31], v[100:103], v[132:135], v[28:31]\n"
      "ds_read_b64_tr_b16 v[136:137], v182 offset:6144\n"
      "v_mfma_f32_16x16x32_bf16 v[24:27], v[100:103], v[128:131], v[24:27]\n"
      "ds_read_b64_tr_b16 v[138:139], v182 offset:6912\n"
      "v_mfma_f32_16x16x32_bf16 v[20:23], v[100:103], v[124:127], v[20:23]\n"
      "ds_read_b128 v[160:163], v177 offset:0\n"
      "v_mfma_f32_16x16x32_bf16 v[16:19], v[100:103], v[120:123], v[16:19]\n"
      "ds_read_b128 v[164:167], v177 offset:2048\n"
      "s_waitcnt lgkmcnt(10)\n"
      "v_mfma_f32_16x16x32_bf16 v[32:35], v[104:107], v[120:123], v[32:35]\n"
      "ds_read_b128 v[168:171], v177 offset:4096\n"
      "v_mfma_f32_16x16x32_bf16 v[36:39], v[104:107], v[124:127], v[36:39]\n"
      "ds_read_b128 v[172:175], v177 offset:6144\n"
      "v_mfma_f32_16x16x32_bf16 v[40:43], v[104:107], v[128:131], v[40:43]\n"
      "ds_read_b64_tr_b16 v[140:141], v182 offset:18432\n"
      "v_mfma_f32_16x16x32_bf16 v[44:47], v[104:107], v[132:135], v[44:47]\n"
      "ds_read_b64_tr_b16 v[142:143], v182 offset:19200\n"
      "s_waitcnt lgkmcnt(12)\n"
      "v_mfma_f32_16x16x32_bf16 v[60:63], v[108:111], v[132:135], v[60:63]\n"
      "ds_read_b64_tr_b16 v[144:145], v182 offset:6208\n"
      "v_mfma_f32_16x16x32_bf16 v[56:59], v[108:111], v[128:131], v[56:59]\n"
      "ds_read_b64_tr_b16 v[146:147], v182 offset:6976\n"
      "v_mfma_f32_16x16x32_bf16 v[52:55], v[108:111], v[124:127], v[52:55]\n"
      "ds_read_b64_tr_b16 v[148:149], v182 offset:18496\n"
      "v_mfma_f32_16x16x32_bf16 v[48:51], v[108:111], v[120:123], v[48:51]\n"
      "ds_read_b64_tr_b16 v[150:151], v182 offset:19264\n"
      "s_waitcnt lgkmcnt(14)\n"
      "v_mfma_f32_16x16x32_bf16 v[64:67], v[112:115], v[120:123], v[64:67]\n"
      "ds_read_b64_tr_b16 v[152:153], v182 offset:6272\n"
      "v_mfma_f32_16x16x32_bf16 v[68:71], v[112:115], v[124:127], v[68:71]\n"
      "ds_read_b64_tr_b16 v[154:155], v182 offset:7040\n"
      "v_mfma_f32_16x16x32_bf16 v[72:75], v[112:115], v[128:131], v[72:75]\n"
      "ds_read_b64_tr_b16 v[156:157], v182 offset:18560\n"
      "v_mfma_f32_16x16x32_bf16 v[76:79], v[112:115], v[132:135], v[76:79]\n"
      "ds_read_b64_tr_b16 v[158:159], v182 offset:19328\n"
      "s_waitcnt lgkmcnt(15)\n"
      "v_mfma_f32_16x16x32_bf16 v[92:95], v[116:119], v[132:135], v[92:95]\n"
      "v_mfma_f32_16x16x32_bf16 v[88:91], v[116:119], v[128:131], v[88:91]\n"
      "v_mfma_f32_16x16x32_bf16 v[84:87], v[116:119], v[124:127], v[84:87]\n"
      "v_mfma_f32_16x16x32_bf16 v[80:83], v[116:119], v[120:123], v[80:83]\n"
      "s_waitcnt lgkmcnt(13)\n"
      "v_mfma_f32_16x16x32_bf16 v[0:3], v[136:139], v[160:163], v[0:3]\n"
      "s_waitcnt lgkmcnt(12)\n"
      "v_mfma_f32_16x16x32_bf16 v[4:7], v[136:139], v[164:167], v[4:7]\n"
      "s_waitcnt lgkmcnt(11)\n"
      "v_mfma_f32_16x16x32_bf16 v[8:11], v[136:139], v[168:171], v[8:11]\n"
      "s_waitcnt lgkmcnt(10)\n"
      "v_mfma_f32_16x16x32_bf16 v[12:15], v[136:139], v[172:175], v[12:15]\n"
      "s_barrier\n"
      "s_waitcnt lgkmcnt(8)\n"
      "v_mfma_f32_16x16x32_bf16 v[28:31], v[140:143], v[172:175], v[28:31]\n"
      "ds_read_b64_tr_b16 v[96:97], v183 offset:0\n"
      "v_mfma_f32_16x16x32_bf16 v[24:27], v[140:143], v[168:171], v[24:27]\n"
      "ds_read_b64_tr_b16 v[98:99], v183 offset:768\n"
      "v_mfma_f32_16x16x32_bf16 v[20:23], v[140:143], v[164:167], v[20:23]\n"
      "ds_read_b128 v[120:123], v178 offset:0\n"
      "v_mfma_f32_16x16x32_bf16 v[16:19], v[140:143], v[160:163], v[16:19]\n"
      "ds_read_b128 v[124:127], v178 offset:2048\n"
      "s_waitcnt lgkmcnt(10)\n"
      "v_mfma_f32_16x16x32_bf16 v[32:35], v[144:147], v[160:163], v[32:35]\n"
      "ds_read_b128 v[128:131], v178 offset:4096\n"
      "v_mfma_f32_16x16x32_bf16 v[36:39], v[144:147], v[164:167], v[36:39]\n"
      "ds_read_b128 v[132:135], v178 offset:6144\n"
      "v_mfma_f32_16x16x32_bf16 v[40:43], v[144:147], v[168:171], v[40:43]\n"
      "ds_read_b64_tr_b16 v[100:101], v183 offset:12288\n"
      "v_mfma_f32_16x16x32_bf16 v[44:47], v[144:147], v[172:175], v[44:47]\n"
      "ds_read_b64_tr_b16 v[102:103], v183 offset:13056\n"
      "s_waitcnt lgkmcnt(12)\n"
      "v_mfma_f32_16x16x32_bf16 v[60:63], v[148:151], v[172:175], v[60:63]\n"
      "ds_read_b64_tr_b16 v[104:105], v183 offset:64\n"
      "v_mfma_f32_16x16x32_bf16 v[56:59], v[148:151], v[168:171], v[56:59]\n"
      "ds_read_b64_tr_b16 v[106:107], v183 offset:832\n"
      "v_mfma_f32_16x16x32_bf16 v[52:55], v[148:151], v[164:167], v[52:55]\n"
      "ds_read_b64_tr_b16 v[108:109], v183 offset:12352\n"
      "v_mfma_f32_16x16x32_bf16 v[48:51], v[148:151], v[160:163], v[48:51]\n"
      "ds_read_b64_tr_b16 v[110:111], v183 offset:13120\n"
      "s_waitcnt lgkmcnt(14)\n"
      "v_mfma_f32_16x16x32_bf16 v[64:67], v[152:155], v[160:163], v[64:67]\n"
      "ds_read_b64_tr_b16 v[112:113], v183 offset:128\n"
      "v_mfma_f32_16x16x32_bf16 v[68:71], v[152:155], v[164:167], v[68:71]\n"
      "ds_read_b64_tr_b16 v[114:115], v183 offset:896\n"
      "v_mfma_f32_16x16x32_bf16 v[72:75], v[152:155], v[168:171], v[72:75]\n"
      "ds_read_b64_tr_b16 v[116:117], v183 offset:12416\n"
      "v_mfma_f32_16x16x32_bf16 v[76:79], v[152:155], v[172:175], v[76:79]\n"
      "ds_read_b64_tr_b16 v[118:119], v183 offset:13184\n"
      "s_waitcnt lgkmcnt(15)\n"
      "v_mfma_f32_16x16x32_bf16 v[92:95], v[156:159], v[172:175], v[92:95]\n"
      "v_mfma_f32_16x16x32_bf16 v[88:91], v[156:159], v[168:171], v[88:91]\n"
      "v_mfma_f32_16x16x32_bf16 v[84:87], v[156:159], v[164:167], v[84:87]\n"
      "v_mfma_f32_16x16x32_bf16 v[80:83], v[156:159], v[160:163], v[80:83]\n"
      "s_waitcnt lgkmcnt(13)\n"
      "v_mfma_f32_16x16x32_bf16 v[0:3], v[96:99], v[120:123], v[0:3]\n"
      "s_waitcnt lgkmcnt(12)\n"
      "v_mfma_f32_16x16x32_bf16 v[4:7], v[96:99], v[124:127], v[4:7]\n"
      "s_waitcnt lgkmcnt(11)\n"
      "v_mfma_f32_16x16x32_bf16 v[8:11], v[96:99], v[128:131], v[8:11]\n"
      "s_waitcnt lgkmcnt(10)\n"
      "v_mfma_f32_16x16x32_bf16 v[12:15], v[96:99], v[132:135], v[12:15]\n"
      "s_barrier\n"
      "s_waitcnt lgkmcnt(8)\n"
      "v_mfma_f32_16x16x32_bf16 v[28:31], v[100:103], v[132:135], v[28:31]\n"
      "ds_read_b64_tr_b16 v[136:137], v183 offset:6144\n"
      "v_mfma_f32_16x16x32_bf16 v[24:27], v[100:103], v[128:131], v[24:27]\n"
      "ds_read_b64_tr_b16 v[138:139], v183 offset:6912\n"
      "v_mfma_f32_16x16x32_bf16 v[20:23], v[100:103], v[124:127], v[20:23]\n"
      "ds_read_b128 v[160:163], v179 offset:0\n"
      "v_mfma_f32_16x16x32_bf16 v[16:19], v[100:103], v[120:123], v[16:19]\n"
      "ds_read_b128 v[164:167], v179 offset:2048\n"
      "s_waitcnt lgkmcnt(10)\n"
      "v_mfma_f32_16x16x32_bf16 v[32:35], v[104:107], v[120:123], v[32:35]\n"
      "ds_read_b128 v[168:171], v179 offset:4096\n"
      "v_mfma_f32_16x16x32_bf16 v[36:39], v[104:107], v[124:127], v[36:39]\n"
      "ds_read_b128 v[172:175], v179 offset:6144\n"
      "v_mfma_f32_16x16x32_bf16 v[40:43], v[104:107], v[128:131], v[40:43]\n"
      "ds_read_b64_tr_b16 v[140:141], v183 offset:18432\n"
      "v_mfma_f32_16x16x32_bf16 v[44:47], v[104:107], v[132:135], v[44:47]\n"
      "ds_read_b64_tr_b16 v[142:143], v183 offset:19200\n"
      "s_waitcnt lgkmcnt(12)\n"
      "v_mfma_f32_16x16x32_bf16 v[60:63], v[108:111], v[132:135], v[60:63]\n"
      "ds_read_b64_tr_b16 v[144:145], v183 offset:6208\n"
      "v_mfma_f32_16x16x32_bf16 v[56:59], v[108:111], v[128:131], v[56:59]\n"
      "ds_read_b64_tr_b16 v[146:147], v183 offset:6976\n"
      "v_mfma_f32_16x16x32_bf16 v[52:55], v[108:111], v[124:127], v[52:55]\n"
      "ds_read_b64_tr_b16 v[148:149], v183 offset:18496\n"
      "v_mfma_f32_16x16x32_bf16 v[48:51], v[108:111], v[120:123], v[48:51]\n"
      "ds_read_b64_tr_b16 v[150:151], v183 offset:19264\n"
      "s_waitcnt lgkmcnt(14)\n"
      "v_mfma_f32_16x16x32_bf16 v[64:67], v[112:115], v[120:123], v[64:67]\n"
      "ds_read_b64_tr_b16 v[152:153], v183 offset:6272\n"
      "v_mfma_f32_16x16x32_bf16 v[68:71], v[112:115], v[124:127], v[68:71]\n"
      "ds_read_b64_tr_b16 v[154:155], v183 offset:7040\n"
      "v_mfma_f32_16x16x32_bf16 v[72:75], v[112:115], v[128:131], v[72:75]\n"
      "ds_read_b64_tr_b16 v[156:157], v183 offset:18560\n"
      "v_mfma_f32_16x16x32_bf16 v[76:79], v[112:115], v[132:135], v[76:79]\n"
      "ds_read_b64_tr_b16 v[158:159], v183 offset:19328\n"
      "s_waitcnt lgkmcnt(15)\n"
      "v_mfma_f32_16x16x32_bf16 v[92:95], v[116:119], v[132:135], v[92:95]\n"
      "v_mfma_f32_16x16x32_bf16 v[88:91], v[116:119], v[128:131], v[88:91]\n"
      "v_mfma_f32_16x16x32_bf16 v[84:87], v[116:119], v[124:127], v[84:87]\n"
      "v_mfma_f32_16x16x32_bf16 v[80:83], v[116:119], v[120:123], v[80:83]\n"
      "s_waitcnt lgkmcnt(13)\n"
      "v_mfma_f32_16x16x32_bf16 v[0:3], v[136:139], v[160:163], v[0:3]\n"
      "s_waitcnt lgkmcnt(12)\n"
      "v_mfma_f32_16x16x32_bf16 v[4:7], v[136:139], v[164:167], v[4:7]\n"
      "s_waitcnt lgkmcnt(11)\n"
      "v_mfma_f32_16x16x32_bf16 v[8:11], v[136:139], v[168:171], v[8:11]\n"
      "s_waitcnt lgkmcnt(10)\n"
      "v_mfma_f32_16x16x32_bf16 v[12:15], v[136:139], v[172:175], v[12:15]\n"
      "s_waitcnt lgkmcnt(0)\n"
      "s_barrier\n"
      "v_mfma_f32_16x16x32_bf16 v[28:31], v[140:143], v[172:175], v[28:31]\n"
      "v_mfma_f32_16x16x32_bf16 v[24:27], v[140:143], v[168:171], v[24:27]\n"
      "v_mfma_f32_16x16x32_bf16 v[20:23], v[140:143], v[164:167], v[20:23]\n"
      "v_mfma_f32_16x16x32_bf16 v[16:19], v[140:143], v[160:163], v[16:19]\n"
      "v_mfma_f32_16x16x32_bf16 v[32:35], v[144:147], v[160:163], v[32:35]\n"
      "v_mfma_f32_16x16x32_bf16 v[36:39], v[144:147], v[164:167], v[36:39]\n"
      "v_mfma_f32_16x16x32_bf16 v[40:43], v[144:147], v[168:171], v[40:43]\n"
      "v_mfma_f32_16x16x32_bf16 v[44:47], v[144:147], v[172:175], v[44:47]\n"
      "v_mfma_f32_16x16x32_bf16 v[60:63], v[148:151], v[172:175], v[60:63]\n"
      "v_mfma_f32_16x16x32_bf16 v[56:59], v[148:151], v[168:171], v[56:59]\n"
      "v_mfma_f32_16x16x32_bf16 v[52:55], v[148:151], v[164:167], v[52:55]\n"
      "v_mfma_f32_16x16x32_bf16 v[48:51], v[148:151], v[160:163], v[48:51]\n"
      "v_mfma_f32_16x16x32_bf16 v[64:67], v[152:155], v[160:163], v[64:67]\n"
      "v_mfma_f32_16x16x32_bf16 v[68:71], v[152:155], v[164:167], v[68:71]\n"
      "v_mfma_f32_16x16x32_bf16 v[72:75], v[152:155], v[168:171], v[72:75]\n"
      "v_mfma_f32_16x16x32_bf16 v[76:79], v[152:155], v[172:175], v[76:79]\n"
      "v_mfma_f32_16x16x32_bf16 v[92:95], v[156:159], v[172:175], v[92:95]\n"
      "v_mfma_f32_16x16x32_bf16 v[88:91], v[156:159], v[168:171], v[88:91]\n"
      "v_mfma_f32_16x16x32_bf16 v[84:87], v[156:159], v[164:167], v[84:87]\n"
      "v_mfma_f32_16x16x32_bf16 v[80:83], v[156:159], v[160:163], v[80:83]\n"
      "Lm43_%=:\n"
      ".p2align 3\n"
      "s_cmp_eq_u32 s51, 15\n"
      "s_cbranch_scc1 Lg44_%=\n"
      "v_pk_mul_f32 v[216:217], v[0:1], v[0:1]\n"
      "v_pk_mul_f32 v[218:219], v[2:3], v[2:3]\n"
      "v_pk_mul_f32 v[220:221], v[4:5], v[4:5]\n"
      "v_pk_mul_f32 v[222:223], v[6:7], v[6:7]\n"
      "v_pk_fma_f32 v[216:217], v[216:217], s[70:71], v[192:193] op_sel_hi:[1,0,0]\n"
      "v_pk_fma_f32 v[218:219], v[218:219], s[70:71], v[192:193] op_sel_hi:[1,0,0]\n"
      "v_pk_fma_f32 v[220:221], v[220:221], s[70:71], v[192:193] op_sel_hi:[1,0,0]\n"
      "v_pk_fma_f32 v[222:223], v[222:223], s[70:71], v[192:193] op_sel_hi:[1,0,0]\n"
      "v_pk_mul_f32 v[216:217], v[216:217], v[0:1]\n"
      "v_pk_mul_f32 v[218:219], v[218:219], v[2:3]\n"
      "v_pk_mul_f32 v[220:221], v[220:221], v[4:5]\n"
      "v_pk_mul_f32 v[222:223], v[222:223], v[6:7]\n"
      "v_exp_f32 v216, v216\n"
      "v_exp_f32 v217, v217\n"
      "v_exp_f32 v218, v218\n"
      "v_exp_f32 v219, v219\n"
      "v_exp_f32 v220, v220\n"
      "v_exp_f32 v221, v221\n"
      "v_exp_f32 v222, v222\n"
      "v_exp_f32 v223, v223\n"
      "v_pk_add_f32 v[216:217], v[216:217], 1.0 op_sel_hi:[1,0]\n"
      "v_pk_add_f32 v[218:219], v[218:219], 1.0 op_sel_hi:[1,0]\n"
      "v_pk_add_f32 v[220:221], v[220:221], 1.0 op_sel_hi:[1,0]\n"
      "v_pk_mul_f32 v[16:17], v[16:17], v[0:1]\n"
      "v_pk_add_f32 v[222:223], v[222:223], 1.0 op_sel_hi:[1,0]\n"
      "v_rcp_f32 v216, v216\n"
      "v_rcp_f32 v217, v217\n"
      "v_rcp_f32 v218, v218\n"
      "v_rcp_f32 v219, v219\n"
      "v_rcp_f32 v220, v220\n"
      "v_rcp_f32 v221, v221\n"
      "v_rcp_f32 v222, v222\n"
      "v_rcp_f32 v223, v223\n"
      "v_pk_mul_f32 v[18:19], v[18:19], v[2:3]\n"
      "v_pk_mul_f32 v[20:21], v[20:21], v[4:5]\n"
      "v_pk_mul_f32 v[22:23], v[22:23], v[6:7]\n"
      "v_pk_mul_f32 v[16:17], v[16:17], v[216:217]\n"
      "v_pk_mul_f32 v[20:21], v[20:21], v[220:221]\n"
      "v_pk_mul_f32 v[18:19], v[18:19], v[218:219]\n"
      "v_pk_mul_f32 v[22:23], v[22:23], v[222:223]\n"
      "v_cvt_pk_bf16_f32 v0, v16, v17\n"
      "v_cvt_pk_bf16_f32 v2, v20, v21\n"
      "v_cvt_pk_bf16_f32 v1, v18, v19\n"
      "v_cvt_pk_bf16_f32 v3, v22, v23\n"
      "s_add_u32 s56, s65, 0\n"
      "v_permlane16_swap_b32 v0, v2\n"
      "v_permlane16_swap_b32 v1, v3\n"
      "buffer_store_dwordx4 v[0:3], v209, s[24:27], s56 offen sc1\n"
      "v_pk_mul_f32 v[224:225], v[8:9], v[8:9]\n"
      "v_pk_mul_f32 v[226:227], v[10:11], v[10:11]\n"
      "v_pk_mul_f32 v[228:229], v[12:13], v[12:13]\n"
      "v_pk_mul_f32 v[230:231], v[14:15], v[14:15]\n"
      "v_pk_fma_f32 v[224:225], v[224:225], s[70:71], v[192:193] op_sel_hi:[1,0,0]\n"
      "v_pk_fma_f32 v[226:227], v[226:227], s[70:71], v[192:193] op_sel_hi:[1,0,0]\n"
      "v_pk_fma_f32 v[228:229], v[228:229], s[70:71], v[192:193] op_sel_hi:[1,0,0]\n"
      "v_pk_fma_f32 v[230:231], v[230:231], s[70:71], v[192:193] op_sel_hi:[1,0,0]\n"
      "v_pk_mul_f32 v[224:225], v[224:225], v[8:9]\n"
      "v_pk_mul_f32 v[226:227], v[226:227], v[10:11]\n"
      "v_pk_mul_f32 v[228:229], v[228:229], v[12:13]\n"
      "v_pk_mul_f32 v[230:231], v[230:231], v[14:15]\n"
      "v_exp_f32 v224, v224\n"
      "v_exp_f32 v225, v225\n"
      "v_exp_f32 v226, v226\n"
      "v_exp_f32 v227, v227\n"
      "v_exp_f32 v228, v228\n"
      "v_exp_f32 v229, v229\n"
      "v_exp_f32 v230, v230\n"
      "v_exp_f32 v231, v231\n"
      "v_pk_add_f32 v[224:225], v[224:225], 1.0 op_sel_hi:[1,0]\n"
      "v_pk_add_f32 v[226:227], v[226:227], 1.0 op_sel_hi:[1,0]\n"
      "v_pk_add_f32 v[228:229], v[228:229], 1.0 op_sel_hi:[1,0]\n"
      "v_pk_mul_f32 v[24:25], v[24:25], v[8:9]\n"
      "v_pk_add_f32 v[230:231], v[230:231], 1.0 op_sel_hi:[1,0]\n"
      "v_rcp_f32 v224, v224\n"
      "v_rcp_f32 v225, v225\n"
      "v_rcp_f32 v226, v226\n"
      "v_rcp_f32 v227, v227\n"
      "v_rcp_f32 v228, v228\n"
      "v_rcp_f32 v229, v229\n"
      "v_rcp_f32 v230, v230\n"
      "v_rcp_f32 v231, v231\n"
      "v_pk_mul_f32 v[26:27], v[26:27], v[10:11]\n"
      "v_pk_mul_f32 v[28:29], v[28:29], v[12:13]\n"
      "v_pk_mul_f32 v[30:31], v[30:31], v[14:15]\n"
      "v_pk_mul_f32 v[24:25], v[24:25], v[224:225]\n"
      "v_pk_mul_f32 v[28:29], v[28:29], v[228:229]\n"
      "v_pk_mul_f32 v[26:27], v[26:27], v[226:227]\n"
      "v_pk_mul_f32 v[30:31], v[30:31], v[230:231]\n"
      "v_cvt_pk_bf16_f32 v8, v24, v25\n"
      "v_cvt_pk_bf16_f32 v10, v28, v29\n"
      "v_cvt_pk_bf16_f32 v9, v26, v27\n"
      "v_cvt_pk_bf16_f32 v11, v30, v31\n"
      "s_add_u32 s56, s65, 88064\n"
      "v_permlane16_swap_b32 v8, v10\n"
      "v_permlane16_swap_b32 v9, v11\n"
      "buffer_store_dwordx4 v[8:11], v209, s[24:27], s56 offen sc1\n"
      "s_cmp_eq_u32 s51, 14\n"
      "s_cbranch_scc1 Lc46_%=\n"
      "v_pk_mul_f32 v[216:217], v[32:33], v[32:33]\n"
      "v_pk_mul_f32 v[218:219], v[34:35], v[34:35]\n"
      "v_pk_mul_f32 v[220:221], v[36:37], v[36:37]\n"
      "v_pk_mul_f32 v[222:223], v[38:39], v[38:39]\n"
      "v_pk_fma_f32 v[216:217], v[216:217], s[70:71], v[192:193] op_sel_hi:[1,0,0]\n"
      "v_pk_fma_f32 v[218:219], v[218:219], s[70:71], v[192:193] op_sel_hi:[1,0,0]\n"
      "v_pk_fma_f32 v[220:221], v[220:221], s[70:71], v[192:193] op_sel_hi:[1,0,0]\n"
      "v_pk_fma_f32 v[222:223], v[222:223], s[70:71], v[192:193] op_sel_hi:[1,0,0]\n"
      "v_pk_mul_f32 v[216:217], v[216:217], v[32:33]\n"
      "v_pk_mul_f32 v[218:219], v[218:219], v[34:35]\n"
      "v_pk_mul_f32 v[220:221], v[220:221], v[36:37]\n"
      "v_pk_mul_f32 v[222:223], v[222:223], v[38:39]\n"
      "v_exp_f32 v216, v216\n"
      "v_exp_f32 v217, v217\n"
      "v_exp_f32 v218, v218\n"
      "v_exp_f32 v219, v219\n"
      "v_exp_f32 v220, v220\n"
      "v_exp_f32 v221, v221\n"
      "v_exp_f32 v222, v222\n"
      "v_exp_f32 v223, v223\n"
      "v_pk_add_f32 v[216:217], v[216:217], 1.0 op_sel_hi:[1,0]\n"
      "v_pk_add_f32 v[218:219], v[218:219], 1.0 op_sel_hi:[1,0]\n"
      "v_pk_add_f32 v[220:221], v[220:221], 1.0 op_sel_hi:[1,0]\n"
      "v_pk_mul_f32 v[48:49], v[48:49], v[32:33]\n"
      "v_pk_add_f32 v[222:223], v[222:223], 1.0 op_sel_hi:[1,0]\n"
      "v_rcp_f32 v216, v216\n"
      "v_rcp_f32 v217, v217\n"
      "v_rcp_f32 v218, v218\n"
      "v_rcp_f32 v219, v219\n"
      "v_rcp_f32 v220, v220\n"
      "v_rcp_f32 v221, v221\n"
      "v_rcp_f32 v222, v222\n"
      "v_rcp_f32 v223, v223\n"
      "v_pk_mul_f32 v[50:51], v[50:51], v[34:35]\n"
      "v_pk_mul_f32 v[52:53], v[52:53], v[36:37]\n"
      "v_pk_mul_f32 v[54:55], v[54:55], v[38:39]\n"
      "v_pk_mul_f32 v[48:49], v[48:49], v[216:217]\n"
      "v_pk_mul_f32 v[52:53], v[52:53], v[220:221]\n"
      "v_pk_mul_f32 v[50:51], v[50:51], v[218:219]\n"
      "v_pk_mul_f32 v[54:55], v[54:55], v[222:223]\n"
      "v_cvt_pk_bf16_f32 v32, v48, v49\n"
      "v_cvt_pk_bf16_f32 v34, v52, v53\n"
      "v_cvt_pk_bf16_f32 v33, v50, v51\n"
      "v_cvt_pk_bf16_f32 v35, v54, v55\n"
      "s_add_u32 s56, s65, 1024\n"
      "v_permlane16_swap_b32 v32, v34\n"
      "v_permlane16_swap_b32 v33, v35\n"
      "buffer_store_dwordx4 v[32:35], v209, s[24:27], s56 offen sc1\n"
      "v_pk_mul_f32 v[224:225], v[40:41], v[40:41]\n"
      "v_pk_mul_f32 v[226:227], v[42:43], v[42:43]\n"
      "v_pk_mul_f32 v[228:229], v[44:45], v[44:45]\n"
      "v_pk_mul_f32 v[230:231], v[46:47], v[46:47]\n"
      "v_pk_fma_f32 v[224:225], v[224:225], s[70:71], v[192:193] op_sel_hi:[1,0,0]\n"
      "v_pk_fma_f32 v[226:227], v[226:227], s[70:71], v[192:193] op_sel_hi:[1,0,0]\n"
      "v_pk_fma_f32 v[228:229], v[228:229], s[70:71], v[192:193] op_sel_hi:[1,0,0]\n"
      "v_pk_fma_f32 v[230:231], v[230:231], s[70:71], v[192:193] op_sel_hi:[1,0,0]\n"
      "v_pk_mul_f32 v[224:225], v[224:225], v[40:41]\n"
      "v_pk_mul_f32 v[226:227], v[226:227], v[42:43]\n"
      "v_pk_mul_f32 v[228:229], v[228:229], v[44:45]\n"
      "v_pk_mul_f32 v[230:231], v[230:231], v[46:47]\n"
      "v_exp_f32 v224, v224\n"
      "v_exp_f32 v225, v225\n"
      "v_exp_f32 v226, v226\n"
      "v_exp_f32 v227, v227\n"
      "v_exp_f32 v228, v228\n"
      "v_exp_f32 v229, v229\n"
      "v_exp_f32 v230, v230\n"
      "v_exp_f32 v231, v231\n"
      "v_pk_add_f32 v[224:225], v[224:225], 1.0 op_sel_hi:[1,0]\n"
      "v_pk_add_f32 v[226:227], v[226:227], 1.0 op_sel_hi:[1,0]\n"
      "v_pk_add_f32 v[228:229], v[228:229], 1.0 op_sel_hi:[1,0]\n"
      "v_pk_mul_f32 v[56:57], v[56:57], v[40:41]\n"
      "v_pk_add_f32 v[230:231], v[230:231], 1.0 op_sel_hi:[1,0]\n"
      "v_rcp_f32 v224, v224\n"
      "v_rcp_f32 v225, v225\n"
      "v_rcp_f32 v226, v226\n"
      "v_rcp_f32 v227, v227\n"
      "v_rcp_f32 v228, v228\n"
      "v_rcp_f32 v229, v229\n"
      "v_rcp_f32 v230, v230\n"
      "v_rcp_f32 v231, v231\n"
      "v_pk_mul_f32 v[58:59], v[58:59], v[42:43]\n"
      "v_pk_mul_f32 v[60:61], v[60:61], v[44:45]\n"
      "v_pk_mul_f32 v[62:63], v[62:63], v[46:47]\n"
      "v_pk_mul_f32 v[56:57], v[56:57], v[224:225]\n"
      "v_pk_mul_f32 v[60:61], v[60:61], v[228:229]\n"
      "v_pk_mul_f32 v[58:59], v[58:59], v[226:227]\n"
      "v_pk_mul_f32 v[62:63], v[62:63], v[230:231]\n"
      "v_cvt_pk_bf16_f32 v40, v56, v57\n"
      "v_cvt_pk_bf16_f32 v42, v60, v61\n"
      "v_cvt_pk_bf16_f32 v41, v58, v59\n"
      "v_cvt_pk_bf16_f32 v43, v62, v63\n"
      "s_add_u32 s56, s65, 89088\n"
      "v_permlane16_swap_b32 v40, v42\n"
      "v_permlane16_swap_b32 v41, v43\n"
      "buffer_store_dwordx4 v[40:43], v209, s[24:27], s56 offen sc1\n"
      "v_pk_mul_f32 v[216:217], v[64:65], v[64:65]\n"
      "v_pk_mul_f32 v[218:219], v[66:67], v[66:67]\n"
      "v_pk_mul_f32 v[220:221], v[68:69], v[68:69]\n"
      "v_pk_mul_f32 v[222:223], v[70:71], v[70:71]\n"
      "v_pk_fma_f32 v[216:217], v[216:217], s[70:71], v[192:193] op_sel_hi:[1,0,0]\n"
      "v_pk_fma_f32 v[218:219], v[218:219], s[70:71], v[192:193] op_sel_hi:[1,0,0]\n"
      "v_pk_fma_f32 v[220:221], v[220:221], s[70:71], v[192:193] op_sel_hi:[1,0,0]\n"
      "v_pk_fma_f32 v[222:223], v[222:223], s[70:71], v[192:193] op_sel_hi:[1,0,0]\n"
      "v_pk_mul_f32 v[216:217], v[216:217], v[64:65]\n"
      "v_pk_mul_f32 v[218:219], v[218:219], v[66:67]\n"
      "v_pk_mul_f32 v[220:221], v[220:221], v[68:69]\n"
      "v_pk_mul_f32 v[222:223], v[222:223], v[70:71]\n"
      "v_exp_f32 v216, v216\n"
      "v_exp_f32 v217, v217\n"
      "v_exp_f32 v218, v218\n"
      "v_exp_f32 v219, v219\n"
      "v_exp_f32 v220, v220\n"
      "v_exp_f32 v221, v221\n"
      "v_exp_f32 v222, v222\n"
      "v_exp_f32 v223, v223\n"
      "v_pk_add_f32 v[216:217], v[216:217], 1.0 op_sel_hi:[1,0]\n"
      "v_pk_add_f32 v[218:219], v[218:219], 1.0 op_sel_hi:[1,0]\n"
      "v_pk_add_f32 v[220:221], v[220:221], 1.0 op_sel_hi:[1,0]\n"
      "v_pk_mul_f32 v[80:81], v[80:81], v[64:65]\n"
      "v_pk_add_f32 v[222:223], v[222:223], 1.0 op_sel_hi:[1,0]\n"
      "v_rcp_f32 v216, v216\n"
      "v_rcp_f32 v217, v217\n"
      "v_rcp_f32 v218, v218\n"
      "v_rcp_f32 v219, v219\n"
      "v_rcp_f32 v220, v220\n"
      "v_rcp_f32 v221, v221\n"
      "v_rcp_f32 v222, v222\n"
      "v_rcp_f32 v223, v223\n"
      "v_pk_mul_f32 v[82:83], v[82:83], v[66:67]\n"
      "v_pk_mul_f32 v[84:85], v[84:85], v[68:69]\n"
      "v_pk_mul_f32 v[86:87], v[86:87], v[70:71]\n"
      "v_pk_mul_f32 v[80:81], v[80:81], v[216:217]\n"
      "v_pk_mul_f32 v[84:85], v[84:85], v[220:221]\n"
      "v_pk_mul_f32 v[82:83], v[82:83], v[218:219]\n"
      "v_pk_mul_f32 v[86:87], v[86:87], v[222:223]\n"
      "v_cvt_pk_bf16_f32 v64, v80, v81\n"
      "v_cvt_pk_bf16_f32 v66, v84, v85\n"
      "v_cvt_pk_bf16_f32 v65, v82, v83\n"
      "v_cvt_pk_bf16_f32 v67, v86, v87\n"
      "s_add_u32 s56, s65, 2048\n"
      "v_permlane16_swap_b32 v64, v66\n"
      "v_permlane16_swap_b32 v65, v67\n"
      "buffer_store_dwordx4 v[64:67], v209, s[24:27], s56 offen sc1\n"
      "ds_read_b32 v253, v251 offset:128\n"
      "v_pk_mul_f32 v[224:225], v[72:73], v[72:73]\n"
      "v_pk_mul_f32 v[226:227], v[74:75], v[74:75]\n"
      "v_pk_mul_f32 v[228:229], v[76:77], v[76:77]\n"
      "v_pk_mul_f32 v[230:231], v[78:79], v[78:79]\n"
      "v_pk_fma_f32 v[224:225], v[224:225], s[70:71], v[192:193] op_sel_hi:[1,0,0]\n"
      "v_pk_fma_f32 v[226:227], v[226:227], s[70:71], v[192:193] op_sel_hi:[1,0,0]\n"
      "v_pk_fma_f32 v[228:229], v[228:229], s[70:71], v[192:193] op_sel_hi:[1,0,0]\n"
      "v_pk_fma_f32 v[230:231], v[230:231], s[70:71], v[192:193] op_sel_hi:[1,0,0]\n"
      "v_pk_mul_f32 v[224:225], v[224:225], v[72:73]\n"
      "v_pk_mul_f32 v[226:227], v[226:227], v[74:75]\n"
      "v_pk_mul_f32 v[228:229], v[228:229], v[76:77]\n"
      "v_pk_mul_f32 v[230:231], v[230:231], v[78:79]\n"
      "v_exp_f32 v224, v224\n"
      "v_exp_f32 v225, v225\n"
      "v_exp_f32 v226, v226\n"
      "v_exp_f32 v227, v227\n"
      "v_exp_f32 v228, v228\n"
      "v_exp_f32 v229, v229\n"
      "v_exp_f32 v230, v230\n"
      "v_exp_f32 v231, v231\n"
      "v_pk_add_f32 v[224:225], v[224:225], 1.0 op_sel_hi:[1,0]\n"
      "v_pk_add_f32 v[226:227], v[226:227], 1.0 op_sel_hi:[1,0]\n"
      "v_pk_add_f32 v[228:229], v[228:229], 1.0 op_sel_hi:[1,0]\n"
      "v_pk_mul_f32 v[88:89], v[88:89], v[72:73]\n"
      "v_pk_add_f32 v[230:231], v[230:231], 1.0 op_sel_hi:[1,0]\n"
      "v_rcp_f32 v224, v224\n"
      "v_rcp_f32 v225, v225\n"
      "v_rcp_f32 v226, v226\n"
      "v_rcp_f32 v227, v227\n"
      "v_rcp_f32 v228, v228\n"
      "v_rcp_f32 v229, v229\n"
      "v_rcp_f32 v230, v230\n"
      "v_rcp_f32 v231, v231\n"
      "v_pk_mul_f32 v[90:91], v[90:91], v[74:75]\n"
      "v_pk_mul_f32 v[92:93], v[92:93], v[76:77]\n"
      "v_pk_mul_f32 v[94:95], v[94:95], v[78:79]\n"
      "v_pk_mul_f32 v[88:89], v[88:89], v[224:225]\n"
      "v_pk_mul_f32 v[92:93], v[92:93], v[228:229]\n"
      "v_pk_mul_f32 v[90:91], v[90:91], v[226:227]\n"
      "v_pk_mul_f32 v[94:95], v[94:95], v[230:231]\n"
      "v_cvt_pk_bf16_f32 v72, v88, v89\n"
      "v_cvt_pk_bf16_f32 v74, v92, v93\n"
      "v_cvt_pk_bf16_f32 v73, v90, v91\n"
      "v_cvt_pk_bf16_f32 v75, v94, v95\n"
      "s_add_u32 s56, s65, 90112\n"
      "v_permlane16_swap_b32 v72, v74\n"
      "v_permlane16_swap_b32 v73, v75\n"
      "buffer_store_dwordx4 v[72:75], v209, s[24:27], s56 offen sc1\n"
      "Le45_%=:\n"
      "s_waitcnt lgkmcnt(0)\n"
      "v_readfirstlane_b32 s67, v253\n"
      "s_cmp_lg_u32 s67, 0\n"
      "s_cbranch_scc1 Lw47_%=\n"
      "Lr48_%=:\n"
      "ds_read_b64_tr_b16 v[96:97], v194 offset:0\n"
      "ds_read_b64_tr_b16 v[98:99], v194 offset:512\n"
      "ds_read_b64_tr_b16 v[100:101], v195 offset:0\n"
      "ds_read_b64_tr_b16 v[102:103], v195 offset:512\n"
      "ds_read_b64_tr_b16 v[104:105], v196 offset:0\n"
      "ds_read_b64_tr_b16 v[106:107], v196 offset:512\n"
      "ds_read_b64_tr_b16 v[108:109], v197 offset:0\n"
      "ds_read_b64_tr_b16 v[110:111], v197 offset:512\n"
      "ds_read_b64_tr_b16 v[112:113], v194 offset:8192\n"
      "ds_read_b64_tr_b16 v[114:115], v194 offset:8704\n"
      "ds_read_b64_tr_b16 v[116:117], v195 offset:8192\n"
      "ds_read_b64_tr_b16 v[118:119], v195 offset:8704\n"
      "ds_read_b64_tr_b16 v[120:121], v196 offset:8192\n"
      "ds_read_b64_tr_b16 v[122:123], v196 offset:8704\n"
      "ds_read_b64_tr_b16 v[124:125], v197 offset:8192\n"
      "ds_read_b64_tr_b16 v[126:127], v197 offset:8704\n"
      "ds_read_b64_tr_b16 v[128:129], v194 offset:16384\n"
      "ds_read_b64_tr_b16 v[130:131], v194 offset:16896\n"
      "ds_read_b64_tr_b16 v[132:133], v195 offset:16384\n"
      "ds_read_b64_tr_b16 v[134:135], v195 offset:16896\n"
      "ds_read_b64_tr_b16 v[136:137], v196 offset:16384\n"
      "ds_read_b64_tr_b16 v[138:139], v196 offset:16896\n"
      "ds_read_b64_tr_b16 v[140:141], v197 offset:16384\n"
      "ds_read_b64_tr_b16 v[142:143], v197 offset:16896\n"
      "ds_read_b64_tr_b16 v[144:145], v194 offset:24576\n"
      "ds_read_b64_tr_b16 v[146:147], v194 offset:25088\n"
      "ds_read_b64_tr_b16 v[148:149], v195 offset:24576\n"
      "ds_read_b64_tr_b16 v[150:151], v195 offset:25088\n"
      "ds_read_b64_tr_b16 v[152:153], v196 offset:24576\n"
      "ds_read_b64_tr_b16 v[154:155], v196 offset:25088\n"
      "ds_read_b64_tr_b16 v[156:157], v197 offset:24576\n"
      "ds_read_b64_tr_b16 v[158:159], v197 offset:25088\n"
      "ds_read_b64_tr_b16 v[160:161], v194 offset:32768\n"
      "ds_read_b64_tr_b16 v[162:163], v194 offset:33280\n"
      "ds_read_b64_tr_b16 v[164:165], v195 offset:32768\n"
      "ds_read_b64_tr_b16 v[166:167], v195 offset:33280\n"
      "ds_read_b64_tr_b16 v[168:169], v196 offset:32768\n"
      "ds_read_b64_tr_b16 v[170:171], v196 offset:33280\n"
      "ds_read_b64_tr_b16 v[172:173], v197 offset:32768\n"
      "ds_read_b64_tr_b16 v[174:175], v197 offset:33280\n"
      "v_add_u32 v194, -10688, v194\n"
      "v_add_u32 v195, -10688, v195\n"
      "v_add_u32 v196, -10688, v196\n"
      "v_add_u32 v197, -10688, v197\n"
      "ds_read_b64_tr_b16 v[176:177], v194 offset:0\n"
      "ds_read_b64_tr_b16 v[178:179], v194 offset:512\n"
      "ds_read_b64_tr_b16 v[180:181], v195 offset:0\n"
      "ds_read_b64_tr_b16 v[182:183], v195 offset:512\n"
      "ds_read_b64_tr_b16 v[184:185], v196 offset:0\n"
      "ds_read_b64_tr_b16 v[186:187], v196 offset:512\n"
      "ds_read_b64_tr_b16 v[188:189], v197 offset:0\n"
      "ds_read_b64_tr_b16 v[190:191], v197 offset:512\n"
      "s_waitcnt vmcnt(0) lgkmcnt(0)\n"
      "s_barrier\n"
      "s_cmp_lg_u32 s44, 0\n"
      "s_cbranch_scc1 Ln50_%=\n"
      "s_mov_b64 exec, 1\n"
      "buffer_store_dwordx2 v[246:247], off, s[24:27], s64 offset:8 sc1\n"
      "s_mov_b64 exec, -1\n"
      "Ln50_%=:\n"
      "s_lshl_b32 s57, s44, 10\n"
      "v_mov_b32 v238, 2.0\n"
      "s_add_u32 m0, s57, 147456\n"
      "s_mov_b32 s62, 65536\n"
      "buffer_load_dwordx4 v249, s[20:23], s62 offen offset:0 lds\n"
      "s_add_u32 m0, s57, 151552\n"
      "s_mov_b32 s62, 163840\n"
      "buffer_load_dwordx4 v249, s[20:23], s62 offen offset:0 lds\n"
      "s_add_u32 m0, s57, 155648\n"
      "s_mov_b32 s62, 262144\n"
      "buffer_load_dwordx4 v249, s[20:23], s62 offen offset:0 lds\n"
      "s_add_u32 m0, s57, 159744\n"
      "s_mov_b32 s62, 360448\n"
      "buffer_load_dwordx4 v249, s[20:23], s62 offen offset:0 lds\n"
      "s_add_u32 m0, s57, 40960\n"
      "s_mov_b32 s62, 458752\n"
      "buffer_load_dwordx4 v249, s[20:23], s62 offen offset:0 lds\n"
      "s_add_u32 m0, s57, 45056\n"
      "s_mov_b32 s62, 557056\n"
      "buffer_load_dwordx4 v249, s[20:23], s62 offen offset:0 lds\n"
      "s_add_u32 m0, s57, 49152\n"
      "s_mov_b32 s62, 655360\n"
      "buffer_load_dwordx4 v249, s[20:23], s62 offen offset:0 lds\n"
      "s_add_u32 m0, s57, 53248\n"
      "s_mov_b32 s62, 753664\n"
      "buffer_load_dwordx4 v249, s[20:23], s62 offen offset:0 lds\n"
      "s_add_u32 m0, s57, 57344\n"
      "s_mov_b32 s62, 851968\n"
      "buffer_load_dwordx4 v249, s[20:23], s62 offen offset:0 lds\n"
      "s_add_u32 m0, s57, 61440\n"
      "s_mov_b32 s62, 950272\n"
      "buffer_load_dwordx4 v249, s[20:23], s62 offen offset:0 lds\n"
      "s_add_u32 m0, s57, 65536\n"
      "s_mov_b32 s62, 1048576\n"
      "buffer_load_dwordx4 v249, s[20:23], s62 offen offset:0 lds\n"
      "global_load_dwordx2 v[232:233], v248, s[24:25] offset:8 sc1\n"
      "s_add_u32 m0, s57, 69632\n"
      "s_mov_b32 s62, 1146880\n"
      "buffer_load_dwordx4 v249, s[20:23], s62 offen offset:0 lds\n"
      "s_add_u32 m0, s57, 73728\n"
      "s_mov_b32 s62, 1245184\n"
      "buffer_load_dwordx4 v249, s[20:23], s62 offen offset:0 lds\n"
      "s_add_u32 m0, s57, 77824\n"
      "s_mov_b32 s62, 1343488\n"
      "buffer_load_dwordx4 v249, s[20:23], s62 offen offset:0 lds\n"
      "s_add_u32 m0, s57, 30272\n"
      "s_mov_b32 s62, 1376256\n"
      "buffer_load_dwordx4 v249, s[20:23], s62 offen offset:0 lds\n"
      "s_lshl2_add_u32 m0, s44, 144\n"
      "s_mov_b64 exec, 1\n"
      "buffer_load_dword v238, s[20:23], 0 offen lds\n"
      "s_mov_b64 exec, -1\n"
      "s_setprio 0\n"
      "s_waitcnt vmcnt(5)\n"
      "v_cmp_ne_u64 vcc, s[54:55], v[232:233]\n"
      "s_cbranch_vccz Lo51_%=\n"
      "s_mov_b32 s68, 65536\n"
      "Lp52_%=:\n"
      "global_load_dwordx2 v[240:241], v248, s[24:25] offset:8 sc1\n"
      "s_sub_u32 s68, s68, 1\n"
      "s_waitcnt vmcnt(0)\n"
      "s_cbranch_scc1 Lq53_%=\n"
      "v_cmp_ne_u64 vcc, s[54:55], v[240:241]\n"
      "s_cbranch_vccz Lq53_%=\n"
      "s_sleep 1\n"
      "s_branch Lp52_%=\n"
      "Lq53_%=:\n"
      "Lo51_%=:\n"
      ".p2align 3\n"
      "buffer_load_dword v242, v208, s[24:27], s60 offen sc1\n"
      "s_mov_b32 s62, 4456448\n"
      "buffer_load_dwordx4 v[32:35], v199, s[24:27], s62 offen sc1\n"
      "s_mov_b32 s62, 4462592\n"
      "buffer_load_dwordx4 v[36:39], v199, s[24:27], s62 offen sc1\n"
      "s_mov_b32 s62, 4468736\n"
      "buffer_load_dwordx4 v[40:43], v199, s[24:27], s62 offen sc1\n"
      "s_mov_b32 s62, 4456448\n"
      "buffer_load_dwordx4 v[44:47], v198, s[24:27], s62 offen sc1\n"
      "s_mov_b32 s62, 4474880\n"
      "buffer_load_dwordx4 v[48:51], v199, s[24:27], s62 offen sc1\n"
      "s_mov_b32 s62, 4462592\n"
      "buffer_load_dwordx4 v[52:55], v198, s[24:27], s62 offen sc1\n"
      "s_mov_b32 s62, 4481024\n"
      "buffer_load_dwordx4 v[56:59], v199, s[24:27], s62 offen sc1\n"
      "s_mov_b32 s62, 4468736\n"
      "buffer_load_dwordx4 v[60:63], v198, s[24:27], s62 offen sc1\n"
      "s_mov_b32 s62, 4487168\n"
      "buffer_load_dwordx4 v[64:67], v199, s[24:27], s62 offen sc1\n"
      "s_mov_b32 s62, 4474880\n"
      "buffer_load_dwordx4 v[68:71], v198, s[24:27], s62 offen sc1\n"
      "s_mov_b32 s62, 4493312\n"
      "buffer_load_dwordx4 v[72:75], v199, s[24:27], s62 offen sc1\n"
      "s_mov_b32 s62, 4481024\n"
      "buffer_load_dwordx4 v[76:79], v198, s[24:27], s62 offen sc1\n"
      "ds_read_b32 v253, v251 offset:132\n"
      "s_waitcnt vmcnt(11)\n"
      "v_mfma_f32_16x16x32_bf16 v[16:19], v[96:99], v[32:35], 0\n"
      "v_mfma_f32_16x16x32_bf16 v[20:23], v[100:103], v[32:35], 0\n"
      "v_mfma_f32_16x16x32_bf16 v[24:27], v[104:107], v[32:35], 0\n"
      "v_mfma_f32_16x16x32_bf16 v[28:31], v[108:111], v[32:35], 0\n"
      "s_mov_b32 s62, 4457472\n"
      "buffer_load_dwordx4 v[32:35], v199, s[24:27], s62 offen sc1\n"
      "s_waitcnt vmcnt(11)\n"
      "v_mfma_f32_16x16x32_bf16 v[16:19], v[112:115], v[36:39], v[16:19]\n"
      "v_mfma_f32_16x16x32_bf16 v[20:23], v[116:119], v[36:39], v[20:23]\n"
      "v_mfma_f32_16x16x32_bf16 v[24:27], v[120:123], v[36:39], v[24:27]\n"
      "v_mfma_f32_16x16x32_bf16 v[28:31], v[124:127], v[36:39], v[28:31]\n"
      "s_mov_b32 s62, 4487168\n"
      "buffer_load_dwordx4 v[36:39], v198, s[24:27], s62 offen sc1\n"
      "s_waitcnt vmcnt(11)\n"
      "v_mfma_f32_16x16x32_bf16 v[16:19], v[128:131], v[40:43], v[16:19]\n"
      "v_mfma_f32_16x16x32_bf16 v[20:23], v[132:135], v[40:43], v[20:23]\n"
      "v_mfma_f32_16x16x32_bf16 v[24:27], v[136:139], v[40:43], v[24:27]\n"
      "v_mfma_f32_16x16x32_bf16 v[28:31], v[140:143], v[40:43], v[28:31]\n"
      "s_mov_b32 s62, 4463616\n"
      "buffer_load_dwordx4 v[40:43], v199, s[24:27], s62 offen sc1\n"
      "s_waitcnt vmcnt(11)\n"
      "v_mfma_f32_16x16x32_bf16 v[12:15], v[108:111], v[44:47], 0\n"
      "v_mfma_f32_16x16x32_bf16 v[8:11], v[104:107], v[44:47], 0\n"
      "v_mfma_f32_16x16x32_bf16 v[4:7], v[100:103], v[44:47], 0\n"
      "v_mfma_f32_16x16x32_bf16 v[0:3], v[96:99], v[44:47], 0\n"
      "s_mov_b32 s62, 4493312\n"
      "buffer_load_dwordx4 v[44:47], v198, s[24:27], s62 offen sc1\n"
      "s_waitcnt vmcnt(11)\n"
      "v_mfma_f32_16x16x32_bf16 v[16:19], v[144:147], v[48:51], v[16:19]\n"
      "v_mfma_f32_16x16x32_bf16 v[20:23], v[148:151], v[48:51], v[20:23]\n"
      "v_mfma_f32_16x16x32_bf16 v[24:27], v[152:155], v[48:51], v[24:27]\n"
      "v_mfma_f32_16x16x32_bf16 v[28:31], v[156:159], v[48:51], v[28:31]\n"
      "s_mov_b32 s62, 4469760\n"
      "buffer_load_dwordx4 v[48:51], v199, s[24:27], s62 offen sc1\n"
      "s_waitcnt vmcnt(11)\n"
      "v_mfma_f32_16x16x32_bf16 v[12:15], v[124:127], v[52:55], v[12:15]\n"
      "v_mfma_f32_16x16x32_bf16 v[8:11], v[120:123], v[52:55], v[8:11]\n"
      "v_mfma_f32_16x16x32_bf16 v[4:7], v[116:119], v[52:55], v[4:7]\n"
      "v_mfma_f32_16x16x32_bf16 v[0:3], v[112:115], v[52:55], v[0:3]\n"
      "s_mov_b32 s62, 4457472\n"
      "buffer_load_dwordx4 v[52:55], v198, s[24:27], s62 offen sc1\n"
      "s_waitcnt lgkmcnt(0)\n"
      "v_readfirstlane_b32 s67, v253\n"
      "s_cmp_lg_u32 s67, 0\n"
      "s_cbranch_scc1 Lw54_%=\n"
      "Lr55_%=:\n"
      "s_waitcnt vmcnt(11)\n"
      "v_mfma_f32_16x16x32_bf16 v[16:19], v[160:163], v[56:59], v[16:19]\n"
      "ds_read_b64_tr_b16 v[96:97], v194 offset:51648\n"
      "ds_read_b64_tr_b16 v[98:99], v194 offset:52160\n"
      "v_mfma_f32_16x16x32_bf16 v[20:23], v[164:167], v[56:59], v[20:23]\n"
      "ds_read_b64_tr_b16 v[100:101], v195 offset:51648\n"
      "ds_read_b64_tr_b16 v[102:103], v195 offset:52160\n"
      "v_mfma_f32_16x16x32_bf16 v[24:27], v[168:171], v[56:59], v[24:27]\n"
      "v_mfma_f32_16x16x32_bf16 v[28:31], v[172:175], v[56:59], v[28:31]\n"
      "s_mov_b32 s62, 4475904\n"
      "buffer_load_dwordx4 v[56:59], v199, s[24:27], s62 offen sc1\n"
      "s_waitcnt vmcnt(11)\n"
      "v_mfma_f32_16x16x32_bf16 v[12:15], v[140:143], v[60:63], v[12:15]\n"
      "ds_read_b64_tr_b16 v[104:105], v196 offset:51648\n"
      "ds_read_b64_tr_b16 v[106:107], v196 offset:52160\n"
      "v_mfma_f32_16x16x32_bf16 v[8:11], v[136:139], v[60:63], v[8:11]\n"
      "ds_read_b64_tr_b16 v[108:109], v197 offset:51648\n"
      "ds_read_b64_tr_b16 v[110:111], v197 offset:52160\n"
      "v_mfma_f32_16x16x32_bf16 v[4:7], v[132:135], v[60:63], v[4:7]\n"
      "v_mfma_f32_16x16x32_bf16 v[0:3], v[128:131], v[60:63], v[0:3]\n"
      "s_mov_b32 s62, 4463616\n"
      "buffer_load_dwordx4 v[60:63], v198, s[24:27], s62 offen sc1\n"
      "s_waitcnt vmcnt(11)\n"
      "v_mfma_f32_16x16x32_bf16 v[16:19], v[176:179], v[64:67], v[16:19]\n"
      "ds_read_b64_tr_b16 v[112:113], v194 offset:59840\n"
      "ds_read_b64_tr_b16 v[114:115], v194 offset:60352\n"
      "v_mfma_f32_16x16x32_bf16 v[20:23], v[180:183], v[64:67], v[20:23]\n"
      "ds_read_b64_tr_b16 v[116:117], v195 offset:59840\n"
      "ds_read_b64_tr_b16 v[118:119], v195 offset:60352\n"
      "v_mfma_f32_16x16x32_bf16 v[24:27], v[184:187], v[64:67], v[24:27]\n"
      "v_mfma_f32_16x16x32_bf16 v[28:31], v[188:191], v[64:67], v[28:31]\n"
      "s_mov_b32 s62, 4482048\n"
      "buffer_load_dwordx4 v[64:67], v199, s[24:27], s62 offen sc1\n"
      "s_waitcnt vmcnt(11)\n"
      "v_mfma_f32_16x16x32_bf16 v[12:15], v[156:159], v[68:71], v[12:15]\n"
      "ds_read_b64_tr_b16 v[120:121], v196 offset:59840\n"
      "ds_read_b64_tr_b16 v[122:123], v196 offset:60352\n"
      "v_mfma_f32_16x16x32_bf16 v[8:11], v[152:155], v[68:71], v[8:11]\n"
      "ds_read_b64_tr_b16 v[124:125], v197 offset:59840\n"
      "ds_read_b64_tr_b16 v[126:127], v197 offset:60352\n"
      "v_mfma_f32_16x16x32_bf16 v[4:7], v[148:151], v[68:71], v[4:7]\n"
      "v_mfma_f32_16x16x32_bf16 v[0:3], v[144:147], v[68:71], v[0:3]\n"
      "s_mov_b32 s62, 4469760\n"
      "buffer_load_dwordx4 v[68:71], v198, s[24:27], s62 offen sc1\n"
      "s_waitcnt vmcnt(11) lgkmcnt(14)\n"
      "v_mfma_f32_16x16x32_bf16 v[16:19], v[96:99], v[72:75], v[16:19]\n"
      "v_add_u32 v194, 68032, v194\n"
      "v_add_u32 v195, 68032, v195\n"
      "s_waitcnt lgkmcnt(12)\n"
      "v_mfma_f32_16x16x32_bf16 v[20:23], v[100:103], v[72:75], v[20:23]\n"
      "v_add_u32 v196, 68032, v196\n"
      "v_add_u32 v197, 68032, v197\n"
      "s_waitcnt lgkmcnt(10)\n"
      "v_mfma_f32_16x16x32_bf16 v[24:27], v[104:107], v[72:75], v[24:27]\n"
      "ds_read_b64_tr_b16 v[128:129], v194 offset:0\n"
      "ds_read_b64_tr_b16 v[130:131], v194 offset:512\n"
      "s_waitcnt lgkmcnt(10)\n"
      "v_mfma_f32_16x16x32_bf16 v[28:31], v[108:111], v[72:75], v[28:31]\n"
      "s_mov_b32 s62, 4488192\n"
      "buffer_load_dwordx4 v[72:75], v199, s[24:27], s62 offen sc1\n"
      "s_waitcnt vmcnt(11)\n"
      "v_mfma_f32_16x16x32_bf16 v[12:15], v[172:175], v[76:79], v[12:15]\n"
      "ds_read_b64_tr_b16 v[132:133], v195 offset:0\n"
      "ds_read_b64_tr_b16 v[134:135], v195 offset:512\n"
      "v_mfma_f32_16x16x32_bf16 v[8:11], v[168:171], v[76:79], v[8:11]\n"
      "ds_read_b64_tr_b16 v[136:137], v196 offset:0\n"
      "ds_read_b64_tr_b16 v[138:139], v196 offset:512\n"
      "v_mfma_f32_16x16x32_bf16 v[4:7], v[164:167], v[76:79], v[4:7]\n"
      "ds_read_b64_tr_b16 v[140:141], v197 offset:0\n"
      "ds_read_b64_tr_b16 v[142:143], v197 offset:512\n"
      "v_mfma_f32_16x16x32_bf16 v[0:3], v[160:163], v[76:79], v[0:3]\n"
      "s_mov_b32 s62, 4475904\n"
      "buffer_load_dwordx4 v[76:79], v198, s[24:27], s62 offen sc1\n"
      "s_waitcnt vmcnt(11) lgkmcnt(14)\n"
      "v_mfma_f32_16x16x32_bf16 v[16:19], v[112:115], v[32:35], v[16:19]\n"
      "ds_read_b64_tr_b16 v[144:145], v194 offset:8192\n"
      "ds_read_b64_tr_b16 v[146:147], v194 offset:8704\n"
      "s_waitcnt lgkmcnt(14)\n"
      "v_mfma_f32_16x16x32_bf16 v[20:23], v[116:119], v[32:35], v[20:23]\n"
      "ds_read_b64_tr_b16 v[148:149], v195 offset:8192\n"
      "ds_read_b64_tr_b16 v[150:151], v195 offset:8704\n"
      "s_waitcnt lgkmcnt(14)\n"
      "v_mfma_f32_16x16x32_bf16 v[24:27], v[120:123], v[32:35], v[24:27]\n"
      "s_waitcnt lgkmcnt(12)\n"
      "v_mfma_f32_16x16x32_bf16 v[28:31], v[124:127], v[32:35], v[28:31]\n"
      "s_mov_b32 s62, 4494336\n"
      "buffer_load_dwordx4 v[32:35], v199, s[24:27], s62 offen sc1\n"
      "s_waitcnt vmcnt(11)\n"
      "v_mfma_f32_16x16x32_bf16 v[12:15], v[188:191], v[36:39], v[12:15]\n"
      "ds_read_b64_tr_b16 v[152:153], v196 offset:8192\n"
      "ds_read_b64_tr_b16 v[154:155], v196 offset:8704\n"
      "v_mfma_f32_16x16x32_bf16 v[8:11], v[184:187], v[36:39], v[8:11]\n"
      "ds_read_b64_tr_b16 v[156:157], v197 offset:8192\n"
      "ds_read_b64_tr_b16 v[158:159], v197 offset:8704\n"
      "v_mfma_f32_16x16x32_bf16 v[4:7], v[180:183], v[36:39], v[4:7]\n"
      "v_mfma_f32_16x16x32_bf16 v[0:3], v[176:179], v[36:39], v[0:3]\n"
      "s_mov_b32 s62, 4482048\n"
      "buffer_load_dwordx4 v[36:39], v198, s[24:27], s62 offen sc1\n"
      "s_waitcnt vmcnt(11) lgkmcnt(14)\n"
      "v_mfma_f32_16x16x32_bf16 v[16:19], v[128:131], v[40:43], v[16:19]\n"
      "ds_read_b64_tr_b16 v[160:161], v194 offset:16384\n"
      "ds_read_b64_tr_b16 v[162:163], v194 offset:16896\n"
      "s_waitcnt lgkmcnt(14)\n"
      "v_mfma_f32_16x16x32_bf16 v[20:23], v[132:135], v[40:43], v[20:23]\n"
      "ds_read_b64_tr_b16 v[164:165], v195 offset:16384\n"
      "ds_read_b64_tr_b16 v[166:167], v195 offset:16896\n"
      "s_waitcnt lgkmcnt(14)\n"
      "v_mfma_f32_16x16x32_bf16 v[24:27], v[136:139], v[40:43], v[24:27]\n"
      "s_waitcnt lgkmcnt(12)\n"
      "v_mfma_f32_16x16x32_bf16 v[28:31], v[140:143], v[40:43], v[28:31]\n"
      "s_mov_b32 s62, 4458496\n"
      "buffer_load_dwordx4 v[40:43], v199, s[24:27], s62 offen sc1\n"
      "s_waitcnt vmcnt(11)\n"
      "v_mfma_f32_16x16x32_bf16 v[12:15], v[108:111], v[44:47], v[12:15]\n"
      "ds_read_b64_tr_b16 v[168:169], v196 offset:16384\n"
      "ds_read_b64_tr_b16 v[170:171], v196 offset:16896\n"
      "v_mfma_f32_16x16x32_bf16 v[8:11], v[104:107], v[44:47], v[8:11]\n"
      "ds_read_b64_tr_b16 v[172:173], v197 offset:16384\n"
      "ds_read_b64_tr_b16 v[174:175], v197 offset:16896\n"
      "v_mfma_f32_16x16x32_bf16 v[4:7], v[100:103], v[44:47], v[4:7]\n"
      "v_mfma_f32_16x16x32_bf16 v[0:3], v[96:99], v[44:47], v[0:3]\n"
      "s_mov_b32 s62, 4488192\n"
      "buffer_load_dwordx4 v[44:47], v198, s[24:27], s62 offen sc1\n"
      "s_waitcnt vmcnt(11) lgkmcnt(14)\n"
      "v_mfma_f32_16x16x32_bf16 v[16:19], v[144:147], v[48:51], v[16:19]\n"
      "ds_read_b64_tr_b16 v[176:177], v194 offset:24576\n"
      "ds_read_b64_tr_b16 v[178:179], v194 offset:25088\n"
      "s_waitcnt lgkmcnt(14)\n"
      "v_mfma_f32_16x16x32_bf16 v[20:23], v[148:151], v[48:51], v[20:23]\n"
      "ds_read_b64_tr_b16 v[180:181], v195 offset:24576\n"
      "ds_read_b64_tr_b16 v[182:183], v195 offset:25088\n"
      "s_waitcnt lgkmcnt(14)\n"
      "v_mfma_f32_16x16x32_bf16 v[24:27], v[152:155], v[48:51], v[24:27]\n"
      "s_waitcnt lgkmcnt(12)\n"
      "v_mfma_f32_16x16x32_bf16 v[28:31], v[156:159], v[48:51], v[28:31]\n"
      "s_mov_b32 s62, 4464640\n"
      "buffer_load_dwordx4 v[48:51], v199, s[24:27], s62 offen sc1\n"
      "s_waitcnt vmcnt(11)\n"
      "v_mfma_f32_16x16x32_bf16 v[12:15], v[124:127], v[52:55], v[12:15]\n"
      "ds_read_b64_tr_b16 v[184:185], v196 offset:24576\n"
      "ds_read_b64_tr_b16 v[186:187], v196 offset:25088\n"
      "v_mfma_f32_16x16x32_bf16 v[8:11], v[120:123], v[52:55], v[8:11]\n"
      "ds_read_b64_tr_b16 v[188:189], v197 offset:24576\n"
      "ds_read_b64_tr_b16 v[190:191], v197 offset:25088\n"
      "v_mfma_f32_16x16x32_bf16 v[4:7], v[116:119], v[52:55], v[4:7]\n"
      "v_mfma_f32_16x16x32_bf16 v[0:3], v[112:115], v[52:55], v[0:3]\n"
      "s_mov_b32 s62, 4494336\n"
      "buffer_load_dwordx4 v[52:55], v198, s[24:27], s62 offen sc1\n"
      "s_waitcnt vmcnt(11) lgkmcnt(14)\n"
      "v_mfma_f32_16x16x32_bf16 v[16:19], v[160:163], v[56:59], v[16:19]\n"
      "ds_read_b128 v[236:239], v251 offset:144\n"
      "ds_read_b64_tr_b16 v[96:97], v194 offset:32768\n"
      "s_waitcnt lgkmcnt(14)\n"
      "v_mfma_f32_16x16x32_bf16 v[20:23], v[164:167], v[56:59], v[20:23]\n"
      "ds_read_b64_tr_b16 v[98:99], v194 offset:33280\n"
      "ds_read_b64_tr_b16 v[100:101], v195 offset:32768\n"
      "s_waitcnt lgkmcnt(14)\n"
      "v_mfma_f32_16x16x32_bf16 v[24:27], v[168:171], v[56:59], v[24:27]\n"
      "ds_read_b64_tr_b16 v[102:103], v195 offset:33280\n"
      "s_waitcnt lgkmcnt(13)\n"
      "v_mfma_f32_16x16x32_bf16 v[28:31], v[172:175], v[56:59], v[28:31]\n"
      "s_mov_b32 s62, 4470784\n"
      "buffer_load_dwordx4 v[56:59], v199, s[24:27], s62 offen sc1\n"
      "s_waitcnt vmcnt(11)\n"
      "v_mfma_f32_16x16x32_bf16 v[12:15], v[140:143], v[60:63], v[12:15]\n"
      "ds_read_b64_tr_b16 v[104:105], v196 offset:32768\n"
      "ds_read_b64_tr_b16 v[106:107], v196 offset:33280\n"
      "v_mfma_f32_16x16x32_bf16 v[8:11], v[136:139], v[60:63], v[8:11]\n"
      "ds_read_b64_tr_b16 v[108:109], v197 offset:32768\n"
      "ds_read_b64_tr_b16 v[110:111], v197 offset:33280\n"
      "v_mfma_f32_16x16x32_bf16 v[4:7], v[132:135], v[60:63], v[4:7]\n"
      "v_mfma_f32_16x16x32_bf16 v[0:3], v[128:131], v[60:63], v[0:3]\n"
      "s_mov_b32 s62, 4458496\n"
      "buffer_load_dwordx4 v[60:63], v198, s[24:27], s62 offen sc1\n"
      "s_waitcnt vmcnt(11) lgkmcnt(15)\n"
      "v_mfma_f32_16x16x32_bf16 v[16:19], v[176:179], v[64:67], v[16:19]\n"
      "s_waitcnt lgkmcnt(8)\n"
      "v_or3_b32 v236, v236, v237, v238\n"
      "v_or_b32 v236, v236, v239\n"
      "v_mfma_f32_16x16x32_bf16 v[20:23], v[180:183], v[64:67], v[20:23]\n"
      "ds_read_b64_tr_b16 v[112:113], v194 offset:40960\n"
      "ds_read_b64_tr_b16 v[114:115], v194 offset:41472\n"
      "v_mfma_f32_16x16x32_bf16 v[24:27], v[184:187], v[64:67], v[24:27]\n"
      "ds_read_b64_tr_b16 v[116:117], v195 offset:40960\n"
      "v_mfma_f32_16x16x32_bf16 v[28:31], v[188:191], v[64:67], v[28:31]\n"
      "s_mov_b32 s62, 4476928\n"
      "buffer_load_dwordx4 v[64:67], v199, s[24:27], s62 offen sc1\n"
      "s_waitcnt vmcnt(11)\n"
      "v_mfma_f32_16x16x32_bf16 v[12:15], v[156:159], v[68:71], v[12:15]\n"
      "ds_read_b64_tr_b16 v[118:119], v195 offset:41472\n"
      "ds_read_b64_tr_b16 v[120:121], v196 offset:40960\n"
      "v_mfma_f32_16x16x32_bf16 v[8:11], v[152:155], v[68:71], v[8:11]\n"
      "ds_read_b64_tr_b16 v[122:123], v196 offset:41472\n"
      "ds_read_b64_tr_b16 v[124:125], v197 offset:40960\n"
      "v_mfma_f32_16x16x32_bf16 v[4:7], v[148:151], v[68:71], v[4:7]\n"
      "ds_read_b64_tr_b16 v[126:127], v197 offset:41472\n"
      "v_mfma_f32_16x16x32_bf16 v[0:3], v[144:147], v[68:71], v[0:3]\n"
      "s_mov_b32 s62, 4464640\n"
      "buffer_load_dwordx4 v[68:71], v198, s[24:27], s62 offen sc1\n"
      "v_cmp_ne_u32 vcc, 0, v236\n"
      "s_cbranch_vccnz Lw57_%=\n"
      "Lr58_%=:\n"
      "s_waitcnt vmcnt(11) lgkmcnt(14)\n"
      "v_mfma_f32_16x16x32_bf16 v[16:19], v[96:99], v[72:75], v[16:19]\n"
      "ds_read_b64_tr_b16 v[128:129], v194 offset:49152\n"
      "ds_read_b64_tr_b16 v[130:131], v194 offset:49664\n"
      "s_waitcnt lgkmcnt(14)\n"
      "v_mfma_f32_16x16x32_bf16 v[20:23], v[100:103], v[72:75], v[20:23]\n"
      "ds_read_b64_tr_b16 v[132:133], v195 offset:49152\n"
      "ds_read_b64_tr_b16 v[134:135], v195 offset:49664\n"
      "s_waitcnt lgkmcnt(14)\n"
      "v_mfma_f32_16x16x32_bf16 v[24:27], v[104:107], v[72:75], v[24:27]\n"
      "s_waitcnt lgkmcnt(12)\n"
      "v_mfma_f32_16x16x32_bf16 v[28:31], v[108:111], v[72:75], v[28:31]\n"
      "s_mov_b32 s62, 4483072\n"
      "buffer_load_dwordx4 v[72:75], v199, s[24:27], s62 offen sc1\n"
      "s_waitcnt vmcnt(11)\n"
      "v_mfma_f32_16x16x32_bf16 v[12:15], v[172:175], v[76:79], v[12:15]\n"
      "ds_read_b64_tr_b16 v[136:137], v196 offset:49152\n"
      "ds_read_b64_tr_b16 v[138:139], v196 offset:49664\n"
      "v_mfma_f32_16x16x32_bf16 v[8:11], v[168:171], v[76:79], v[8:11]\n"
      "ds_read_b64_tr_b16 v[140:141], v197 offset:49152\n"
      "ds_read_b64_tr_b16 v[142:143], v197 offset:49664\n"
      "v_mfma_f32_16x16x32_bf16 v[4:7], v[164:167], v[76:79], v[4:7]\n"
      "v_mfma_f32_16x16x32_bf16 v[0:3], v[160:163], v[76:79], v[0:3]\n"
      "s_mov_b32 s62, 4470784\n"
      "buffer_load_dwordx4 v[76:79], v198, s[24:27], s62 offen sc1\n"
      "s_waitcnt vmcnt(11) lgkmcnt(14)\n"
      "v_mfma_f32_16x16x32_bf16 v[16:19], v[112:115], v[32:35], v[16:19]\n"
      "s_mov_b64 exec, 1\n"
      "ds_add_u32 v251, v252 offset:36\n"
      "s_mov_b64 exec, -1\n"
      "s_waitcnt lgkmcnt(13)\n"
      "v_mfma_f32_16x16x32_bf16 v[20:23], v[116:119], v[32:35], v[20:23]\n"
      "ds_read_b64_tr_b16 v[144:145], v194 offset:57344\n"
      "ds_read_b64_tr_b16 v[146:147], v194 offset:57856\n"
      "s_waitcnt lgkmcnt(13)\n"
      "v_mfma_f32_16x16x32_bf16 v[24:27], v[120:123], v[32:35], v[24:27]\n"
      "ds_read_b64_tr_b16 v[148:149], v195 offset:57344\n"
      "ds_read_b64_tr_b16 v[150:151], v195 offset:57856\n"
      "s_waitcnt lgkmcnt(13)\n"
      "v_mfma_f32_16x16x32_bf16 v[28:31], v[124:127], v[32:35], v[28:31]\n"
      "s_mov_b32 s62, 4489216\n"
      "buffer_load_dwordx4 v[32:35], v199, s[24:27], s62 offen sc1\n"
      "s_waitcnt vmcnt(11)\n"
      "v_mfma_f32_16x16x32_bf16 v[12:15], v[188:191], v[36:39], v[12:15]\n"
      "ds_read_b64_tr_b16 v[152:153], v196 offset:57344\n"
      "ds_read_b64_tr_b16 v[154:155], v196 offset:57856\n"
      "v_mfma_f32_16x16x32_bf16 v[8:11], v[184:187], v[36:39], v[8:11]\n"
      "ds_read_b64_tr_b16 v[156:157], v197 offset:57344\n"
      "ds_read_b64_tr_b16 v[158:159], v197 offset:57856\n"
      "v_mfma_f32_16x16x32_bf16 v[4:7], v[180:183], v[36:39], v[4:7]\n"
      "v_mfma_f32_16x16x32_bf16 v[0:3], v[176:179], v[36:39], v[0:3]\n"
      "s_mov_b32 s62, 4476928\n"
      "buffer_load_dwordx4 v[36:39], v198, s[24:27], s62 offen sc1\n"
      "s_waitcnt vmcnt(11) lgkmcnt(15)\n"
      "v_mfma_f32_16x16x32_bf16 v[16:19], v[128:131], v[40:43], v[16:19]\n"
      "v_add_u32 v194, -57344, v194\n"
      "v_add_u32 v195, -57344, v195\n"
      "s_waitcnt lgkmcnt(13)\n"
      "v_mfma_f32_16x16x32_bf16 v[20:23], v[132:135], v[40:43], v[20:23]\n"
      "v_add_u32 v196, -57344, v196\n"
      "v_add_u32 v197, -57344, v197\n"
      "s_waitcnt lgkmcnt(11)\n"
      "v_mfma_f32_16x16x32_bf16 v[24:27], v[136:139], v[40:43], v[24:27]\n"
      "ds_read_b64_tr_b16 v[160:161], v194 offset:0\n"
      "ds_read_b64_tr_b16 v[162:163], v194 offset:512\n"
      "s_waitcnt lgkmcnt(11)\n"
      "v_mfma_f32_16x16x32_bf16 v[28:31], v[140:143], v[40:43], v[28:31]\n"
      "s_mov_b32 s62, 4495360\n"
      "buffer_load_dwordx4 v[40:43], v199, s[24:27], s62 offen sc1\n"
      "s_waitcnt vmcnt(11)\n"
      "v_mfma_f32_16x16x32_bf16 v[12:15], v[108:111], v[44:47], v[12:15]\n"
      "ds_read_b64_tr_b16 v[164:165], v195 offset:0\n"
      "ds_read_b64_tr_b16 v[166:167], v195 offset:512\n"
      "v_mfma_f32_16x16x32_bf16 v[8:11], v[104:107], v[44:47], v[8:11]\n"
      "ds_read_b64_tr_b16 v[168:169], v196 offset:0\n"
      "ds_read_b64_tr_b16 v[170:171], v196 offset:512\n"
      "v_mfma_f32_16x16x32_bf16 v[4:7], v[100:103], v[44:47], v[4:7]\n"
      "ds_read_b64_tr_b16 v[172:173], v197 offset:0\n"
      "ds_read_b64_tr_b16 v[174:175], v197 offset:512\n"
      "v_mfma_f32_16x16x32_bf16 v[0:3], v[96:99], v[44:47], v[0:3]\n"
      "s_mov_b32 s62, 4483072\n"
      "buffer_load_dwordx4 v[44:47], v198, s[24:27], s62 offen sc1\n"
      "s_waitcnt vmcnt(11) lgkmcnt(14)\n"
      "v_mfma_f32_16x16x32_bf16 v[16:19], v[144:147], v[48:51], v[16:19]\n"
      "ds_read_b64_tr_b16 v[176:177], v194 offset:8192\n"
      "ds_read_b64_tr_b16 v[178:179], v194 offset:8704\n"
      "s_waitcnt lgkmcnt(14)\n"
      "v_mfma_f32_16x16x32_bf16 v[20:23], v[148:151], v[48:51], v[20:23]\n"
      "ds_read_b64_tr_b16 v[180:181], v195 offset:8192\n"
      "ds_read_b64_tr_b16 v[182:183], v195 offset:8704\n"
      "s_waitcnt lgkmcnt(14)\n"
      "v_mfma_f32_16x16x32_bf16 v[24:27], v[152:155], v[48:51], v[24:27]\n"
      "s_waitcnt lgkmcnt(12)\n"
      "v_mfma_f32_16x16x32_bf16 v[28:31], v[156:159], v[48:51], v[28:31]\n"
      "s_mov_b32 s62, 4489216\n"
      "buffer_load_dwordx4 v[48:51], v198, s[24:27], s62 offen sc1\n"
      "s_waitcnt vmcnt(11)\n"
      "v_mfma_f32_16x16x32_bf16 v[12:15], v[124:127], v[52:55], v[12:15]\n"
      "ds_read_b64_tr_b16 v[184:185], v196 offset:8192\n"
      "ds_read_b64_tr_b16 v[186:187], v196 offset:8704\n"
      "v_mfma_f32_16x16x32_bf16 v[8:11], v[120:123], v[52:55], v[8:11]\n"
      "ds_read_b64_tr_b16 v[188:189], v197 offset:8192\n"
      "ds_read_b64_tr_b16 v[190:191], v197 offset:8704\n"
      "v_mfma_f32_16x16x32_bf16 v[4:7], v[116:119], v[52:55], v[4:7]\n"
      "v_mfma_f32_16x16x32_bf16 v[0:3], v[112:115], v[52:55], v[0:3]\n"
      "s_mov_b32 s62, 4495360\n"
      "buffer_load_dwordx4 v[52:55], v198, s[24:27], s62 offen sc1\n"
      "s_waitcnt vmcnt(11) lgkmcnt(14)\n"
      "v_mfma_f32_16x16x32_bf16 v[16:19], v[160:163], v[56:59], v[16:19]\n"
      "v_sub_f32 v244, 1.0, v242\n"
      "ds_read_b64_tr_b16 v[96:97], v194 offset:16384\n"
      "s_waitcnt lgkmcnt(13)\n"
      "v_mfma_f32_16x16x32_bf16 v[20:23], v[164:167], v[56:59], v[20:23]\n"
      "ds_read_b64_tr_b16 v[98:99], v194 offset:16896\n"
      "ds_read_b64_tr_b16 v[100:101], v195 offset:16384\n"
      "s_waitcnt lgkmcnt(13)\n"
      "v_mfma_f32_16x16x32_bf16 v[24:27], v[168:171], v[56:59], v[24:27]\n"
      "ds_read_b64_tr_b16 v[102:103], v195 offset:16896\n"
      "s_waitcnt lgkmcnt(12)\n"
      "v_mfma_f32_16x16x32_bf16 v[28:31], v[172:175], v[56:59], v[28:31]\n"
      "s_mul_i32 s62, s83, 3072\n"
      "s_sub_u32 s62, 4499456, s62\n"
      "buffer_load_dwordx4 v[56:59], v198, s[24:27], s62 offen sc1\n"
      "s_waitcnt vmcnt(11)\n"
      "v_mfma_f32_16x16x32_bf16 v[12:15], v[140:143], v[60:63], v[12:15]\n"
      "ds_read_b64_tr_b16 v[104:105], v196 offset:16384\n"
      "ds_read_b64_tr_b16 v[106:107], v196 offset:16896\n"
      "v_mfma_f32_16x16x32_bf16 v[8:11], v[136:139], v[60:63], v[8:11]\n"
      "ds_read_b64_tr_b16 v[108:109], v197 offset:16384\n"
      "ds_read_b64_tr_b16 v[110:111], v197 offset:16896\n"
      "v_mfma_f32_16x16x32_bf16 v[4:7], v[132:135], v[60:63], v[4:7]\n"
      "v_mfma_f32_16x16x32_bf16 v[0:3], v[128:131], v[60:63], v[0:3]\n"
      "s_waitcnt vmcnt(10) lgkmcnt(14)\n"
      "v_mfma_f32_16x16x32_bf16 v[16:19], v[176:179], v[64:67], v[16:19]\n"
      "v_cvt_pk_bf16_f32 v244, v244, v244\n"
      "ds_read_b32 v253, v251 offset:36\n"
      "s_waitcnt lgkmcnt(13)\n"
      "v_mfma_f32_16x16x32_bf16 v[20:23], v[180:183], v[64:67], v[20:23]\n"
      "ds_read_b64_tr_b16 v[112:113], v194 offset:24576\n"
      "ds_read_b64_tr_b16 v[114:115], v194 offset:25088\n"
      "s_waitcnt lgkmcnt(13)\n"
      "v_mfma_f32_16x16x32_bf16 v[24:27], v[184:187], v[64:67], v[24:27]\n"
      "ds_read_b64_tr_b16 v[116:117], v195 offset:24576\n"
      "s_waitcnt lgkmcnt(12)\n"
      "v_mfma_f32_16x16x32_bf16 v[28:31], v[188:191], v[64:67], v[28:31]\n"
      "s_waitcnt vmcnt(9)\n"
      "v_mfma_f32_16x16x32_bf16 v[12:15], v[156:159], v[68:71], v[12:15]\n"
      "ds_read_b64_tr_b16 v[118:119], v195 offset:25088\n"
      "ds_read_b64_tr_b16 v[120:121], v196 offset:24576\n"
      "v_mfma_f32_16x16x32_bf16 v[8:11], v[152:155], v[68:71], v[8:11]\n"
      "ds_read_b64_tr_b16 v[122:123], v196 offset:25088\n"
      "ds_read_b64_tr_b16 v[124:125], v197 offset:24576\n"
      "v_mfma_f32_16x16x32_bf16 v[4:7], v[148:151], v[68:71], v[4:7]\n"
      "ds_read_b64_tr_b16 v[126:127], v197 offset:25088\n"
      "v_mfma_f32_16x16x32_bf16 v[0:3], v[144:147], v[68:71], v[0:3]\n"
      "s_waitcnt vmcnt(8) lgkmcnt(15)\n"
      "v_mfma_f32_16x16x32_bf16 v[16:19], v[96:99], v[72:75], v[16:19]\n"
      "v_lshlrev_b32 v244, 16, v244\n"
      "ds_read_b64_tr_b16 v[128:129], v194 offset:32768\n"
      "s_waitcnt lgkmcnt(14)\n"
      "v_mfma_f32_16x16x32_bf16 v[20:23], v[100:103], v[72:75], v[20:23]\n"
      "ds_read_b64_tr_b16 v[130:131], v194 offset:33280\n"
      "ds_read_b64_tr_b16 v[132:133], v195 offset:32768\n"
      "s_waitcnt lgkmcnt(14)\n"
      "v_mfma_f32_16x16x32_bf16 v[24:27], v[104:107], v[72:75], v[24:27]\n"
      "ds_read_b64_tr_b16 v[134:135], v195 offset:33280\n"
      "s_waitcnt lgkmcnt(13)\n"
      "v_mfma_f32_16x16x32_bf16 v[28:31], v[108:111], v[72:75], v[28:31]\n"
      "s_waitcnt vmcnt(7)\n"
      "v_mfma_f32_16x16x32_bf16 v[12:15], v[172:175], v[76:79], v[12:15]\n"
      "ds_read_b64_tr_b16 v[136:137], v196 offset:32768\n"
      "ds_read_b64_tr_b16 v[138:139], v196 offset:33280\n"
      "v_mfma_f32_16x16x32_bf16 v[8:11], v[168:171], v[76:79], v[8:11]\n"
      "ds_read_b64_tr_b16 v[140:141], v197 offset:32768\n"
      "ds_read_b64_tr_b16 v[142:143], v197 offset:33280\n"
      "v_mfma_f32_16x16x32_bf16 v[4:7], v[164:167], v[76:79], v[4:7]\n"
      "v_mfma_f32_16x16x32_bf16 v[0:3], v[160:163], v[76:79], v[0:3]\n"
      "s_waitcnt lgkmcnt(15)\n"
      "v_readfirstlane_b32 s67, v253\n"
      "s_cmp_lt_u32 s67, 4\n"
      "s_cbranch_scc1 Lw71_%=\n"
      "Lr72_%=:\n"
      "s_waitcnt vmcnt(6) lgkmcnt(14)\n"
      "v_mfma_f32_16x16x32_bf16 v[16:19], v[112:115], v[32:35], v[16:19]\n"
      "s_lshl_b32 s56, s83, 12\n"
      "s_sub_u32 s56, -10688, s56\n"
      "s_waitcnt lgkmcnt(12)\n"
      "v_mfma_f32_16x16x32_bf16 v[20:23], v[116:119], v[32:35], v[20:23]\n"
      "v_add_u32 v194, s56, v194\n"
      "v_add_u32 v195, s56, v195\n"
      "s_waitcnt lgkmcnt(10)\n"
      "v_mfma_f32_16x16x32_bf16 v[24:27], v[120:123], v[32:35], v[24:27]\n"
      "v_add_u32 v196, s56, v196\n"
      "v_add_u32 v197, s56, v197\n"
      "s_waitcnt lgkmcnt(8)\n"
      "v_mfma_f32_16x16x32_bf16 v[28:31], v[124:127], v[32:35], v[28:31]\n"
      "ds_read_b64_tr_b16 v[144:145], v194 offset:0\n"
      "s_waitcnt vmcnt(5)\n"
      "v_mfma_f32_16x16x32_bf16 v[12:15], v[188:191], v[36:39], v[12:15]\n"
      "ds_read_b64_tr_b16 v[146:147], v194 offset:512\n"
      "ds_read_b64_tr_b16 v[148:149], v195 offset:0\n"
      "v_mfma_f32_16x16x32_bf16 v[8:11], v[184:187], v[36:39], v[8:11]\n"
      "ds_read_b64_tr_b16 v[150:151], v195 offset:512\n"
      "ds_read_b64_tr_b16 v[152:153], v196 offset:0\n"
      "v_mfma_f32_16x16x32_bf16 v[4:7], v[180:183], v[36:39], v[4:7]\n"
      "ds_read_b64_tr_b16 v[154:155], v196 offset:512\n"
      "ds_read_b64_tr_b16 v[156:157], v197 offset:0\n"
      "v_mfma_f32_16x16x32_bf16 v[0:3], v[176:179], v[36:39], v[0:3]\n"
      "ds_read_b64_tr_b16 v[158:159], v197 offset:512\n"
      "s_waitcnt vmcnt(4) lgkmcnt(14)\n"
      "v_mfma_f32_16x16x32_bf16 v[16:19], v[128:131], v[40:43], v[16:19]\n"
      "s_waitcnt lgkmcnt(12)\n"
      "v_mfma_f32_16x16x32_bf16 v[20:23], v[132:135], v[40:43], v[20:23]\n"
      "s_waitcnt lgkmcnt(10)\n"
      "v_mfma_f32_16x16x32_bf16 v[24:27], v[136:139], v[40:43], v[24:27]\n"
      "s_waitcnt lgkmcnt(8)\n"
      "v_mfma_f32_16x16x32_bf16 v[28:31], v[140:143], v[40:43], v[28:31]\n"
      "s_waitcnt vmcnt(3)\n"
      "v_mfma_f32_16x16x32_bf16 v[12:15], v[108:111], v[44:47], v[12:15]\n"
      "v_mfma_f32_16x16x32_bf16 v[8:11], v[104:107], v[44:47], v[8:11]\n"
      "v_mfma_f32_16x16x32_bf16 v[4:7], v[100:103], v[44:47], v[4:7]\n"
      "v_mfma_f32_16x16x32_bf16 v[0:3], v[96:99], v[44:47], v[0:3]\n"
      "ds_write_b128 v205, v[16:19] offset:0\n"
      "ds_write_b128 v205, v[20:23] offset:1024\n"
      "ds_write_b128 v205, v[24:27] offset:2048\n"
      "ds_write_b128 v205, v[28:31] offset:3072\n"
      "v_xor_b32 v205, 0x1000, v205\n"
      "s_waitcnt vmcnt(2)\n"
      "v_mfma_f32_16x16x32_bf16 v[12:15], v[124:127], v[48:51], v[12:15]\n"
      "v_mfma_f32_16x16x32_bf16 v[8:11], v[120:123], v[48:51], v[8:11]\n"
      "v_mfma_f32_16x16x32_bf16 v[4:7], v[116:119], v[48:51], v[4:7]\n"
      "v_mfma_f32_16x16x32_bf16 v[0:3], v[112:115], v[48:51], v[0:3]\n"
      "s_waitcnt lgkmcnt(0)\n"
      "s_barrier\n"
      "ds_read_b64_tr_b16 v[232:233], v206 offset:0\n"
      "ds_read_b128 v[216:219], v205 offset:0\n"
      "ds_read_b64_tr_b16 v[234:235], v206 offset:2048\n"
      "ds_read_b128 v[220:223], v205 offset:1024\n"
      "ds_read_b64_tr_b16 v[236:237], v206 offset:4096\n"
      "ds_read_b128 v[224:227], v205 offset:2048\n"
      "ds_read_b64_tr_b16 v[238:239], v206 offset:6144\n"
      "ds_read_b128 v[228:231], v205 offset:3072\n"
      "s_waitcnt vmcnt(1)\n"
      "v_mfma_f32_16x16x32_bf16 v[12:15], v[140:143], v[52:55], v[12:15]\n"
      "v_mfma_f32_16x16x32_bf16 v[8:11], v[136:139], v[52:55], v[8:11]\n"
      "v_mfma_f32_16x16x32_bf16 v[4:7], v[132:135], v[52:55], v[4:7]\n"
      "v_mfma_f32_16x16x32_bf16 v[0:3], v[128:131], v[52:55], v[0:3]\n"
      "s_waitcnt lgkmcnt(7)\n"
      "v_lshlrev_b32 v16, 16, v232\n"
      "v_and_b32 v17, 0xffff0000, v232\n"
      "v_lshlrev_b32 v18, 16, v233\n"
      "v_and_b32 v19, 0xffff0000, v233\n"
      "s_waitcnt lgkmcnt(5)\n"
      "v_lshlrev_b32 v20, 16, v234\n"
      "v_and_b32 v21, 0xffff0000, v234\n"
      "v_lshlrev_b32 v22, 16, v235\n"
      "v_and_b32 v23, 0xffff0000, v235\n"
      "s_waitcnt lgkmcnt(3)\n"
      "v_lshlrev_b32 v24, 16, v236\n"
      "v_and_b32 v25, 0xffff0000, v236\n"
      "v_lshlrev_b32 v26, 16, v237\n"
      "v_and_b32 v27, 0xffff0000, v237\n"
      "s_waitcnt lgkmcnt(1)\n"
      "v_lshlrev_b32 v28, 16, v238\n"
      "v_and_b32 v29, 0xffff0000, v238\n"
      "v_lshlrev_b32 v30, 16, v239\n"
      "v_and_b32 v31, 0xffff0000, v239\n"
      "v_pk_mul_f32 v[16:17], v[16:17], v[242:243] op_sel_hi:[1,0]\n"
      "v_pk_mul_f32 v[18:19], v[18:19], v[242:243] op_sel_hi:[1,0]\n"
      "v_pk_mul_f32 v[20:21], v[20:21], v[242:243] op_sel_hi:[1,0]\n"
      "v_pk_mul_f32 v[22:23], v[22:23], v[242:243] op_sel_hi:[1,0]\n"
      "v_pk_mul_f32 v[24:25], v[24:25], v[242:243] op_sel_hi:[1,0]\n"
      "v_pk_mul_f32 v[26:27], v[26:27], v[242:243] op_sel_hi:[1,0]\n"
      "v_pk_mul_f32 v[28:29], v[28:29], v[242:243] op_sel_hi:[1,0]\n"
      "v_pk_mul_f32 v[30:31], v[30:31], v[242:243] op_sel_hi:[1,0]\n"
      "v_pk_fma_f32 v[16:17], v[216:217], v[244:245], v[16:17] op_sel_hi:[1,0,1]\n"
      "v_pk_fma_f32 v[18:19], v[218:219], v[244:245], v[18:19] op_sel_hi:[1,0,1]\n"
      "v_pk_fma_f32 v[20:21], v[220:221], v[244:245], v[20:21] op_sel_hi:[1,0,1]\n"
      "v_pk_fma_f32 v[22:23], v[222:223], v[244:245], v[22:23] op_sel_hi:[1,0,1]\n"
      "v_pk_fma_f32 v[24:25], v[224:225], v[244:245], v[24:25] op_sel_hi:[1,0,1]\n"
      "v_pk_fma_f32 v[26:27], v[226:227], v[244:245], v[26:27] op_sel_hi:[1,0,1]\n"
      "s_waitcnt lgkmcnt(0)\n"
      "v_pk_fma_f32 v[28:29], v[228:229], v[244:245], v[28:29] op_sel_hi:[1,0,1]\n"
      "v_pk_fma_f32 v[30:31], v[230:231], v[244:245], v[30:31] op_sel_hi:[1,0,1]\n"
      "s_waitcnt vmcnt(0)\n"
      "v_mfma_f32_16x16x32_bf16 v[12:15], v[156:159], v[56:59], v[12:15]\n"
      "v_mfma_f32_16x16x32_bf16 v[8:11], v[152:155], v[56:59], v[8:11]\n"
      "v_mfma_f32_16x16x32_bf16 v[4:7], v[148:151], v[56:59], v[4:7]\n"
      "v_mfma_f32_16x16x32_bf16 v[0:3], v[144:147], v[56:59], v[0:3]\n"
      "s_nop 4\n"
      "v_pk_fma_f32 v[30:31], v[14:15], v[244:245], v[30:31] op_sel_hi:[1,0,1]\n"
      "v_pk_fma_f32 v[28:29], v[12:13], v[244:245], v[28:29] op_sel_hi:[1,0,1]\n"
      "v_pk_fma_f32 v[26:27], v[10:11], v[244:245], v[26:27] op_sel_hi:[1,0,1]\n"
      "v_pk_fma_f32 v[24:25], v[8:9], v[244:245], v[24:25] op_sel_hi:[1,0,1]\n"
      "v_pk_fma_f32 v[22:23], v[6:7], v[244:245], v[22:23] op_sel_hi:[1,0,1]\n"
      "v_pk_fma_f32 v[20:21], v[4:5], v[244:245], v[20:21] op_sel_hi:[1,0,1]\n"
      "v_pk_fma_f32 v[18:19], v[2:3], v[244:245], v[18:19] op_sel_hi:[1,0,1]\n"
      "v_pk_fma_f32 v[16:17], v[0:1], v[244:245], v[16:17] op_sel_hi:[1,0,1]\n"
      "v_cvt_pk_bf16_f32 v223, v30, v31\n"
      "v_cvt_pk_bf16_f32 v222, v28, v29\n"
      "v_cvt_pk_bf16_f32 v221, v26, v27\n"
      "v_cvt_pk_bf16_f32 v220, v24, v25\n"
      "v_cvt_pk_bf16_f32 v219, v22, v23\n"
      "v_cvt_pk_bf16_f32 v218, v20, v21\n"
      "v_cvt_pk_bf16_f32 v217, v18, v19\n"
      "v_cvt_pk_bf16_f32 v216, v16, v17\n"
      "v_permlane16_swap_b32 v220, v222\n"
      "v_permlane16_swap_b32 v221, v223\n"
      "v_permlane16_swap_b32 v216, v218\n"
      "v_permlane16_swap_b32 v217, v219\n"
      "global_store_dwordx4 v207, v[220:223], s[42:43] offset:64\n"
      "global_store_dwordx4 v207, v[216:219], s[42:43] offset:0\n"
      "s_endpgm\n"
      "Lsp8_%=:\n"
      "v_mul_u32_u24 v36, 12, v24\n"
      "v_sub_u32 v28, v28, v36\n"
      "v_add_u32 v28, 0, v28\n"
      "v_mul_u32_u24 v36, 12, v25\n"
      "v_sub_u32 v29, v29, v36\n"
      "v_add_u32 v29, 64, v29\n"
      "v_mul_u32_u24 v36, 12, v26\n"
      "v_sub_u32 v30, v30, v36\n"
      "v_add_u32 v30, 128, v30\n"
      "s_cmp_eq_u32 s51, 15\n"
      "s_cbranch_scc1 Lg10_%=\n"
      "v_cmp_gt_u32 vcc, 4, v28\n"
      "s_nop 1\n"
      "v_cndmask_b32 v5, 2.0, v5, vcc\n"
      "v_mov_b32 v8, v5\n"
      "v_cmp_gt_u32 vcc, 4, v29\n"
      "s_nop 1\n"
      "v_cndmask_b32 v6, 2.0, v6, vcc\n"
      "v_mov_b32 v9, v6\n"
      "v_cmp_gt_u32 vcc, 4, v30\n"
      "s_nop 1\n"
      "v_cndmask_b32 v7, 2.0, v7, vcc\n"
      "v_mov_b32 v10, v7\n"
      "s_branch Lsr9_%=\n"
      "Lg10_%=:\n"
      "s_sub_u32 s12, s38, 0x800\n"
      "s_subb_u32 s13, s39, 0\n"
      "s_movk_i32 s14, 0xc00\n"
      "s_mov_b64 s[16:17], s[12:13]\n"
      "s_movk_i32 s18, 0xc00\n"
      "s_mov_b32 s85, 2\n"
      "v_and_b32 v36, 1, v24\n"
      "v_sub_u32 v37, v24, v36\n"
      "v_lshlrev_b32 v37, 1, v37\n"
      "v_add_u32 v37, 2048, v37\n"
      "v_lshl_or_b32 v36, v28, 1, v36\n"
      "v_cmp_eq_u32 vcc, 0, v36\n"
      "s_nop 1\n"
      "v_cndmask_b32 v5, 2.0, v37, vcc\n"
      "v_cmp_eq_u32 vcc, 1, v36\n"
      "s_nop 1\n"
      "v_cndmask_b32 v8, 2.0, v37, vcc\n"
      "v_and_b32 v36, 1, v25\n"
      "v_sub_u32 v37, v25, v36\n"
      "v_lshlrev_b32 v37, 1, v37\n"
      "v_add_u32 v37, 1024, v37\n"
      "v_lshl_or_b32 v36, v29, 1, v36\n"
      "v_cmp_eq_u32 vcc, 0, v36\n"
      "s_nop 1\n"
      "v_cndmask_b32 v6, 2.0, v37, vcc\n"
      "v_cmp_eq_u32 vcc, 1, v36\n"
      "s_nop 1\n"
      "v_cndmask_b32 v9, 2.0, v37, vcc\n"
      "v_and_b32 v36, 1, v26\n"
      "v_sub_u32 v37, v26, v36\n"
      "v_lshlrev_b32 v37, 1, v37\n"
      "v_add_u32 v37, 0, v37\n"
      "v_lshl_or_b32 v36, v30, 1, v36\n"
      "v_cmp_eq_u32 vcc, 0, v36\n"
      "s_nop 1\n"
      "v_cndmask_b32 v7, 2.0, v37, vcc\n"
      "v_cmp_eq_u32 vcc, 1, v36\n"
      "s_nop 1\n"
      "v_cndmask_b32 v10, 2.0, v37, vcc\n"
      "s_branch Lsr9_%=\n"
      "Lw20_%=:\n"
      "s_mov_b32 s68, 524288\n"
      "Ls22_%=:\n"
      "s_sleep 1\n"
      "ds_read_b32 v253, v251 offset:28\n"
      "s_sub_u32 s68, s68, 1\n"
      "s_cbranch_scc1 Lr21_%=\n"
      "s_waitcnt lgkmcnt(0)\n"
      "v_readfirstlane_b32 s67, v253\n"
      "s_cmp_lt_u32 s67, 4\n"
      "s_cbranch_scc1 Ls22_%=\n"
      "s_branch Lr21_%=\n"
      "Lw25_%=:\n"
      "s_mov_b32 s68, 524288\n"
      "Ls27_%=:\n"
      "ds_read_b32 v253, v251 offset:32\n"
      "s_sub_u32 s68, s68, 1\n"
      "s_cbranch_scc1 Lr26_%=\n"
      "s_waitcnt lgkmcnt(0)\n"
      "v_readfirstlane_b32 s67, v253\n"
      "s_cmp_lt_u32 s67, 4\n"
      "s_cbranch_scc1 Ls27_%=\n"
      "s_branch Lr26_%=\n"
      "Lw28_%=:\n"
      "s_mov_b32 s68, 524288\n"
      "Ls30_%=:\n"
      "s_sleep 4\n"
      "ds_read_b32 v253, v251 offset:36\n"
      "s_sub_u32 s68, s68, 1\n"
      "s_cbranch_scc1 Lr29_%=\n"
      "s_waitcnt lgkmcnt(0)\n"
      "v_readfirstlane_b32 s67, v253\n"
      "s_cmp_lt_u32 s67, 4\n"
      "s_cbranch_scc1 Ls30_%=\n"
      "s_branch Lr29_%=\n"
      "Ll42_%=:\n"
      "ds_read_b64_tr_b16 v[96:97], v182 offset:0\n"
      "ds_read_b64_tr_b16 v[98:99], v182 offset:768\n"
      "ds_read_b128 v[120:123], v176 offset:0\n"
      "ds_read_b128 v[124:127], v176 offset:2048\n"
      "ds_read_b128 v[128:131], v176 offset:4096\n"
      "ds_read_b128 v[132:135], v176 offset:6144\n"
      "ds_read_b64_tr_b16 v[100:101], v182 offset:12288\n"
      "ds_read_b64_tr_b16 v[102:103], v182 offset:13056\n"
      "s_lshl_b32 s56, s83, 9\n"
      "s_lshl1_add_u32 s57, s49, s84\n"
      "s_mul_i32 s57, s57, 172\n"
      "s_mul_i32 s58, s51, 3\n"
      "s_add_u32 s57, s57, s58\n"
      "s_lshl_b32 s57, s57, 10\n"
      "v_lshrrev_b32 v213, 4, v250\n"
      "v_bfe_u32 v214, v250, 2, 2\n"
      "s_add_u32 s65, s57, 4456448\n"
      "v_lshl_add_u32 v215, v213, 3, v214\n"
      "s_lshl_b32 s59, s83, 12\n"
      "v_bfe_u32 v211, v250, 4, 1\n"
      "v_bfe_u32 v214, v250, 3, 2\n"
      "s_add_u32 s59, s59, 40960\n"
      "s_lshl2_add_u32 s66, s52, s44\n"
      "v_mul_u32_u24 v211, 44032, v211\n"
      "v_lshrrev_b32 v212, 5, v250\n"
      "v_lshl_add_u32 v215, v215, 2, v214\n"
      "s_mul_i32 s60, s83, 3\n"
      "s_mul_i32 s61, s66, 43\n"
      "v_and_b32 v214, 3, v250\n"
      "s_add_u32 s61, s61, s60\n"
      "v_lshl_add_u32 v211, v212, 8, v211\n"
      "s_lshl_b32 s61, s61, 10\n"
      "v_and_b32 v212, 15, v250\n"
      "v_lshl_add_u32 v215, v215, 2, v214\n"
      "s_xor_b32 s62, s66, 1\n"
      "s_mul_i32 s62, s62, 43\n"
      "v_lshl_add_u32 v211, v212, 4, v211\n"
      "v_lshl_add_u32 v194, v215, 3, s59\n"
      "s_add_u32 s62, s62, s60\n"
      "s_lshl_b32 s62, s62, 10\n"
      "v_add_u32 v209, s56, v211\n"
      "v_mov_b32 v192, 0xc0135761\n"
      "s_mov_b32 s70, 0xbdd2d3e8\n"
      "v_xor_b32 v195, 32, v194\n"
      "v_xor_b32 v196, 64, v194\n"
      "v_xor_b32 v197, 96, v194\n"
      "v_lshl_add_u32 v198, v250, 4, s61\n"
      "v_lshl_add_u32 v199, v250, 4, s62\n"
      "v_lshrrev_b32 v215, 5, v250\n"
      "v_and_b32 v211, 15, v250\n"
      "s_lshl_b32 s56, s66, 4\n"
      "v_and_b32 v213, 3, v250\n"
      "v_and_b32 v214, 16, v250\n"
      "v_lshlrev_b32 v215, 4, v215\n"
      "s_lshl_b32 s57, s44, 12\n"
      "v_add_u32 v211, s56, v211\n"
      "s_add_u32 s57, s57, 81920\n"
      "v_lshrrev_b32 v212, 2, v250\n"
      "v_lshlrev_b32 v213, 3, v213\n"
      "v_lshl_add_u32 v214, v214, 1, v215\n"
      "s_lshl_b32 s58, s44, 5\n"
      "v_lshl_add_u32 v212, v212, 7, v213\n"
      "s_add_u32 s58, s58, 512\n"
      "v_lshl_add_u32 v214, v211, 10, v214\n"
      "s_lshl_b32 s59, s53, 7\n"
      "v_lshlrev_b32 v208, 2, v211\n"
      "v_lshl_add_u32 v205, v250, 4, s57\n"
      "v_add_u32 v206, s58, v212\n"
      "v_add_u32 v207, s59, v214\n"
      "s_mov_b32 s60, 36864\n"
      "s_lshl4_add_u32 s56, s49, s51\n"
      "s_lshl2_add_u32 s56, s56, s44\n"
      "s_lshl_b32 s56, s56, 7\n"
      "s_add_u32 s64, s56, 65536\n"
      "s_lshr_b32 s57, s52, 1\n"
      "s_lshl_b32 s57, s57, 13\n"
      "s_add_u32 s57, s57, 65536\n"
      "s_and_b32 s58, s52, 1\n"
      "s_lshl1_add_u32 s58, s83, s58\n"
      "v_min_u32 v212, 13, v250\n"
      "s_lshl_b32 s58, s58, 8\n"
      "v_min_u32 v214, 17, v250\n"
      "s_add_u32 s58, s58, s57\n"
      "v_and_b32 v213, 14, v212\n"
      "v_and_b32 v215, 30, v214\n"
      "s_and_b32 s59, s52, 1\n"
      "s_lshl_b32 s59, s59, 8\n"
      "v_lshl_add_u32 v211, v250, 1, s83\n"
      "v_add_u32 v220, 7, v250\n"
      "v_mad_u32_u24 v212, v213, 3, v212\n"
      "v_add_u32 v214, v214, v215\n"
      "s_add_u32 s59, s59, s57\n"
      "s_add_u32 s59, s59, 3584\n"
      "v_cmp_gt_u32 vcc, 14, v250\n"
      "v_min3_u32 v211, v211, v220, 15\n"
      "v_lshl_add_u32 v193, v212, 7, s58\n"
      "v_lshl_add_u32 v214, v214, 7, s59\n"
      "v_lshl_add_u32 v248, v211, 9, s57\n"
      "v_cndmask_b32 v193, v214, v193, vcc\n"
      "v_lshrrev_b32 v230, 3, v250\n"
      "s_lshl_b32 s56, s53, 7\n"
      "v_and_b32 v231, 2, v230\n"
      "s_and_b32 s57, s44, 1\n"
      "v_xor_b32 v231, v250, v231\n"
      "s_lshl_b32 s57, s57, 6\n"
      "v_and_b32 v231, 7, v231\n"
      "s_lshl_b32 s58, s44, 13\n"
      "v_lshl_add_u32 v231, v231, 4, s56\n"
      "v_lshl_add_u32 v220, v230, 10, v231\n"
      "v_xor_b32 v231, 64, v220\n"
      "v_xor_b32 v220, s57, v220\n"
      "v_add_u32 v221, 7168, v231\n"
      "v_add_u32 v249, s58, v220\n"
      "s_waitcnt lgkmcnt(5)\n"
      "v_mfma_f32_16x16x32_bf16 v[0:3], v[96:99], v[120:123], 0\n"
      "s_waitcnt lgkmcnt(4)\n"
      "v_mfma_f32_16x16x32_bf16 v[4:7], v[96:99], v[124:127], 0\n"
      "s_waitcnt lgkmcnt(3)\n"
      "v_mfma_f32_16x16x32_bf16 v[8:11], v[96:99], v[128:131], 0\n"
      "s_waitcnt lgkmcnt(2)\n"
      "v_mfma_f32_16x16x32_bf16 v[12:15], v[96:99], v[132:135], 0\n"
      "s_waitcnt lgkmcnt(0)\n"
      "v_mfma_f32_16x16x32_bf16 v[28:31], v[100:103], v[132:135], 0\n"
      "v_mfma_f32_16x16x32_bf16 v[24:27], v[100:103], v[128:131], 0\n"
      "v_mfma_f32_16x16x32_bf16 v[20:23], v[100:103], v[124:127], 0\n"
      "v_mfma_f32_16x16x32_bf16 v[16:19], v[100:103], v[120:123], 0\n"
      "s_barrier\n"
      "ds_read_b64_tr_b16 v[136:137], v182 offset:6144\n"
      "ds_read_b64_tr_b16 v[138:139], v182 offset:6912\n"
      "ds_read_b128 v[160:163], v177 offset:0\n"
      "ds_read_b128 v[164:167], v177 offset:2048\n"
      "ds_read_b128 v[168:171], v177 offset:4096\n"
      "ds_read_b128 v[172:175], v177 offset:6144\n"
      "ds_read_b64_tr_b16 v[140:141], v182 offset:18432\n"
      "ds_read_b64_tr_b16 v[142:143], v182 offset:19200\n"
      "s_waitcnt lgkmcnt(5)\n"
      "v_mfma_f32_16x16x32_bf16 v[0:3], v[136:139], v[160:163], v[0:3]\n"
      "s_waitcnt lgkmcnt(4)\n"
      "v_mfma_f32_16x16x32_bf16 v[4:7], v[136:139], v[164:167], v[4:7]\n"
      "s_waitcnt lgkmcnt(3)\n"
      "v_mfma_f32_16x16x32_bf16 v[8:11], v[136:139], v[168:171], v[8:11]\n"
      "s_waitcnt lgkmcnt(2)\n"
      "v_mfma_f32_16x16x32_bf16 v[12:15], v[136:139], v[172:175], v[12:15]\n"
      "s_waitcnt lgkmcnt(0)\n"
      "v_mfma_f32_16x16x32_bf16 v[28:31], v[140:143], v[172:175], v[28:31]\n"
      "v_mfma_f32_16x16x32_bf16 v[24:27], v[140:143], v[168:171], v[24:27]\n"
      "v_mfma_f32_16x16x32_bf16 v[20:23], v[140:143], v[164:167], v[20:23]\n"
      "v_mfma_f32_16x16x32_bf16 v[16:19], v[140:143], v[160:163], v[16:19]\n"
      "s_barrier\n"
      "ds_read_b64_tr_b16 v[96:97], v183 offset:0\n"
      "ds_read_b64_tr_b16 v[98:99], v183 offset:768\n"
      "ds_read_b128 v[120:123], v178 offset:0\n"
      "ds_read_b128 v[124:127], v178 offset:2048\n"
      "ds_read_b128 v[128:131], v178 offset:4096\n"
      "ds_read_b128 v[132:135], v178 offset:6144\n"
      "ds_read_b64_tr_b16 v[100:101], v183 offset:12288\n"
      "ds_read_b64_tr_b16 v[102:103], v183 offset:13056\n"
      "s_waitcnt lgkmcnt(5)\n"
      "v_mfma_f32_16x16x32_bf16 v[0:3], v[96:99], v[120:123], v[0:3]\n"
      "s_waitcnt lgkmcnt(4)\n"
      "v_mfma_f32_16x16x32_bf16 v[4:7], v[96:99], v[124:127], v[4:7]\n"
      "s_waitcnt lgkmcnt(3)\n"
      "v_mfma_f32_16x16x32_bf16 v[8:11], v[96:99], v[128:131], v[8:11]\n"
      "s_waitcnt lgkmcnt(2)\n"
      "v_mfma_f32_16x16x32_bf16 v[12:15], v[96:99], v[132:135], v[12:15]\n"
      "s_waitcnt lgkmcnt(0)\n"
      "v_mfma_f32_16x16x32_bf16 v[28:31], v[100:103], v[132:135], v[28:31]\n"
      "v_mfma_f32_16x16x32_bf16 v[24:27], v[100:103], v[128:131], v[24:27]\n"
      "v_mfma_f32_16x16x32_bf16 v[20:23], v[100:103], v[124:127], v[20:23]\n"
      "v_mfma_f32_16x16x32_bf16 v[16:19], v[100:103], v[120:123], v[16:19]\n"
      "s_barrier\n"
      "ds_read_b64_tr_b16 v[136:137], v183 offset:6144\n"
      "ds_read_b64_tr_b16 v[138:139], v183 offset:6912\n"
      "ds_read_b128 v[160:163], v179 offset:0\n"
      "ds_read_b128 v[164:167], v179 offset:2048\n"
      "ds_read_b128 v[168:171], v179 offset:4096\n"
      "ds_read_b128 v[172:175], v179 offset:6144\n"
      "ds_read_b64_tr_b16 v[140:141], v183 offset:18432\n"
      "ds_read_b64_tr_b16 v[142:143], v183 offset:19200\n"
      "s_waitcnt lgkmcnt(5)\n"
      "v_mfma_f32_16x16x32_bf16 v[0:3], v[136:139], v[160:163], v[0:3]\n"
      "s_waitcnt lgkmcnt(4)\n"
      "v_mfma_f32_16x16x32_bf16 v[4:7], v[136:139], v[164:167], v[4:7]\n"
      "s_waitcnt lgkmcnt(3)\n"
      "v_mfma_f32_16x16x32_bf16 v[8:11], v[136:139], v[168:171], v[8:11]\n"
      "s_waitcnt lgkmcnt(2)\n"
      "v_mfma_f32_16x16x32_bf16 v[12:15], v[136:139], v[172:175], v[12:15]\n"
      "s_waitcnt lgkmcnt(0)\n"
      "v_mfma_f32_16x16x32_bf16 v[28:31], v[140:143], v[172:175], v[28:31]\n"
      "v_mfma_f32_16x16x32_bf16 v[24:27], v[140:143], v[168:171], v[24:27]\n"
      "v_mfma_f32_16x16x32_bf16 v[20:23], v[140:143], v[164:167], v[20:23]\n"
      "v_mfma_f32_16x16x32_bf16 v[16:19], v[140:143], v[160:163], v[16:19]\n"
      "s_barrier\n"
      "ds_read_b64_tr_b16 v[96:97], v184 offset:0\n"
      "ds_read_b64_tr_b16 v[98:99], v184 offset:768\n"
      "ds_read_b128 v[120:123], v180 offset:0\n"
      "ds_read_b128 v[124:127], v180 offset:2048\n"
      "ds_read_b128 v[128:131], v180 offset:4096\n"
      "ds_read_b128 v[132:135], v180 offset:6144\n"
      "ds_read_b64_tr_b16 v[100:101], v184 offset:12288\n"
      "ds_read_b64_tr_b16 v[102:103], v184 offset:13056\n"
      "s_waitcnt lgkmcnt(5)\n"
      "v_mfma_f32_16x16x32_bf16 v[0:3], v[96:99], v[120:123], v[0:3]\n"
      "s_waitcnt lgkmcnt(4)\n"
      "v_mfma_f32_16x16x32_bf16 v[4:7], v[96:99], v[124:127], v[4:7]\n"
      "s_waitcnt lgkmcnt(3)\n"
      "v_mfma_f32_16x16x32_bf16 v[8:11], v[96:99], v[128:131], v[8:11]\n"
      "s_waitcnt lgkmcnt(2)\n"
      "v_mfma_f32_16x16x32_bf16 v[12:15], v[96:99], v[132:135], v[12:15]\n"
      "s_waitcnt lgkmcnt(0)\n"
      "v_mfma_f32_16x16x32_bf16 v[28:31], v[100:103], v[132:135], v[28:31]\n"
      "v_mfma_f32_16x16x32_bf16 v[24:27], v[100:103], v[128:131], v[24:27]\n"
      "v_mfma_f32_16x16x32_bf16 v[20:23], v[100:103], v[124:127], v[20:23]\n"
      "v_mfma_f32_16x16x32_bf16 v[16:19], v[100:103], v[120:123], v[16:19]\n"
      "s_barrier\n"
      "ds_read_b64_tr_b16 v[136:137], v184 offset:6144\n"
      "ds_read_b64_tr_b16 v[138:139], v184 offset:6912\n"
      "ds_read_b128 v[160:163], v181 offset:0\n"
      "ds_read_b128 v[164:167], v181 offset:2048\n"
      "ds_read_b128 v[168:171], v181 offset:4096\n"
      "ds_read_b128 v[172:175], v181 offset:6144\n"
      "ds_read_b64_tr_b16 v[140:141], v184 offset:18432\n"
      "ds_read_b64_tr_b16 v[142:143], v184 offset:19200\n"
      "s_waitcnt lgkmcnt(5)\n"
      "v_mfma_f32_16x16x32_bf16 v[0:3], v[136:139], v[160:163], v[0:3]\n"
      "s_waitcnt lgkmcnt(4)\n"
      "v_mfma_f32_16x16x32_bf16 v[4:7], v[136:139], v[164:167], v[4:7]\n"
      "s_waitcnt lgkmcnt(3)\n"
      "v_mfma_f32_16x16x32_bf16 v[8:11], v[136:139], v[168:171], v[8:11]\n"
      "s_waitcnt lgkmcnt(2)\n"
      "v_mfma_f32_16x16x32_bf16 v[12:15], v[136:139], v[172:175], v[12:15]\n"
      "s_waitcnt lgkmcnt(0)\n"
      "v_mfma_f32_16x16x32_bf16 v[28:31], v[140:143], v[172:175], v[28:31]\n"
      "v_mfma_f32_16x16x32_bf16 v[24:27], v[140:143], v[168:171], v[24:27]\n"
      "v_mfma_f32_16x16x32_bf16 v[20:23], v[140:143], v[164:167], v[20:23]\n"
      "v_mfma_f32_16x16x32_bf16 v[16:19], v[140:143], v[160:163], v[16:19]\n"
      "s_barrier\n"
      "ds_read_b64_tr_b16 v[96:97], v182 offset:0\n"
      "ds_read_b64_tr_b16 v[98:99], v182 offset:768\n"
      "ds_read_b128 v[120:123], v176 offset:0\n"
      "ds_read_b128 v[124:127], v176 offset:2048\n"
      "ds_read_b128 v[128:131], v176 offset:4096\n"
      "ds_read_b128 v[132:135], v176 offset:6144\n"
      "ds_read_b64_tr_b16 v[100:101], v182 offset:12288\n"
      "ds_read_b64_tr_b16 v[102:103], v182 offset:13056\n"
      "s_waitcnt lgkmcnt(5)\n"
      "v_mfma_f32_16x16x32_bf16 v[0:3], v[96:99], v[120:123], v[0:3]\n"
      "s_waitcnt lgkmcnt(4)\n"
      "v_mfma_f32_16x16x32_bf16 v[4:7], v[96:99], v[124:127], v[4:7]\n"
      "s_waitcnt lgkmcnt(3)\n"
      "v_mfma_f32_16x16x32_bf16 v[8:11], v[96:99], v[128:131], v[8:11]\n"
      "s_waitcnt lgkmcnt(2)\n"
      "v_mfma_f32_16x16x32_bf16 v[12:15], v[96:99], v[132:135], v[12:15]\n"
      "s_waitcnt lgkmcnt(0)\n"
      "v_mfma_f32_16x16x32_bf16 v[28:31], v[100:103], v[132:135], v[28:31]\n"
      "v_mfma_f32_16x16x32_bf16 v[24:27], v[100:103], v[128:131], v[24:27]\n"
      "v_mfma_f32_16x16x32_bf16 v[20:23], v[100:103], v[124:127], v[20:23]\n"
      "v_mfma_f32_16x16x32_bf16 v[16:19], v[100:103], v[120:123], v[16:19]\n"
      "s_barrier\n"
      "ds_read_b64_tr_b16 v[136:137], v182 offset:6144\n"
      "ds_read_b64_tr_b16 v[138:139], v182 offset:6912\n"
      "ds_read_b128 v[160:163], v177 offset:0\n"
      "ds_read_b128 v[164:167], v177 offset:2048\n"
      "ds_read_b128 v[168:171], v177 offset:4096\n"
      "ds_read_b128 v[172:175], v177 offset:6144\n"
      "ds_read_b64_tr_b16 v[140:141], v182 offset:18432\n"
      "ds_read_b64_tr_b16 v[142:143], v182 offset:19200\n"
      "s_waitcnt lgkmcnt(5)\n"
      "v_mfma_f32_16x16x32_bf16 v[0:3], v[136:139], v[160:163], v[0:3]\n"
      "s_waitcnt lgkmcnt(4)\n"
      "v_mfma_f32_16x16x32_bf16 v[4:7], v[136:139], v[164:167], v[4:7]\n"
      "s_waitcnt lgkmcnt(3)\n"
      "v_mfma_f32_16x16x32_bf16 v[8:11], v[136:139], v[168:171], v[8:11]\n"
      "s_waitcnt lgkmcnt(2)\n"
      "v_mfma_f32_16x16x32_bf16 v[12:15], v[136:139], v[172:175], v[12:15]\n"
      "s_waitcnt lgkmcnt(0)\n"
      "v_mfma_f32_16x16x32_bf16 v[28:31], v[140:143], v[172:175], v[28:31]\n"
      "v_mfma_f32_16x16x32_bf16 v[24:27], v[140:143], v[168:171], v[24:27]\n"
      "v_mfma_f32_16x16x32_bf16 v[20:23], v[140:143], v[164:167], v[20:23]\n"
      "v_mfma_f32_16x16x32_bf16 v[16:19], v[140:143], v[160:163], v[16:19]\n"
      "s_barrier\n"
      "ds_read_b64_tr_b16 v[96:97], v183 offset:0\n"
      "ds_read_b64_tr_b16 v[98:99], v183 offset:768\n"
      "ds_read_b128 v[120:123], v178 offset:0\n"
      "ds_read_b128 v[124:127], v178 offset:2048\n"
      "ds_read_b128 v[128:131], v178 offset:4096\n"
      "ds_read_b128 v[132:135], v178 offset:6144\n"
      "ds_read_b64_tr_b16 v[100:101], v183 offset:12288\n"
      "ds_read_b64_tr_b16 v[102:103], v183 offset:13056\n"
      "s_waitcnt lgkmcnt(5)\n"
      "v_mfma_f32_16x16x32_bf16 v[0:3], v[96:99], v[120:123], v[0:3]\n"
      "s_waitcnt lgkmcnt(4)\n"
      "v_mfma_f32_16x16x32_bf16 v[4:7], v[96:99], v[124:127], v[4:7]\n"
      "s_waitcnt lgkmcnt(3)\n"
      "v_mfma_f32_16x16x32_bf16 v[8:11], v[96:99], v[128:131], v[8:11]\n"
      "s_waitcnt lgkmcnt(2)\n"
      "v_mfma_f32_16x16x32_bf16 v[12:15], v[96:99], v[132:135], v[12:15]\n"
      "s_waitcnt lgkmcnt(0)\n"
      "v_mfma_f32_16x16x32_bf16 v[28:31], v[100:103], v[132:135], v[28:31]\n"
      "v_mfma_f32_16x16x32_bf16 v[24:27], v[100:103], v[128:131], v[24:27]\n"
      "v_mfma_f32_16x16x32_bf16 v[20:23], v[100:103], v[124:127], v[20:23]\n"
      "v_mfma_f32_16x16x32_bf16 v[16:19], v[100:103], v[120:123], v[16:19]\n"
      "s_barrier\n"
      "ds_read_b64_tr_b16 v[136:137], v183 offset:6144\n"
      "ds_read_b64_tr_b16 v[138:139], v183 offset:6912\n"
      "ds_read_b128 v[160:163], v179 offset:0\n"
      "ds_read_b128 v[164:167], v179 offset:2048\n"
      "ds_read_b128 v[168:171], v179 offset:4096\n"
      "ds_read_b128 v[172:175], v179 offset:6144\n"
      "ds_read_b64_tr_b16 v[140:141], v183 offset:18432\n"
      "ds_read_b64_tr_b16 v[142:143], v183 offset:19200\n"
      "s_waitcnt lgkmcnt(5)\n"
      "v_mfma_f32_16x16x32_bf16 v[0:3], v[136:139], v[160:163], v[0:3]\n"
      "s_waitcnt lgkmcnt(4)\n"
      "v_mfma_f32_16x16x32_bf16 v[4:7], v[136:139], v[164:167], v[4:7]\n"
      "s_waitcnt lgkmcnt(3)\n"
      "v_mfma_f32_16x16x32_bf16 v[8:11], v[136:139], v[168:171], v[8:11]\n"
      "s_waitcnt lgkmcnt(2)\n"
      "v_mfma_f32_16x16x32_bf16 v[12:15], v[136:139], v[172:175], v[12:15]\n"
      "s_waitcnt lgkmcnt(0)\n"
      "v_mfma_f32_16x16x32_bf16 v[28:31], v[140:143], v[172:175], v[28:31]\n"
      "v_mfma_f32_16x16x32_bf16 v[24:27], v[140:143], v[168:171], v[24:27]\n"
      "v_mfma_f32_16x16x32_bf16 v[20:23], v[140:143], v[164:167], v[20:23]\n"
      "v_mfma_f32_16x16x32_bf16 v[16:19], v[140:143], v[160:163], v[16:19]\n"
      "s_barrier\n"
      "ds_read_b64_tr_b16 v[96:97], v184 offset:0\n"
      "ds_read_b64_tr_b16 v[98:99], v184 offset:768\n"
      "ds_read_b128 v[120:123], v180 offset:0\n"
      "ds_read_b128 v[124:127], v180 offset:2048\n"
      "ds_read_b128 v[128:131], v180 offset:4096\n"
      "ds_read_b128 v[132:135], v180 offset:6144\n"
      "ds_read_b64_tr_b16 v[100:101], v184 offset:12288\n"
      "ds_read_b64_tr_b16 v[102:103], v184 offset:13056\n"
      "s_waitcnt lgkmcnt(5)\n"
      "v_mfma_f32_16x16x32_bf16 v[0:3], v[96:99], v[120:123], v[0:3]\n"
      "s_waitcnt lgkmcnt(4)\n"
      "v_mfma_f32_16x16x32_bf16 v[4:7], v[96:99], v[124:127], v[4:7]\n"
      "s_waitcnt lgkmcnt(3)\n"
      "v_mfma_f32_16x16x32_bf16 v[8:11], v[96:99], v[128:131], v[8:11]\n"
      "s_waitcnt lgkmcnt(2)\n"
      "v_mfma_f32_16x16x32_bf16 v[12:15], v[96:99], v[132:135], v[12:15]\n"
      "s_waitcnt lgkmcnt(0)\n"
      "v_mfma_f32_16x16x32_bf16 v[28:31], v[100:103], v[132:135], v[28:31]\n"
      "v_mfma_f32_16x16x32_bf16 v[24:27], v[100:103], v[128:131], v[24:27]\n"
      "v_mfma_f32_16x16x32_bf16 v[20:23], v[100:103], v[124:127], v[20:23]\n"
      "v_mfma_f32_16x16x32_bf16 v[16:19], v[100:103], v[120:123], v[16:19]\n"
      "s_barrier\n"
      "ds_read_b64_tr_b16 v[136:137], v184 offset:6144\n"
      "ds_read_b64_tr_b16 v[138:139], v184 offset:6912\n"
      "ds_read_b128 v[160:163], v181 offset:0\n"
      "ds_read_b128 v[164:167], v181 offset:2048\n"
      "ds_read_b128 v[168:171], v181 offset:4096\n"
      "ds_read_b128 v[172:175], v181 offset:6144\n"
      "ds_read_b64_tr_b16 v[140:141], v184 offset:18432\n"
      "ds_read_b64_tr_b16 v[142:143], v184 offset:19200\n"
      "s_waitcnt lgkmcnt(5)\n"
      "v_mfma_f32_16x16x32_bf16 v[0:3], v[136:139], v[160:163], v[0:3]\n"
      "s_waitcnt lgkmcnt(4)\n"
      "v_mfma_f32_16x16x32_bf16 v[4:7], v[136:139], v[164:167], v[4:7]\n"
      "s_waitcnt lgkmcnt(3)\n"
      "v_mfma_f32_16x16x32_bf16 v[8:11], v[136:139], v[168:171], v[8:11]\n"
      "s_waitcnt lgkmcnt(2)\n"
      "v_mfma_f32_16x16x32_bf16 v[12:15], v[136:139], v[172:175], v[12:15]\n"
      "s_waitcnt lgkmcnt(0)\n"
      "v_mfma_f32_16x16x32_bf16 v[28:31], v[140:143], v[172:175], v[28:31]\n"
      "v_mfma_f32_16x16x32_bf16 v[24:27], v[140:143], v[168:171], v[24:27]\n"
      "v_mfma_f32_16x16x32_bf16 v[20:23], v[140:143], v[164:167], v[20:23]\n"
      "v_mfma_f32_16x16x32_bf16 v[16:19], v[140:143], v[160:163], v[16:19]\n"
      "s_barrier\n"
      "ds_read_b64_tr_b16 v[96:97], v182 offset:0\n"
      "ds_read_b64_tr_b16 v[98:99], v182 offset:768\n"
      "ds_read_b128 v[120:123], v176 offset:0\n"
      "ds_read_b128 v[124:127], v176 offset:2048\n"
      "ds_read_b128 v[128:131], v176 offset:4096\n"
      "ds_read_b128 v[132:135], v176 offset:6144\n"
      "ds_read_b64_tr_b16 v[100:101], v182 offset:12288\n"
      "ds_read_b64_tr_b16 v[102:103], v182 offset:13056\n"
      "s_waitcnt lgkmcnt(5)\n"
      "v_mfma_f32_16x16x32_bf16 v[0:3], v[96:99], v[120:123], v[0:3]\n"
      "s_waitcnt lgkmcnt(4)\n"
      "v_mfma_f32_16x16x32_bf16 v[4:7], v[96:99], v[124:127], v[4:7]\n"
      "s_waitcnt lgkmcnt(3)\n"
      "v_mfma_f32_16x16x32_bf16 v[8:11], v[96:99], v[128:131], v[8:11]\n"
      "s_waitcnt lgkmcnt(2)\n"
      "v_mfma_f32_16x16x32_bf16 v[12:15], v[96:99], v[132:135], v[12:15]\n"
      "s_waitcnt lgkmcnt(0)\n"
      "v_mfma_f32_16x16x32_bf16 v[28:31], v[100:103], v[132:135], v[28:31]\n"
      "v_mfma_f32_16x16x32_bf16 v[24:27], v[100:103], v[128:131], v[24:27]\n"
      "v_mfma_f32_16x16x32_bf16 v[20:23], v[100:103], v[124:127], v[20:23]\n"
      "v_mfma_f32_16x16x32_bf16 v[16:19], v[100:103], v[120:123], v[16:19]\n"
      "s_barrier\n"
      "ds_read_b64_tr_b16 v[136:137], v182 offset:6144\n"
      "ds_read_b64_tr_b16 v[138:139], v182 offset:6912\n"
      "ds_read_b128 v[160:163], v177 offset:0\n"
      "ds_read_b128 v[164:167], v177 offset:2048\n"
      "ds_read_b128 v[168:171], v177 offset:4096\n"
      "ds_read_b128 v[172:175], v177 offset:6144\n"
      "ds_read_b64_tr_b16 v[140:141], v182 offset:18432\n"
      "ds_read_b64_tr_b16 v[142:143], v182 offset:19200\n"
      "s_waitcnt lgkmcnt(5)\n"
      "v_mfma_f32_16x16x32_bf16 v[0:3], v[136:139], v[160:163], v[0:3]\n"
      "s_waitcnt lgkmcnt(4)\n"
      "v_mfma_f32_16x16x32_bf16 v[4:7], v[136:139], v[164:167], v[4:7]\n"
      "s_waitcnt lgkmcnt(3)\n"
      "v_mfma_f32_16x16x32_bf16 v[8:11], v[136:139], v[168:171], v[8:11]\n"
      "s_waitcnt lgkmcnt(2)\n"
      "v_mfma_f32_16x16x32_bf16 v[12:15], v[136:139], v[172:175], v[12:15]\n"
      "s_waitcnt lgkmcnt(0)\n"
      "v_mfma_f32_16x16x32_bf16 v[28:31], v[140:143], v[172:175], v[28:31]\n"
      "v_mfma_f32_16x16x32_bf16 v[24:27], v[140:143], v[168:171], v[24:27]\n"
      "v_mfma_f32_16x16x32_bf16 v[20:23], v[140:143], v[164:167], v[20:23]\n"
      "v_mfma_f32_16x16x32_bf16 v[16:19], v[140:143], v[160:163], v[16:19]\n"
      "s_barrier\n"
      "ds_read_b64_tr_b16 v[96:97], v183 offset:0\n"
      "ds_read_b64_tr_b16 v[98:99], v183 offset:768\n"
      "ds_read_b128 v[120:123], v178 offset:0\n"
      "ds_read_b128 v[124:127], v178 offset:2048\n"
      "ds_read_b128 v[128:131], v178 offset:4096\n"
      "ds_read_b128 v[132:135], v178 offset:6144\n"
      "ds_read_b64_tr_b16 v[100:101], v183 offset:12288\n"
      "ds_read_b64_tr_b16 v[102:103], v183 offset:13056\n"
      "s_waitcnt lgkmcnt(5)\n"
      "v_mfma_f32_16x16x32_bf16 v[0:3], v[96:99], v[120:123], v[0:3]\n"
      "s_waitcnt lgkmcnt(4)\n"
      "v_mfma_f32_16x16x32_bf16 v[4:7], v[96:99], v[124:127], v[4:7]\n"
      "s_waitcnt lgkmcnt(3)\n"
      "v_mfma_f32_16x16x32_bf16 v[8:11], v[96:99], v[128:131], v[8:11]\n"
      "s_waitcnt lgkmcnt(2)\n"
      "v_mfma_f32_16x16x32_bf16 v[12:15], v[96:99], v[132:135], v[12:15]\n"
      "s_waitcnt lgkmcnt(0)\n"
      "v_mfma_f32_16x16x32_bf16 v[28:31], v[100:103], v[132:135], v[28:31]\n"
      "v_mfma_f32_16x16x32_bf16 v[24:27], v[100:103], v[128:131], v[24:27]\n"
      "v_mfma_f32_16x16x32_bf16 v[20:23], v[100:103], v[124:127], v[20:23]\n"
      "v_mfma_f32_16x16x32_bf16 v[16:19], v[100:103], v[120:123], v[16:19]\n"
      "s_barrier\n"
      "ds_read_b64_tr_b16 v[136:137], v183 offset:6144\n"
      "ds_read_b64_tr_b16 v[138:139], v183 offset:6912\n"
      "ds_read_b128 v[160:163], v179 offset:0\n"
      "ds_read_b128 v[164:167], v179 offset:2048\n"
      "ds_read_b128 v[168:171], v179 offset:4096\n"
      "ds_read_b128 v[172:175], v179 offset:6144\n"
      "ds_read_b64_tr_b16 v[140:141], v183 offset:18432\n"
      "ds_read_b64_tr_b16 v[142:143], v183 offset:19200\n"
      "s_waitcnt lgkmcnt(5)\n"
      "v_mfma_f32_16x16x32_bf16 v[0:3], v[136:139], v[160:163], v[0:3]\n"
      "s_waitcnt lgkmcnt(4)\n"
      "v_mfma_f32_16x16x32_bf16 v[4:7], v[136:139], v[164:167], v[4:7]\n"
      "s_waitcnt lgkmcnt(3)\n"
      "v_mfma_f32_16x16x32_bf16 v[8:11], v[136:139], v[168:171], v[8:11]\n"
      "s_waitcnt lgkmcnt(2)\n"
      "v_mfma_f32_16x16x32_bf16 v[12:15], v[136:139], v[172:175], v[12:15]\n"
      "s_waitcnt lgkmcnt(0)\n"
      "v_mfma_f32_16x16x32_bf16 v[28:31], v[140:143], v[172:175], v[28:31]\n"
      "v_mfma_f32_16x16x32_bf16 v[24:27], v[140:143], v[168:171], v[24:27]\n"
      "v_mfma_f32_16x16x32_bf16 v[20:23], v[140:143], v[164:167], v[20:23]\n"
      "v_mfma_f32_16x16x32_bf16 v[16:19], v[140:143], v[160:163], v[16:19]\n"
      "s_barrier\n"
      "s_branch Lm43_%=\n"
      "Lc46_%=:\n"
      "ds_read_b32 v253, v251 offset:128\n"
      "s_branch Le45_%=\n"
      "Lg44_%=:\n"
      "s_cmp_lg_u32 s83, 0\n"
      "s_cbranch_scc1 Lc46_%=\n"
      "s_nop 7\n"
      "s_lshl1_add_u32 s56, s49, s84\n"
      "s_lshl_b32 s56, s56, 8\n"
      "s_add_u32 s56, s56, 36864\n"
      "v_lshlrev_b32 v216, 2, v250\n"
      "s_mov_b64 exec, 0xffff\n"
      "v_add_f32 v0, v0, v17\n"
      "v_cvt_pk_bf16_f32 v0, v0, v0\n"
      "v_lshlrev_b32 v0, 16, v0\n"
      "v_mul_f32 v0, 0xbfb8aa3b, v0\n"
      "v_exp_f32 v0, v0\n"
      "s_nop 0\n"
      "v_add_f32 v0, 1.0, v0\n"
      "v_rcp_f32 v0, v0\n"
      "s_nop 0\n"
      "v_cvt_pk_bf16_f32 v0, v0, v0\n"
      "v_lshlrev_b32 v0, 16, v0\n"
      "buffer_store_dword v0, v216, s[24:27], s56 offen offset:0 sc1\n"
      "v_add_f32 v4, v4, v21\n"
      "v_cvt_pk_bf16_f32 v4, v4, v4\n"
      "v_lshlrev_b32 v4, 16, v4\n"
      "v_mul_f32 v4, 0xbfb8aa3b, v4\n"
      "v_exp_f32 v4, v4\n"
      "s_nop 0\n"
      "v_add_f32 v4, 1.0, v4\n"
      "v_rcp_f32 v4, v4\n"
      "s_nop 0\n"
      "v_cvt_pk_bf16_f32 v4, v4, v4\n"
      "v_lshlrev_b32 v4, 16, v4\n"
      "buffer_store_dword v4, v216, s[24:27], s56 offen offset:64 sc1\n"
      "v_add_f32 v8, v8, v25\n"
      "v_cvt_pk_bf16_f32 v8, v8, v8\n"
      "v_lshlrev_b32 v8, 16, v8\n"
      "v_mul_f32 v8, 0xbfb8aa3b, v8\n"
      "v_exp_f32 v8, v8\n"
      "s_nop 0\n"
      "v_add_f32 v8, 1.0, v8\n"
      "v_rcp_f32 v8, v8\n"
      "s_nop 0\n"
      "v_cvt_pk_bf16_f32 v8, v8, v8\n"
      "v_lshlrev_b32 v8, 16, v8\n"
      "buffer_store_dword v8, v216, s[24:27], s56 offen offset:128 sc1\n"
      "v_add_f32 v12, v12, v29\n"
      "v_cvt_pk_bf16_f32 v12, v12, v12\n"
      "v_lshlrev_b32 v12, 16, v12\n"
      "v_mul_f32 v12, 0xbfb8aa3b, v12\n"
      "v_exp_f32 v12, v12\n"
      "s_nop 0\n"
      "v_add_f32 v12, 1.0, v12\n"
      "v_rcp_f32 v12, v12\n"
      "s_nop 0\n"
      "v_cvt_pk_bf16_f32 v12, v12, v12\n"
      "v_lshlrev_b32 v12, 16, v12\n"
      "buffer_store_dword v12, v216, s[24:27], s56 offen offset:192 sc1\n"
      "s_mov_b64 exec, -1\n"
      "s_branch Lc46_%=\n"
      "Lw47_%=:\n"
      "s_mov_b32 s68, 524288\n"
      "Ls49_%=:\n"
      "s_sleep 1\n"
      "ds_read_b32 v253, v251 offset:128\n"
      "s_sub_u32 s68, s68, 1\n"
      "s_cbranch_scc1 Lr48_%=\n"
      "s_waitcnt lgkmcnt(0)\n"
      "v_readfirstlane_b32 s67, v253\n"
      "s_cmp_lg_u32 s67, 0\n"
      "s_cbranch_scc1 Ls49_%=\n"
      "s_branch Lr48_%=\n"
      "Lw54_%=:\n"
      "s_mov_b32 s68, 524288\n"
      "Ls56_%=:\n"
      "s_sleep 1\n"
      "ds_read_b32 v253, v251 offset:132\n"
      "s_sub_u32 s68, s68, 1\n"
      "s_cbranch_scc1 Lr55_%=\n"
      "s_waitcnt lgkmcnt(0)\n"
      "v_readfirstlane_b32 s67, v253\n"
      "s_cmp_lg_u32 s67, 0\n"
      "s_cbranch_scc1 Ls56_%=\n"
      "s_branch Lr55_%=\n"
      "Lw59_%=:\n"
      "s_mov_b32 s68, 524288\n"
      "Ls61_%=:\n"
      "s_sleep 1\n"
      "ds_read_b32 v253, v251 offset:144\n"
      "s_sub_u32 s68, s68, 1\n"
      "s_cbranch_scc1 Lr60_%=\n"
      "s_waitcnt lgkmcnt(0)\n"
      "v_readfirstlane_b32 s67, v253\n"
      "s_cmp_lg_u32 s67, 0\n"
      "s_cbranch_scc1 Ls61_%=\n"
      "s_branch Lr60_%=\n"
      "Lw62_%=:\n"
      "s_mov_b32 s68, 524288\n"
      "Ls64_%=:\n"
      "s_sleep 1\n"
      "ds_read_b32 v253, v251 offset:148\n"
      "s_sub_u32 s68, s68, 1\n"
      "s_cbranch_scc1 Lr63_%=\n"
      "s_waitcnt lgkmcnt(0)\n"
      "v_readfirstlane_b32 s67, v253\n"
      "s_cmp_lg_u32 s67, 0\n"
      "s_cbranch_scc1 Ls64_%=\n"
      "s_branch Lr63_%=\n"
      "Lw65_%=:\n"
      "s_mov_b32 s68, 524288\n"
      "Ls67_%=:\n"
      "s_sleep 1\n"
      "ds_read_b32 v253, v251 offset:152\n"
      "s_sub_u32 s68, s68, 1\n"
      "s_cbranch_scc1 Lr66_%=\n"
      "s_waitcnt lgkmcnt(0)\n"
      "v_readfirstlane_b32 s67, v253\n"
      "s_cmp_lg_u32 s67, 0\n"
      "s_cbranch_scc1 Ls67_%=\n"
      "s_branch Lr66_%=\n"
      "Lw68_%=:\n"
      "s_mov_b32 s68, 524288\n"
      "Ls70_%=:\n"
      "s_sleep 1\n"
      "ds_read_b32 v253, v251 offset:156\n"
      "s_sub_u32 s68, s68, 1\n"
      "s_cbranch_scc1 Lr69_%=\n"
      "s_waitcnt lgkmcnt(0)\n"
      "v_readfirstlane_b32 s67, v253\n"
      "s_cmp_lg_u32 s67, 0\n"
      "s_cbranch_scc1 Ls70_%=\n"
      "s_branch Lr69_%=\n"
      "Lw57_%=:\n"
      "ds_read_b32 v253, v251 offset:144\n"
      "s_waitcnt lgkmcnt(0)\n"
      "v_readfirstlane_b32 s67, v253\n"
      "s_cmp_lg_u32 s67, 0\n"
      "s_cbranch_scc1 Lw59_%=\n"
      "Lr60_%=:\n"
      "ds_read_b32 v253, v251 offset:148\n"
      "s_waitcnt lgkmcnt(0)\n"
      "v_readfirstlane_b32 s67, v253\n"
      "s_cmp_lg_u32 s67, 0\n"
      "s_cbranch_scc1 Lw62_%=\n"
      "Lr63_%=:\n"
      "ds_read_b32 v253, v251 offset:152\n"
      "s_waitcnt lgkmcnt(0)\n"
      "v_readfirstlane_b32 s67, v253\n"
      "s_cmp_lg_u32 s67, 0\n"
      "s_cbranch_scc1 Lw65_%=\n"
      "Lr66_%=:\n"
      "ds_read_b32 v253, v251 offset:156\n"
      "s_waitcnt lgkmcnt(0)\n"
      "v_readfirstlane_b32 s67, v253\n"
      "s_cmp_lg_u32 s67, 0\n"
      "s_cbranch_scc1 Lw68_%=\n"
      "Lr69_%=:\n"
      "s_branch Lr58_%=\n"
      "Lw71_%=:\n"
      "s_mov_b32 s68, 524288\n"
      "Ls73_%=:\n"
      "s_sleep 1\n"
      "ds_read_b32 v253, v251 offset:36\n"
      "s_sub_u32 s68, s68, 1\n"
      "s_cbranch_scc1 Lr72_%=\n"
      "s_waitcnt lgkmcnt(0)\n"
      "v_readfirstlane_b32 s67, v253\n"
      "s_cmp_lt_u32 s67, 4\n"
      "s_cbranch_scc1 Ls73_%=\n"
      "s_branch Lr72_%=\n"
      : "+{s[0:1]}"(kp), "+{s2}"(bid), "+{v0}"(tid)
      : "{s3}"((unsigned)(size_t)smem)
      : "v255", "s95", "vcc", "scc", "memory");
#endif
}

extern "C" void kernel_launch(void* const* d_in, const int* in_sizes, int n_in, void* d_out, int out_size, void* d_ws, size_t ws_size,
                              hipStream_t stream) {
  const unsigned fmt = 0x20000, nw = 512 * 1376 * 2;
  gated_mix_fused<<<256, 512, 0, stream>>>(d_in[0], 2048 * 512 * 2, fmt, d_in[8], nw, fmt, d_in[9], nw, fmt, d_in[10], nw, fmt, d_ws, WS_END, fmt,
                                           d_in[1], d_in[2], d_in[3], d_in[4], d_in[5], d_in[11], d_in[6], d_out);
}
```
